```python
import torch

def _softmax(x, axis=-1):
    return torch.softmax(x, dim=axis)
def _gelu(x):
    return torch.nn.functional.gelu(x, approximate="tanh")
def _silu(x):
    return torch.nn.functional.silu(x)
def _sigmoid(x):
    return torch.sigmoid(x)
def _layernorm(x, axis=-1, eps=1e-5):
    m = x.mean(dim=axis, keepdim=True)
    v = x.var(dim=axis, keepdim=True, unbiased=False)
    return (x - m) / torch.sqrt(v + eps)
def _scatter_add(tgt, idx, src, axis=0):
    return tgt.clone().index_add_(axis, idx, src)
def _segment_sum(src, seg, n):
    out = torch.zeros((n,) + src.shape[1:], dtype=src.dtype)
    return out.index_add_(0, seg, src)
def _pad(x, pw):
    flat = tuple(v for pair in reversed(pw) for v in pair)
    return torch.nn.functional.pad(x, flat)

kernel_name = 'interleave_layers__linear_kernel_hedgehog__dense_activation_silu__prompt'

def setup_inputs():

    _w = (lambda a: (lambda t: t.to(torch.bfloat16) if t.is_floating_point() else t)(torch.as_tensor(a)))
    def _A_setup():
        B, S, D, H = 1, 2048, 512, 32
        Dh = D // H
        return {
            "x": _w((lambda *s: torch.randn(*s, dtype=torch.bfloat16) * 0.05)(B, S, D)),
            "wqkv": _w((lambda *s: torch.randn(*s, dtype=torch.bfloat16) * 0.4)(D, 3 * D)),
            "w_phi": _w((lambda *s: torch.randn(*s, dtype=torch.bfloat16) * 0.4)(H, Dh, Dh) * (Dh ** -0.5)),
            "wo": _w((lambda *s: torch.randn(*s, dtype=torch.bfloat16) * 0.05)(D, D)),
        }
    _A_in = _A_setup()

    def _B_setup():
        B, S, D, F = 1, 2048, 512, 1792
        return {
            "x": _w((lambda *s: torch.randn(*s, dtype=torch.bfloat16) * 0.05)(B, S, D)),
            "w1": _w((lambda *s: torch.randn(*s, dtype=torch.bfloat16) * 1.6)(D, F) * (D ** -0.5)),
            "w2": _w((lambda *s: torch.randn(*s, dtype=torch.bfloat16) * 1.6)(F, D) * (F ** -0.5)),
        }
    _B_in = _B_setup()

    return {
        "A_x": _A_in["x"],
        "A_wqkv": _A_in["wqkv"],
        "A_w_phi": _A_in["w_phi"],
        "A_wo": _A_in["wo"],
        "B_x": _B_in["x"],
        "B_w1": _B_in["w1"],
        "B_w2": _B_in["w2"],
    }

def reference(A_x, A_wqkv, A_w_phi, A_wo, B_x, B_w1, B_w2):
    def _A_ref(x, wqkv, w_phi, wo):
        B, S, D = x.shape
        H = 32; Dh = D // H
        qkv = (lambda x, *d: x.permute(*d))((x @ wqkv).reshape(B, S, 3, H, Dh), 2, 0, 3, 1, 4)
        q, k, v = qkv[0], qkv[1], qkv[2]
        pq = torch.einsum("bhsd,hdf->bhsf", q, w_phi)
        pk = torch.einsum("bhsd,hdf->bhsf", k, w_phi)
        phi_q = torch.concatenate([_softmax(pq, axis=-1), _softmax(-pq, axis=-1)], axis=-1)
        phi_k = torch.concatenate([_softmax(pk, axis=-1), _softmax(-pk, axis=-1)], axis=-1)
        kv = torch.einsum("bhsf,bhsd->bhfd", phi_k, v)
        z  = torch.einsum("bhsf->bhf", phi_k)
        num = torch.einsum("bhqf,bhfd->bhqd", phi_q, kv)
        den = torch.einsum("bhqf,bhf->bhq", phi_q, z)[..., None] + 1e-6
        o = num / den
        return (lambda x, *d: x.permute(*d))(o, 0, 2, 1, 3).reshape(B, S, D) @ wo

    def _B_ref(x, w1, w2):
        h = torch.einsum("bsd,df->bsf", x, w1)
        h = _silu(h)
        out = torch.einsum("bsf,fd->bsd", h, w2)
        return out

    x = A_x
    x = x + _A_ref(_layernorm(x, axis=-1), A_wqkv, A_w_phi, A_wo)
    x = x + _B_ref(_layernorm(x, axis=-1), B_w1, B_w2)
    x = x + _A_ref(_layernorm(x, axis=-1), A_wqkv, A_w_phi, A_wo)
    x = x + _B_ref(_layernorm(x, axis=-1), B_w1, B_w2)
    return x
```

```cpp
#include <hip/hip_runtime.h>
#include <hip/hip_bf16.h>
typedef __hip_bfloat16 bf16;
#define S_ 2048
#define D_ 512
#define H_ 32
#define F_ 1792
__device__ __forceinline__ float b2f(bf16 x){return __bfloat162float(x);}
__device__ __forceinline__ bf16 f2b(float x){return __float2bfloat16(x);}
__device__ __forceinline__ float rb(float x){return b2f(f2b(x));}

__global__ void ln_k(const bf16* x, bf16* y){
  int r=blockIdx.x, t=threadIdx.x;
  float v[8]; float s=0;
  for(int i=0;i<8;i++){v[i]=b2f(x[r*D_+t*8+i]); s+=v[i];}
  for(int o=32;o;o>>=1) s+=__shfl_xor(s,o);
  float m=s/D_; float q=0;
  for(int i=0;i<8;i++){float d=v[i]-m; q+=d*d;}
  for(int o=32;o;o>>=1) q+=__shfl_xor(q,o);
  float rs=1.0f/sqrtf(q/D_+1e-5f);
  for(int i=0;i<8;i++) y[r*D_+t*8+i]=f2b((v[i]-m)*rs);
}
template<int MODE>
__global__ void gemm_k(const bf16* A,const bf16* B,bf16* C,const bf16* R,int M,int N,int K){
  __shared__ float As[16][17], Bs[16][17];
  int tx=threadIdx.x&15, ty=threadIdx.x>>4;
  int r=blockIdx.y*16+ty, c=blockIdx.x*16+tx;
  float acc=0;
  for(int k0=0;k0<K;k0+=16){
    As[ty][tx]=b2f(A[r*K+k0+tx]);
    Bs[ty][tx]=b2f(B[(k0+ty)*N+c]);
    __syncthreads();
    for(int k=0;k<16;k++) acc+=As[ty][k]*Bs[k][tx];
    __syncthreads();
  }
  if(MODE==1){ float h=rb(acc); acc=h/(1.0f+__expf(-h)); }
  if(MODE==2){ acc=rb(acc)+b2f(R[r*N+c]); }
  C[r*N+c]=f2b(acc);
}
__global__ void phi_k(const bf16* qkv,const bf16* wphi,bf16* phiq,bf16* phik){
  int idx=blockIdx.x*blockDim.x+threadIdx.x;
  int which=idx&1, h=(idx>>1)&31, s=idx>>6;
  const bf16* src=qkv+s*1536+which*512+h*16;
  float q[16]; for(int i=0;i<16;i++) q[i]=b2f(src[i]);
  float p[16]; float mx=-1e30f, mn=1e30f;
  for(int f=0;f<16;f++){float a=0; for(int d=0;d<16;d++) a+=q[d]*b2f(wphi[h*256+d*16+f]); p[f]=rb(a); mx=fmaxf(mx,p[f]); mn=fminf(mn,p[f]);}
  float e1[16],e2[16],s1=0,s2=0;
  for(int f=0;f<16;f++){e1[f]=__expf(p[f]-mx); s1+=e1[f]; e2[f]=__expf(mn-p[f]); s2+=e2[f];}
  bf16* dst=(which?phik:phiq)+(s*32+h)*32;
  for(int f=0;f<16;f++){dst[f]=f2b(e1[f]/s1); dst[16+f]=f2b(e2[f]/s2);}
}
__global__ void kv_k(const bf16* phik,const bf16* qkv,float* kv,float* z){
  int h=blockIdx.x, t=threadIdx.x;
  if(t<512){int f=t>>4,d=t&15; float a=0;
    for(int s=0;s<S_;s++) a+=b2f(phik[(s*32+h)*32+f])*b2f(qkv[s*1536+1024+h*16+d]);
    kv[h*512+t]=rb(a);
  } else if(t<544){int f=t-512; float a=0; for(int s=0;s<S_;s++) a+=b2f(phik[(s*32+h)*32+f]); z[h*32+f]=rb(a);}
}
__global__ void o_k(const bf16* phiq,const float* kv,const float* z,bf16* o){
  int idx=blockIdx.x*blockDim.x+threadIdx.x;
  int d=idx&15,h=(idx>>4)&31,s=idx>>9;
  float n=0,de=0;
  for(int f=0;f<32;f++){float p=b2f(phiq[(s*32+h)*32+f]); n+=p*kv[h*512+f*16+d]; de+=p*z[h*32+f];}
  n=rb(n); de=rb(rb(de)+1e-6f);
  o[s*512+h*16+d]=f2b(n/de);
}
extern "C" void kernel_launch(void* const* d_in, const int* in_sizes, int n_in,
                              void* d_out, int out_size, void* d_ws, size_t ws_size,
                              hipStream_t stream) {
  const bf16* x=(const bf16*)d_in[0];
  const bf16* wqkv=(const bf16*)d_in[1];
  const bf16* wphi=(const bf16*)d_in[2];
  const bf16* wo=(const bf16*)d_in[3];
  const bf16* w1=(const bf16*)d_in[5];
  const bf16* w2=(const bf16*)d_in[6];
  char* ws=(char*)d_ws;
  bf16* xn=(bf16*)(ws);
  bf16* qkv=(bf16*)(ws+(4<<20));
  bf16* phiq=(bf16*)(ws+(12<<20));
  bf16* phik=(bf16*)(ws+(20<<20));
  float* kv=(float*)(ws+(28<<20));
  float* z=(float*)(ws+(29<<20));
  bf16* o=(bf16*)(ws+(30<<20));
  bf16* x1=(bf16*)(ws+(34<<20));
  bf16* h=(bf16*)(ws+(38<<20));
  bf16* x2=(bf16*)(ws+(50<<20));
  const bf16* xin=x;
  for(int l=0;l<2;l++){
    bf16* xout = l==0? x2 : (bf16*)d_out;
    ln_k<<<S_,64,0,stream>>>(xin,xn);
    gemm_k<0><<<dim3(1536/16,S_/16),256,0,stream>>>(xn,wqkv,qkv,nullptr,S_,1536,512);
    phi_k<<<S_*64/256,256,0,stream>>>(qkv,wphi,phiq,phik);
    kv_k<<<32,576,0,stream>>>(phik,qkv,kv,z);
    o_k<<<S_*512/256,256,0,stream>>>(phiq,kv,z,o);
    gemm_k<2><<<dim3(512/16,S_/16),256,0,stream>>>(o,wo,x1,xin,S_,512,512);
    ln_k<<<S_,64,0,stream>>>(x1,xn);
    gemm_k<1><<<dim3(F_/16,S_/16),256,0,stream>>>(xn,w1,h,nullptr,S_,F_,512);
    gemm_k<2><<<dim3(512/16,S_/16),256,0,stream>>>(h,w2,xout,x1,S_,512,F_);
    xin=xout;
  }
}
```

```cpp
#include <hip/hip_runtime.h>
#include <hip/hip_bf16.h>

typedef unsigned u32;
typedef unsigned short u16;
typedef u32 u32x4 __attribute__((ext_vector_type(4)));
typedef u32 u32x2 __attribute__((ext_vector_type(2)));
typedef float f32x4 __attribute__((ext_vector_type(4)));
typedef float f32x2 __attribute__((ext_vector_type(2)));
typedef short s16x4 __attribute__((ext_vector_type(4)));
typedef short s16x8 __attribute__((ext_vector_type(8)));
typedef __bf16 bf16x2 __attribute__((ext_vector_type(2)));
typedef __bf16 bf16x8 __attribute__((ext_vector_type(8)));
#define LAS __attribute__((address_space(3)))
#define GAS __attribute__((address_space(1)))
#define DI __device__ __forceinline__
#define RLX_AGENT __ATOMIC_RELAXED, __HIP_MEMORY_SCOPE_AGENT

constexpr size_t WS_CTL = 0;
constexpr size_t WS_KVP = 1u << 20;
constexpr u32 KVP_UNIT = 4608;
constexpr size_t WS_O = 4u << 20;
constexpr size_t WS_X1 = 8u << 20;
constexpr size_t WS_X2 = 12u << 20;
constexpr size_t WS_ST1 = 14u << 20;
constexpr size_t WS_ST2 = 15u << 20;
constexpr size_t WS_H = 16u << 20;
constexpr size_t WS_STAMP = 40u << 20;
constexpr u32 L_IN1P = 0, L_IN1K = 128, L_REQP = 256, L_REQK = 384, L_IN2P = 512, L_IN2K = 640;

constexpr u32 LD_WA1 = 0;
constexpr u32 LD_STG = 96 * 1024;
constexpr u32 LD_WB1 = 0;
constexpr u32 LD_WA3 = 64 * 1024;
constexpr u32 LD_RING = 0;
constexpr u32 LD_SC = 128 * 1024;
constexpr u32 LD_STAMP = 154 * 1024;
constexpr u32 LD_S = 159 * 1024;
constexpr u32 SPIN_CAP = 1u << 17;

#define BAR() do { asm volatile("s_waitcnt lgkmcnt(0)" ::: "memory"); __builtin_amdgcn_s_barrier(); asm volatile("" ::: "memory"); } while (0)
#define FRESH() do { asm volatile("" : "+s"(ws), "+s"(P), "+s"(j), "+s"(w), "+s"(x), "+s"(wqkv), "+s"(wo), "+s"(w1), "+s"(w2)); th = j >> 3; cg = j & 7; c4 = w & 3; kh = w >> 2; ctl = ws + WS_CTL; } while (0)
#define DRAIN() asm volatile("s_waitcnt vmcnt(0)" ::: "memory")

#define STAMP(i) do {} while (0)

template <int N> DI void waitv(u32x4& a) { asm volatile("s_waitcnt vmcnt(%1)" : "+v"(a) : "i"(N)); }
template <int N> DI void waitv2(u32x4& a, u32x4& b) { asm volatile("s_waitcnt vmcnt(%2)" : "+v"(a), "+v"(b) : "i"(N)); }
template <int N> DI void waitv4(u32x4& a, u32x4& b, u32x4& c, u32x4& d) { asm volatile("s_waitcnt vmcnt(%4)" : "+v"(a), "+v"(b), "+v"(c), "+v"(d) : "i"(N)); }
DI void pin(u32x4& a) { asm volatile("" : "+v"(a)); }
DI void pin2(u32x2& a) { asm volatile("" : "+v"(a)); }
DI void pin1(u32& a) { asm volatile("" : "+v"(a)); }

template <int OFF> DI void gld16_sc1(u32x4& d, const char* sb, u32 vo) { asm volatile("global_load_dwordx4 %0, %1, %2 offset:%3 sc1" : "=v"(d) : "v"(vo), "s"(sb), "i"(OFF) : "memory"); }
template <int OFF> DI void gld16(u32x4& d, const char* sb, u32 vo) { asm volatile("global_load_dwordx4 %0, %1, %2 offset:%3" : "=v"(d) : "v"(vo), "s"(sb), "i"(OFF) : "memory"); }
template <int OFF> DI void gld8_sc1(u32x2& d, const char* sb, u32 vo) { asm volatile("global_load_dwordx2 %0, %1, %2 offset:%3 sc1" : "=v"(d) : "v"(vo), "s"(sb), "i"(OFF) : "memory"); }
template <int OFF> DI void gld8(u32x2& d, const char* sb, u32 vo) { asm volatile("global_load_dwordx2 %0, %1, %2 offset:%3" : "=v"(d) : "v"(vo), "s"(sb), "i"(OFF) : "memory"); }
DI void gld4_sc1(u32& d, const char* sb, u32 vo) { asm volatile("global_load_dword %0, %1, %2 sc1" : "=v"(d) : "v"(vo), "s"(sb) : "memory"); }
template <int OFF> DI void gst16_sc1(char* sb, u32 vo, u32x4 d) { asm volatile("global_store_dwordx4 %0, %1, %2 offset:%3 sc1\n\ts_nop 1" ::"v"(vo), "v"(d), "s"(sb), "i"(OFF) : "memory"); }
DI void gst8_sc1(char* sb, u32 vo, u32x2 d) { asm volatile("global_store_dwordx2 %0, %1, %2 sc1" ::"v"(vo), "v"(d), "s"(sb) : "memory"); }
template <int OFF> DI void gst8(char* sb, u32 vo, u32x2 d) { asm volatile("global_store_dwordx2 %0, %1, %2 offset:%3" ::"v"(vo), "v"(d), "s"(sb), "i"(OFF) : "memory"); }
DI void gst4_sc1(char* sb, u32 vo, u32 d) { asm volatile("global_store_dword %0, %1, %2 sc1" ::"v"(vo), "v"(d), "s"(sb) : "memory"); }
DI void glds(u32 ldsaddr, const char* sb, u32 vo) { asm volatile("s_mov_b32 m0, %0\n\ts_nop 0\n\tglobal_load_lds_dwordx4 %1, %2" ::"s"(ldsaddr), "v"(vo), "s"(sb) : "memory"); }

DI bf16x8 trfrag(LAS char* p, int second) {
  s16x4 a = __builtin_amdgcn_ds_read_tr16_b64_v4i16((LAS s16x4*)p);
  s16x4 b = __builtin_amdgcn_ds_read_tr16_b64_v4i16((LAS s16x4*)(p + second));
  return __builtin_bit_cast(bf16x8, __builtin_shufflevector(a, b, 0, 1, 2, 3, 4, 5, 6, 7));
}
DI f32x4 mfma32(bf16x8 a, bf16x8 b, f32x4 c) { return __builtin_amdgcn_mfma_f32_16x16x32_bf16(a, b, c, 0, 0, 0); }
DI f32x4 mfma32u(bf16x8 a, u32x4 b, f32x4 c) { return __builtin_amdgcn_mfma_f32_16x16x32_bf16(a, __builtin_bit_cast(bf16x8, b), c, 0, 0, 0); }
DI f32x4 mfma16(u32x2 a, u32x2 b, f32x4 c) { return __builtin_amdgcn_mfma_f32_16x16x16bf16_1k(__builtin_bit_cast(s16x4, a), __builtin_bit_cast(s16x4, b), c, 0, 0, 0); }
DI u32 pk2(float a, float b) { f32x2 v = {a, b}; return __builtin_bit_cast(u32, __builtin_convertvector(v, bf16x2)); }
DI u32x2 pk4(f32x4 v) { u32x2 r; r.x = pk2(v[0], v[1]); r.y = pk2(v[2], v[3]); return r; }
DI float bflo(u32 u) { return __uint_as_float(u << 16); }
DI float bfhi(u32 u) { return __uint_as_float(u & 0xffff0000u); }
DI float dot2(u32 a, u32 b, float c) { return __builtin_amdgcn_fdot2_f32_bf16(__builtin_bit_cast(bf16x2, a), __builtin_bit_cast(bf16x2, b), c, false); }
DI void swap32(float& a, float& b) { auto r = __builtin_amdgcn_permlane32_swap(__float_as_uint(a), __float_as_uint(b), false, false); a = __uint_as_float(r[0]); b = __uint_as_float(r[1]); }
DI void swap16(float& a, float& b) { auto r = __builtin_amdgcn_permlane16_swap(__float_as_uint(a), __float_as_uint(b), false, false); a = __uint_as_float(r[0]); b = __uint_as_float(r[1]); }
template <bool MAX> DI void red4(float& a, float& b, float& c, float& d) {
  swap32(a, b); float u0 = MAX ? fmaxf(a, b) : a + b;
  swap32(c, d); float u1 = MAX ? fmaxf(c, d) : c + d;
  swap16(u0, u1); float t = MAX ? fmaxf(u0, u1) : u0 + u1;
  float t2 = t; swap16(t, t2);
  a = t; b = t; swap32(a, b);
  c = t2; d = t2; swap32(c, d);
}
DI void red2sum(float& a, float& b) {
  swap32(a, b); float u = a + b;
  float u2 = u; swap16(u, u2); u = u + u2;
  a = u; b = u; swap32(a, b);
}
DI void softmax2(f32x4 p0, f32x4 p1, u32x2& pos0, u32x2& neg0, u32x2& pos1, u32x2& neg1) {
  const float L2E = 1.4426950408889634f;
  float mx0 = fmaxf(fmaxf(p0[0], p0[1]), fmaxf(p0[2], p0[3])), mn0 = -fminf(fminf(p0[0], p0[1]), fminf(p0[2], p0[3]));
  float mx1 = fmaxf(fmaxf(p1[0], p1[1]), fmaxf(p1[2], p1[3])), mn1 = -fminf(fminf(p1[0], p1[1]), fminf(p1[2], p1[3]));
  red4<true>(mx0, mn0, mx1, mn1);
  f32x4 a0, b0, a1, b1;
#pragma unroll
  for (int r = 0; r < 4; ++r) {
    a0[r] = __builtin_amdgcn_exp2f((p0[r] - mx0) * L2E);
    b0[r] = __builtin_amdgcn_exp2f((-p0[r] - mn0) * L2E);
    a1[r] = __builtin_amdgcn_exp2f((p1[r] - mx1) * L2E);
    b1[r] = __builtin_amdgcn_exp2f((-p1[r] - mn1) * L2E);
  }
  float sa0 = (a0[0] + a0[1]) + (a0[2] + a0[3]), sb0 = (b0[0] + b0[1]) + (b0[2] + b0[3]);
  float sa1 = (a1[0] + a1[1]) + (a1[2] + a1[3]), sb1 = (b1[0] + b1[1]) + (b1[2] + b1[3]);
  red4<false>(sa0, sb0, sa1, sb1);
  pos0 = pk4(a0 * __builtin_amdgcn_rcpf(sa0)); neg0 = pk4(b0 * __builtin_amdgcn_rcpf(sb0));
  pos1 = pk4(a1 * __builtin_amdgcn_rcpf(sa1)); neg1 = pk4(b1 * __builtin_amdgcn_rcpf(sb1));
}

template <int V> struct IC { static constexpr int v = V; };
template <int B, int E, class F> DI void sfor(F&& f) { if constexpr (B < E) { f(IC<B>{}); sfor<B + 1, E>(f); } }
#define SB() __builtin_amdgcn_sched_barrier(0)
template <int NT, int NKB, int D, class LD, class HOOK> DI void gemm(f32x4* acc, u32x4* xf, LD&& ld, HOOK&& hook) {
  bf16x8 wf[D + 1];
  sfor<0, D>([&](auto I) { constexpr int s = I.v; wf[s] = ld(IC<s / NT>{}, IC<s % NT>{}); });
  SB();
  sfor<0, NT * NKB>([&](auto I) {
    constexpr int s = I.v;
    hook(I);
    if constexpr (s + D < NT * NKB) wf[(s + D) % (D + 1)] = ld(IC<(s + D) / NT>{}, IC<(s + D) % NT>{});
    acc[s % NT] = mfma32u(wf[s % (D + 1)], xf[s / NT], acc[s % NT]);
    SB();
  });
}
DI LAS char* opaque(LAS char* p) { asm volatile("" : "+v"(p)); return p; }
template <int CTRL> DI float dppadd(float v) { return v + __uint_as_float(__builtin_amdgcn_update_dpp(0u, __float_as_uint(v), CTRL, 0xf, 0xf, true)); }
DI float rowsum16(float v) { v = dppadd<0xB1>(v); v = dppadd<0x4E>(v); v = dppadd<0x141>(v); v = dppadd<0x140>(v); return v; }

DI u32 first_signal(char* ctl, u32 own_req, u32 peer_in, int lane) {
  u32 sent; gld4_sc1(sent, ctl, own_req); asm volatile("s_waitcnt vmcnt(0)" : "+v"(sent)::"memory");
  if (lane < 16) gst4_sc1(ctl, peer_in, sent + 1u);
  return sent;
}
DI void first_wait(char* ctl, u32 own_in, u32 own_req, u32 peer_in, u32 a0, u32& sent, int lane) {
  for (u32 sp = 0; sp < SPIN_CAP; ++sp) {
    u32 v, r; gld4_sc1(v, ctl, own_in); gld4_sc1(r, ctl, own_req);
    asm volatile("s_waitcnt vmcnt(0)" : "+v"(v), "+v"(r)::"memory");
    if (r != sent) { sent = r; if (lane < 16) gst4_sc1(ctl, peer_in, r + 1u); }
    if (__all(v != a0)) break;
  }
}
DI void first_final(char* ctl, u32 own_req, u32 peer_in, u32 sent, int lane) {
  u32 r; gld4_sc1(r, ctl, own_req); asm volatile("s_waitcnt vmcnt(0)" : "+v"(r)::"memory");
  if (r != sent && lane < 16) gst4_sc1(ctl, peer_in, r + 1u);
}
DI void next_signal(char* ctl, u32 peer_in, u32 val, int lane) { if (lane < 16) gst4_sc1(ctl, peer_in, val); }
DI void next_wait(char* ctl, u32 own_in, u32 val) {
  for (u32 sp = 0; sp < SPIN_CAP; ++sp) {
    u32 v; gld4_sc1(v, ctl, own_in); asm volatile("s_waitcnt vmcnt(0)" : "+v"(v)::"memory");
    if (__all(v >= val)) break;
  }
}

__global__ __launch_bounds__(512, 2) void mega(const char* x, const char* wqkv, const char* wphi,
                                               const char* wo, const char* w1, const char* w2,
                                               char* out, char* ws) {
  __shared__ __attribute__((aligned(16))) char smem[163840];
  LAS char* lds = (LAS char*)smem;
  const u32 lb = (u32)(size_t)lds;
  const int lane = threadIdx.x & 63; int w = __builtin_amdgcn_readfirstlane(threadIdx.x >> 6);
  const int g = lane >> 4, tk = lane & 15, q = (lane >> 2) & 3, p = lane & 3;
  const int bid = blockIdx.x, loc = bid >> 3;
  int P = (bid & 7) * 2 + (loc >> 4), j = loc & 15;
  int th = j >> 3, cg = j & 7, c4 = w & 3, kh = w >> 2;
  char* ctl = ws + WS_CTL;
  const u32 myblk = (u32)(P * 16 + j) * 1024u + 4u * tk;
  const u32 peerP = (u32)(P * 16 + tk) * 1024u + 4u * j, peerK = (u32)(tk * 16 + j) * 1024u + 4u * P;
  const u32 l16 = lane * 16u;
  const u32 swz128 = 16u * ((lane & 7) ^ (g << 1));
  const u32 vo1024 = (lane >> 3) * 1024u + swz128, vo3584 = (lane >> 3) * 3584u + swz128;
  const u32 voA1 = (lane >> 2) * 3072u + 16u * ((lane & 3) ^ ((g & 1) << 1));
  const int s4 = (2 * g + (q >> 1)) & 3;
  u32 trb[4];
#pragma unroll
  for (int nt = 0; nt < 4; ++nt) trb[nt] = (4 * g + q) * 128 + 32 * (nt ^ s4) + 8 * p;
  u32 tra[2];
#pragma unroll
  for (int hd = 0; hd < 2; ++hd) tra[hd] = (4 * g + q) * 64 + 32 * (hd ^ (g & 1)) + 8 * p;
  const u32x4 ones4 = {0x3f803f80u, 0x3f803f80u, 0x3f803f80u, 0x3f803f80u};
  const bf16x8 ones = __builtin_bit_cast(bf16x8, ones4);
  STAMP(0);

  u32 a0 = 0;
  u32x4 resid[2];
  u32x2 wph[2];
  u32x4 xf[16];
  f32x4 acc[8];
  float mean, rstd;
  const f32x4 z4 = {0.f, 0.f, 0.f, 0.f};
#define LD4(i) gld16_sc1<0>(xf[i], xb + (i) * 1024, l16); gld16_sc1<1024>(xf[i + 1], xb + (i) * 1024, l16); gld16_sc1<2048>(xf[i + 2], xb + (i) * 1024, l16); gld16_sc1<3072>(xf[i + 3], xb + (i) * 1024, l16);
#define GLDS_A1(n) glds(lb + LD_WA1 + (n) * 1024, wqkv + 64 * j + (size_t)(((n) & 31) * 16) * 3072 + ((((n) >> 5) + 1) % 3) * 1024, voA1)
#define GLDS_B1(n) glds(lb + LD_WB1 + (n) * 1024, w1 + (size_t)(((n) & 63) * 8) * 3584 + j * 256 + ((n) >> 6) * 128, vo3584)
#define GLDS_B2(n) glds(lb + LD_RING + ((n) & 127) * 1024, w2 + cg * 128 + (size_t)(896 * (((n) >> 3) & 1) + 64 * ((n) >> 4) + 8 * ((n) & 7)) * 1024, vo1024)
#define STATS(st) { pin2(st[0]); pin2(st[1]); float sum = __uint_as_float(st[0].x) + __uint_as_float(st[1].x), ssq = __uint_as_float(st[0].y) + __uint_as_float(st[1].y); \
      red2sum(sum, ssq); mean = sum * (1.f / 512.f); rstd = __builtin_amdgcn_rsqf(fmaxf(ssq * (1.f / 512.f) - mean * mean, 0.f) + 1e-5f); }

  for (int layer = 0; layer < 2; ++layer) {
    FRESH();
#pragma unroll
    for (int t = 0; t < 6; ++t) acc[t] = z4;
    LAS char* wa1[2] = {opaque(lds + LD_WA1 + tra[0]), opaque(lds + LD_WA1 + tra[1])};
    auto ldkv = [&](auto KB, auto T) { return trfrag(wa1[T.v & 1] + (T.v >> 1) * 32768 + KB.v * 2048, 1024); };
    if (layer == 0) {
      gld4_sc1(a0, ctl, myblk + (w == 4 ? L_IN1K : L_IN1P));
      u32 wp[8];
      {
        const u16* wsrc = (const u16*)wphi + (2 * j) * 256 + (4 * g) * 16 + tk;
#pragma unroll
        for (int i = 0; i < 8; ++i) asm volatile("global_load_ushort %0, %1, off offset:%2" : "=v"(wp[i]) : "v"(wsrc), "i"((i >> 2) * 512 + (i & 3) * 32) : "memory");
      }
#pragma unroll
      for (int i = 0; i < 8; ++i) GLDS_A1(8 * w + i);
      const char* xb = x + (size_t)(128 * P + 16 * w) * 1024;
      const u32 xvo = g * 1024u + tk * 16u;
      u32x4 xr[16];
#pragma unroll
      for (int rho = 0; rho < 4; ++rho) {
        gld16<0>(xr[4 * rho + 0], xb + 256 * rho, xvo);
        gld16<0>(xr[4 * rho + 1], xb + 256 * rho + 4096, xvo);
        gld16<0>(xr[4 * rho + 2], xb + 256 * rho + 8192, xvo);
        gld16<0>(xr[4 * rho + 3], xb + 256 * rho + 12288, xvo);
      }
#pragma unroll
      for (int i = 0; i < 4; ++i) GLDS_A1(64 + 4 * w + i);
      u32x2 rs[4];
      {
        const char* rb = x + (size_t)(128 * P + 64 * th + 16 * c4) * 1024 + 128 * cg;
        gld8<0>(rs[0], rb, tk * 1024u + 8u * g); gld8<32>(rs[1], rb, tk * 1024u + 8u * g);
        gld8<64>(rs[2], rb, tk * 1024u + 8u * g); gld8<96>(rs[3], rb, tk * 1024u + 8u * g);
      }
      STAMP(32);
      asm volatile("s_waitcnt vmcnt(24)" : "+v"(a0)::"memory");
      STAMP(33);
      BAR();
      STAMP(34);
      {
        const u32 dst = w == 0 ? peerP + L_REQP : w == 4 ? peerK + L_REQK : myblk + ((w & 1) ? L_IN2K : L_IN2P);
        if (lane < 16) gst4_sc1(ctl, dst, (w & 3) == 0 ? a0 : 0u);
      }
      if (w < 4) {
        LAS char* sp = lds + LD_WA1 + (w >> 1) * 32768 + ((w & 1) ? tra[1] : tra[0]);
        f32x4 accS = z4;
#pragma unroll
        for (int kb = 0; kb < 16; ++kb) accS = mfma32(trfrag(sp + kb * 2048, 1024), ones, accS);
        if (tk == 0) *(LAS f32x4*)(lds + LD_S + w * 64 + g * 16) = accS;
      }
      LAS char* stw = lds + LD_STG + w * 4352 + g * 272 + 16 * tk;
      LAS char* str = lds + LD_STG + w * 4352 + tk * 272 + 8 * g;
      float sum = 0.f, ssq = 0.f;
      auto stage = [&](auto R) {
        constexpr int rho = R.v;
        waitv4<21 - 4 * rho>(xr[4 * rho], xr[4 * rho + 1], xr[4 * rho + 2], xr[4 * rho + 3]);
#pragma unroll
        for (int i = 0; i < 4; ++i) *(LAS u32x4*)(stw + i * 1088) = xr[4 * rho + i];
#pragma unroll
        for (int i = 0; i < 4; ++i) {
          u32x2 lo = *(LAS u32x2*)(str + 64 * i), hi = *(LAS u32x2*)(str + 64 * i + 32);
          xf[4 * rho + i] = u32x4{lo.x, lo.y, hi.x, hi.y};
        }
      };
      STAMP(35);
      stage(IC<0>{});
      STAMP(36);
      gemm<4, 16, 6>(acc, xf, ldkv, [&](auto I) {
        constexpr int s = I.v;
        if constexpr (s % 16 == 8 && s < 48) stage(IC<s / 16 + 1>{});
        sum = dot2(xf[s / 4][s % 4], 0x3f803f80u, sum); ssq = dot2(xf[s / 4][s % 4], xf[s / 4][s % 4], ssq);
      });
      STAMP(37);
      red2sum(sum, ssq);
      mean = sum * (1.f / 512.f);
      rstd = __builtin_amdgcn_rsqf(fmaxf(ssq * (1.f / 512.f) - mean * mean, 0.f) + 1e-5f);
      DRAIN();
      STAMP(38);
#pragma unroll
      for (int i = 0; i < 8; ++i) pin1(wp[i]);
      wph[0] = u32x2{wp[0] | (wp[1] << 16), wp[2] | (wp[3] << 16)};
      wph[1] = u32x2{wp[4] | (wp[5] << 16), wp[6] | (wp[7] << 16)};
#pragma unroll
      for (int i = 0; i < 4; ++i) pin2(rs[i]);
      resid[0] = u32x4{rs[0].x, rs[0].y, rs[1].x, rs[1].y}; resid[1] = u32x4{rs[2].x, rs[2].y, rs[3].x, rs[3].y};
      BAR();
    } else {
      u32x2 st[2];
      const char* sb = ws + WS_ST2 + (size_t)(8 * P + w) * 1024;
      gld8_sc1<0>(st[0], sb, g * 256u + tk * 8u); gld8_sc1<128>(st[1], sb, g * 256u + tk * 8u);
      const char* xb = ws + WS_X2 + (size_t)(8 * P + w) * 16384;
      LD4(0) LD4(4) LD4(8) LD4(12)
      STAMP(40);
      asm volatile("s_waitcnt vmcnt(18)" ::: "memory");
      BAR();
      STAMP(41);
      gemm<4, 16, 6>(acc, xf, ldkv, [&](auto I) { constexpr int s = I.v; if constexpr (s % 4 == 0) waitv<15 - s / 4>(xf[s / 4]); });
      STATS(st)
    }
    STAMP(1 + 12 * layer);
    FRESH();
    {
      u32x2 y[4];
#pragma unroll
      for (int t = 0; t < 4; ++t) { const f32x4 S = *(LAS f32x4*)(lds + LD_S + t * 64 + g * 16); y[t] = pk4((acc[t] - mean * S) * rstd); }
      f32x4 p0 = mfma16(wph[0], y[0], z4), p1 = mfma16(wph[1], y[1], z4);
      u32x2 kp0, kn0, kp1, kn1;
      softmax2(p0, p1, kp0, kn0, kp1, kn1);
      LAS char* kt = lds + LD_SC + (16 * w + tk) * 200 + 8 * g;
      *(LAS u32x2*)(kt) = kp0; *(LAS u32x2*)(kt + 32) = kn0; *(LAS u32x2*)(kt + 64) = y[2];
      *(LAS u32x2*)(kt + 96) = kp1; *(LAS u32x2*)(kt + 128) = kn1; *(LAS u32x2*)(kt + 160) = y[3];
    }
    STAMP(42);
    BAR();
    STAMP(43);
    auto qgemm = [&]() {
      LAS char* wq[2] = {opaque(lds + LD_WA1 + 65536 + tra[0]), opaque(lds + LD_WA1 + 65536 + tra[1])};
      gemm<2, 16, 6>(acc + 4, xf, [&](auto KB, auto T) { return trfrag(wq[T.v] + KB.v * 2048, 1024); }, [&](auto) {});
    };
    if (w < 4) {
      LAS char* ka = lds + LD_SC + (4 * g + q) * 200 + 8 * p + (w >> 1) * 96;
      f32x4 akv = z4, az = z4;
#pragma unroll
      for (int ks = 0; ks < 4; ++ks) {
        bf16x8 A = trfrag(ka + ks * 6400 + (w & 1) * 32, 3200), B = trfrag(ka + ks * 6400 + 64, 3200);
        akv = mfma32(A, B, akv); az = mfma32(A, ones, az);
      }
      char* kb_ = ws + WS_KVP + (size_t)((layer * 16 + j) * 16 + P) * KVP_UNIT;
      asm volatile("s_nop 7" : "+v"(akv), "+v"(az));
      gst16_sc1<0>(kb_ + w * 1024, l16, __builtin_bit_cast(u32x4, akv));
      if (tk == 0) gst16_sc1<0>(kb_ + 4096 + w * 64, g * 16u, __builtin_bit_cast(u32x4, az));
    } else if (layer == 0 && w < 6) {
      LAS char* sp = lds + LD_WA1 + 65536 + ((w & 1) ? tra[1] : tra[0]);
      f32x4 accS = z4;
#pragma unroll
      for (int kb = 0; kb < 16; ++kb) accS = mfma32(trfrag(sp + kb * 2048, 1024), ones, accS);
      if (tk == 0) *(LAS f32x4*)(lds + LD_S + w * 64 + g * 16) = accS;
    }
    DRAIN();
    STAMP(44);
    BAR();
    STAMP(2 + 12 * layer);
    u32 sentK = 0;
    if (w == 4) {
      if (layer == 0) sentK = first_signal(ctl, myblk + L_REQK, peerK + L_IN1K, lane);
      else next_signal(ctl, peerK + L_IN2K, 1u, lane);
    }
    if (w > 4) {
      if (w == 5) { _Pragma("unroll 4") for (int n = 32; n < 64; ++n) glds(lb + LD_WA3 + n * 1024, wo + (size_t)n * 8192 + cg * 128, vo1024); }
      else if (j < 14) { _Pragma("unroll 4") for (int i = 0; i < 32; ++i) { const int n = (w - 6) * 32 + i; GLDS_B1(n); } }
    }
    STAMP(45);
    qgemm();
    STAMP(46);
    u32x4 phq[2];
    {
      const f32x4 S0 = *(LAS f32x4*)(lds + LD_S + 256 + g * 16), S1 = *(LAS f32x4*)(lds + LD_S + 320 + g * 16);
      f32x4 p0 = mfma16(wph[0], pk4((acc[4] - mean * S0) * rstd), z4), p1 = mfma16(wph[1], pk4((acc[5] - mean * S1) * rstd), z4);
      u32x2 a, b, c, d; softmax2(p0, p1, a, b, c, d);
      phq[0] = u32x4{a.x, a.y, b.x, b.y}; phq[1] = u32x4{c.x, c.y, d.x, d.y};
    }
    STAMP(47);
    if (w == 4) {
      if (layer == 0) first_wait(ctl, myblk + L_IN1K, myblk + L_REQK, peerK + L_IN1K, a0, sentK, lane);
      else next_wait(ctl, myblk + L_IN2K, 1u);
    }
    BAR();
    STAMP(3 + 12 * layer);
    FRESH();
    {
      if (w < 4) {
        const char* kb_ = ws + WS_KVP + (size_t)((layer * 16 + j) * 16) * KVP_UNIT;
        u32x4 zz;
        gld16_sc1<0>(zz, kb_ + 4096 + w * 64, tk * KVP_UNIT + g * 16u);
#pragma unroll
        for (int i = 0; i < 16; ++i) gld16_sc1<0>(xf[i], kb_ + i * KVP_UNIT + w * 1024, l16);
        waitv<16>(zz);
        f32x4 sz = __builtin_bit_cast(f32x4, zz);
#pragma unroll
        for (int e = 0; e < 4; ++e) sz[e] = rowsum16(sz[e]);
        if (tk == 0) *(LAS u32x2*)(lds + LD_SC + 2048 + w * 32 + g * 8) = pk4(sz);
        waitv4<8>(xf[0], xf[1], xf[2], xf[3]); waitv4<8>(xf[4], xf[5], xf[6], xf[7]);
        f32x4 s0 = __builtin_bit_cast(f32x4, xf[0]);
#pragma unroll
        for (int i = 1; i < 8; ++i) s0 += __builtin_bit_cast(f32x4, xf[i]);
        waitv4<0>(xf[8], xf[9], xf[10], xf[11]); waitv4<0>(xf[12], xf[13], xf[14], xf[15]);
#pragma unroll
        for (int i = 8; i < 16; ++i) s0 += __builtin_bit_cast(f32x4, xf[i]);
        *(LAS u32x2*)(lds + LD_SC + w * 512 + lane * 8) = pk4(s0);
      } else {
#pragma unroll
        for (int i = 0; i < 8; ++i) { const int n = (w - 4) * 8 + i; glds(lb + LD_WA3 + n * 1024, wo + (size_t)n * 8192 + cg * 128, vo1024); }
        if (w == 4 && layer == 0) first_final(ctl, myblk + L_REQK, peerK + L_IN1K, sentK, lane);
      }
      STAMP(48);
      BAR();
      STAMP(49);
      f32x4 o2[2];
#pragma unroll
      for (int hd = 0; hd < 2; ++hd) {
        LAS char* r0 = lds + LD_SC + hd * 1024 + lane * 8;
        LAS char* z0 = lds + LD_SC + 2048 + hd * 64 + g * 8;
        u32x2 kp = *(LAS u32x2*)(r0), kn = *(LAS u32x2*)(r0 + 512), zp = *(LAS u32x2*)(z0), zn = *(LAS u32x2*)(z0 + 32);
        f32x4 num = mfma32u(__builtin_bit_cast(bf16x8, u32x4{kp.x, kp.y, kn.x, kn.y}), phq[hd], z4);
        f32x4 den = mfma32u(__builtin_bit_cast(bf16x8, u32x4{zp.x, zp.y, zn.x, zn.y}), phq[hd], z4);
        o2[hd] = num * __builtin_amdgcn_rcpf(den[0] + 1e-6f);
      }
      u32x2 oa = pk4(o2[0]), ob = pk4(o2[1]);
      gst16_sc1<0>(ws + WS_O + (size_t)layer * (2u << 20) + (size_t)(8 * P + w) * 16384 + j * 1024, l16, u32x4{oa.x, oa.y, ob.x, ob.y});
      STAMP(50);
      DRAIN();
      BAR();
      STAMP(4 + 12 * layer);
      if (w == 0) {
        if (layer == 0) {
          u32 sent = first_signal(ctl, myblk + L_REQP, peerP + L_IN1P, lane);
          first_wait(ctl, myblk + L_IN1P, myblk + L_REQP, peerP + L_IN1P, a0, sent, lane);
          a0 = sent;
        } else { next_signal(ctl, peerP + L_IN2P, 4u, lane); next_wait(ctl, myblk + L_IN2P, 4u); }
      }
      BAR();
      STAMP(5 + 12 * layer);
    }
#define TILE_REDUCE() { LAS char* ex = lds + LD_SC + 4096 + c4 * 4096 + l16; \
      if (kh) { _Pragma("unroll") for (int t = 0; t < 4; ++t) *(LAS f32x4*)(ex + t * 1024) = acc[t]; } \
      BAR(); \
      if (!kh) { _Pragma("unroll") for (int m = 0; m < 2; ++m) { \
        f32x4 v0 = acc[2 * m] + *(LAS f32x4*)(ex + 2 * m * 1024), v1 = acc[2 * m + 1] + *(LAS f32x4*)(ex + (2 * m + 1) * 1024); u32x4& r = resid[m]; \
        r.x = pk2(v0[0] + bflo(r.x), v0[1] + bfhi(r.x)); r.y = pk2(v0[2] + bflo(r.y), v0[3] + bfhi(r.y)); \
        r.z = pk2(v1[0] + bflo(r.z), v1[1] + bfhi(r.z)); r.w = pk2(v1[2] + bflo(r.w), v1[3] + bfhi(r.w)); } } }
#define TILE_PUBLISH(XB, SBUF) { const size_t ch = (size_t)(8 * P + 4 * th + c4); char* xo = (XB) + (ch * 16 + 2 * cg) * 1024; \
        gst16_sc1<0>(xo, l16, resid[0]); gst16_sc1<1024>(xo, l16, resid[1]); \
        float sum = 0.f, ssq = 0.f; \
        _Pragma("unroll") for (int e = 0; e < 8; ++e) { sum = dot2(resid[e >> 2][e & 3], 0x3f803f80u, sum); ssq = dot2(resid[e >> 2][e & 3], resid[e >> 2][e & 3], ssq); } \
        red2sum(sum, ssq); \
        if (lane < 16) gst8_sc1((SBUF) + (ch * 8 + cg) * 128, tk * 8u, u32x2{__float_as_uint(sum), __float_as_uint(ssq)}); \
        DRAIN(); }
    FRESH();
    {
      const char* xb = ws + WS_O + (size_t)layer * (2u << 20) + (size_t)(8 * P + 4 * th + c4) * 16384 + kh * 8192;
      LD4(0) LD4(4)
      STAMP(51);
      if (w == 0 && layer == 0) { u32 r; gld4_sc1(r, ctl, myblk + L_REQP); asm volatile("s_waitcnt vmcnt(0)" : "+v"(r)::"memory"); if (r != a0 && lane < 16) gst4_sc1(ctl, peerP + L_IN1P, r + 1u); }
#pragma unroll
      for (int t = 0; t < 4; ++t) acc[t] = z4;
      LAS char* wa[4];
#pragma unroll
      for (int t = 0; t < 4; ++t) wa[t] = opaque(lds + LD_WA3 + kh * 32768 + trb[t]);
      gemm<4, 8, 6>(acc, xf, [&](auto KB, auto T) { return trfrag(wa[T.v] + KB.v * 4096, 2048); },
                    [&](auto I) { constexpr int s = I.v; if constexpr (s % 4 == 0) waitv<7 - s / 4>(xf[s / 4]); });
      STAMP(52);
      TILE_REDUCE()
      STAMP(53);
      if (!kh) TILE_PUBLISH(ws + WS_X1 + (size_t)layer * (2u << 20), ws + WS_ST1 + (size_t)layer * (256u << 10))
      STAMP(54);
      BAR();
      STAMP(6 + 12 * layer);
      if (w == 0) { next_signal(ctl, peerP + L_IN2P, 4u * layer + 1u, lane); if (j < 14) next_wait(ctl, myblk + L_IN2P, 4u * layer + 1u); }
      if (kh && j < 14) { _Pragma("unroll 4") for (int i = 0; i < 16; ++i) { const int n = 64 + c4 * 16 + i; GLDS_B1(n); } }
      BAR();
      STAMP(7 + 12 * layer);
    }
    FRESH();
    if (j < 14) {
      u32x2 st[2];
      const char* sb = ws + WS_ST1 + (size_t)layer * (256u << 10) + (size_t)(8 * P + w) * 1024;
      gld8_sc1<0>(st[0], sb, g * 256u + tk * 8u); gld8_sc1<128>(st[1], sb, g * 256u + tk * 8u);
      const char* xb = ws + WS_X1 + (size_t)layer * (2u << 20) + (size_t)(8 * P + w) * 16384;
      LD4(0) LD4(4) LD4(8) LD4(12)
      STAMP(28);
#pragma unroll
      for (int t = 0; t < 8; ++t) acc[t] = z4;
      LAS char* wa[4];
#pragma unroll
      for (int t = 0; t < 4; ++t) wa[t] = opaque(lds + LD_WB1 + trb[t]);
      gemm<4, 16, 6>(acc, xf, [&](auto KB, auto T) { return trfrag(wa[T.v] + KB.v * 4096, 2048); },
                     [&](auto I) { constexpr int s = I.v; if constexpr (s % 4 == 0) waitv<15 - s / 4>(xf[s / 4]); });
      STAMP(25);
      STATS(st)
      BAR();
      STAMP(26);
      if (layer == 0) {
        LAS char* sp = lds + LD_WB1 + (w >> 2) * 65536 + (4 * g + q) * 128 + 32 * ((w & 3) ^ s4) + 8 * p;
        f32x4 accS = z4;
#pragma unroll
        for (int kb = 0; kb < 16; ++kb) accS = mfma32(trfrag(sp + kb * 4096, 2048), ones, accS);
        if (tk == 0) *(LAS f32x4*)(lds + LD_S + 384 + w * 64 + g * 16) = accS;
        BAR();
      }
      char* hb = ws + WS_H + (size_t)layer * (8u << 20) + ((size_t)(8 * P + w) * 56 + 4 * j) * 1024;
      f32x4 Sv[2];
      auto silu1 = [&](auto T, auto E) {
        constexpr int t = T.v, e = E.v;
        if constexpr (e == 0) Sv[t & 1] = *(LAS f32x4*)(lds + LD_S + 384 + t * 64 + g * 16);
        else {
          const float y = (acc[t][e - 1] - mean * Sv[t & 1][e - 1]) * rstd;
          acc[t][e - 1] = y * __builtin_amdgcn_rcpf(1.f + __builtin_amdgcn_exp2f(y * -1.4426950408889634f));
        }
        if constexpr (e == 4 && (t & 1)) { u32x2 a = pk4(acc[t - 1]), b = pk4(acc[t]); gst16_sc1<(t >> 1) * 1024>(hb, l16, u32x4{a.x, a.y, b.x, b.y}); }
      };
#pragma unroll
      for (int t = 0; t < 4; ++t) wa[t] = opaque(lds + LD_WB1 + 65536 + trb[t]);
      gemm<2, 16, 6>(acc + 4, xf, [&](auto KB, auto T) { return trfrag(wa[T.v] + KB.v * 4096, 2048); }, [&](auto I) {
        constexpr int s = I.v;
        if constexpr (s < 8) GLDS_B2(8 * w + s);
        else if constexpr (s < 28) silu1(IC<(s - 8) / 5>{}, IC<(s - 8) % 5>{});
      });
      STAMP(27);
      gemm<2, 16, 6>(acc + 6, xf, [&](auto KB, auto T) { return trfrag(wa[2 + T.v] + KB.v * 4096, 2048); }, [&](auto I) {
        constexpr int s = I.v;
        if constexpr (s >= 4 && s < 24 && s % 2 == 0) silu1(IC<4 + (s - 4) / 10>{}, IC<((s - 4) / 2) % 5>{});
      });
      STAMP(8 + 12 * layer);
      sfor<0, 10>([&](auto I) { silu1(IC<6 + I.v / 5>{}, IC<I.v % 5>{}); });
    } else {
      BAR();
      if (layer == 0) BAR();
#pragma unroll
      for (int i = 0; i < 8; ++i) GLDS_B2(8 * w + i);
    }
    STAMP(55);
    DRAIN();
    BAR();
    STAMP(9 + 12 * layer);
    FRESH();
    if (w == 0) { next_signal(ctl, peerP + L_IN2P, 4u * layer + 2u, lane); next_wait(ctl, myblk + L_IN2P, 4u * layer + 2u); }
    if (kh) { _Pragma("unroll 4") for (int i = 0; i < 16; ++i) { const int n = 64 + c4 * 16 + i; GLDS_B2(n); } }
    BAR();
    STAMP(10 + 12 * layer);
    FRESH();
    {
      u32x4 hf[28];
      const char* hb = ws + WS_H + (size_t)layer * (8u << 20) + ((size_t)(8 * P + 4 * th + c4) * 56 + 28 * kh) * 1024;
#pragma unroll
      for (int i = 0; i < 28; i += 4) { gld16_sc1<0>(hf[i], hb + i * 1024, l16); gld16_sc1<1024>(hf[i + 1], hb + i * 1024, l16); gld16_sc1<2048>(hf[i + 2], hb + i * 1024, l16); gld16_sc1<3072>(hf[i + 3], hb + i * 1024, l16); }
#pragma unroll
      for (int t = 0; t < 4; ++t) acc[t] = z4;
      STAMP(56);
      LAS char* wa[8];
#pragma unroll
      for (int t = 0; t < 4; ++t) { wa[t] = opaque(lds + LD_RING + kh * 8192 + trb[t]); wa[4 + t] = opaque(lds + LD_RING + 65536 + kh * 8192 + trb[t]); }
      gemm<4, 28, 6>(acc, hf, [&](auto KB, auto T) { constexpr int sl = (KB.v >> 1) & 7; return trfrag(wa[(sl >> 2) * 4 + T.v] + (sl & 3) * 16384 + (KB.v & 1) * 4096, 2048); }, [&](auto I) {
        constexpr int s = I.v, kb = s / 4, T = s / 8;
        if constexpr (s % 4 == 0) {
          constexpr int nref = T < 1 ? 0 : (s % 8 == 0 ? (T - 1 < 6 ? T - 1 : 6) : (T < 6 ? T : 6));
          constexpr int nh = 27 - kb + 2 * nref;
          constexpr int nr = T >= 7 && T <= 12 ? 2 * (12 - T) : 63;
          waitv<(s % 8 == 0 && nr < nh) ? nr : nh>(hf[kb]);
        }
        if constexpr (s == 32) STAMP(57);
        if constexpr (s == 64) STAMP(58);
        if constexpr (s % 8 == 0 && T >= 1 && T <= 12) { asm volatile("" ::: "memory"); __builtin_amdgcn_s_barrier(); asm volatile("" ::: "memory"); }
        if constexpr (s % 8 == 0 && T >= 1 && T <= 6) { GLDS_B2((T + 7) * 16 + 2 * w); GLDS_B2((T + 7) * 16 + 2 * w + 1); }
      });
      STAMP(11 + 12 * layer);
      FRESH();
      TILE_REDUCE()
      if (layer == 0) {
        if (!kh) TILE_PUBLISH(ws + WS_X2, ws + WS_ST2)
        else { _Pragma("unroll 4") for (int i = 0; i < 8; ++i) { const int n = c4 * 8 + i; GLDS_A1(n); } }
        BAR();
        STAMP(12);
        if (w == 0) { next_signal(ctl, peerP + L_IN2P, 3u, lane); next_wait(ctl, myblk + L_IN2P, 3u); }
        if (kh) { _Pragma("unroll 4") for (int i = 0; i < 16; ++i) { const int n = 32 + (i >> 3) * 32 + c4 * 8 + (i & 7); GLDS_A1(n); } }
        BAR();
      } else if (!kh) {
        char* ob = out + (size_t)(128 * P + 64 * th + 16 * c4) * 1024 + 128 * cg;
        gst8<0>(ob, tk * 1024u + 8u * g, u32x2{resid[0].x, resid[0].y}); gst8<32>(ob, tk * 1024u + 8u * g, u32x2{resid[0].z, resid[0].w});
        gst8<64>(ob, tk * 1024u + 8u * g, u32x2{resid[1].x, resid[1].y}); gst8<96>(ob, tk * 1024u + 8u * g, u32x2{resid[1].z, resid[1].w});
      }
    }
  }
}

extern "C" void kernel_launch(void* const* d_in, const int* in_sizes, int n_in, void* d_out, int out_size, void* d_ws, size_t ws_size, hipStream_t stream) {
  mega<<<256, 512, 0, stream>>>((const char*)d_in[0], (const char*)d_in[1], (const char*)d_in[2], (const char*)d_in[3], (const char*)d_in[5], (const char*)d_in[6], (char*)d_out, (char*)d_ws);
}
```

```cpp
#include <hip/hip_runtime.h>
#include <hip/hip_bf16.h>

typedef unsigned u32;
typedef unsigned short u16;
typedef u32 u32x4 __attribute__((ext_vector_type(4)));
typedef u32 u32x2 __attribute__((ext_vector_type(2)));
typedef float f32x16 __attribute__((ext_vector_type(16)));
typedef float f32x4 __attribute__((ext_vector_type(4)));
typedef float f32x2 __attribute__((ext_vector_type(2)));
typedef short s16x4 __attribute__((ext_vector_type(4)));
typedef __bf16 bf16x2 __attribute__((ext_vector_type(2)));
typedef __bf16 bf16x8 __attribute__((ext_vector_type(8)));
#define LAS __attribute__((address_space(3)))
#define DI __device__ __forceinline__

constexpr size_t WS_CTL = 0;
constexpr size_t WS_ST1 = 512u << 10;
constexpr size_t WS_ST2 = 1024u << 10;
constexpr size_t WS_KVP = 1280u << 10;
constexpr u32 KVP_UNIT = 9216;
constexpr size_t WS_O = 4u << 20;
constexpr size_t WS_X1 = 8u << 20;
constexpr size_t WS_X2 = 12u << 20;
constexpr size_t WS_H = 14u << 20;
constexpr size_t WS_STAMP = 40u << 20;
constexpr u32 C_IN1 = 1024, C_REQ = 1152;

constexpr u32 LD_STG = 96 * 1024;
constexpr u32 LD_WA3 = 64 * 1024;
constexpr u32 LD_SC = 128 * 1024;
constexpr u32 LD_HX2 = 144 * 1024;
constexpr u32 LD_RS = 152 * 1024;
constexpr u32 LD_STAMP = 154 * 1024;
constexpr u32 LD_S = 158 * 1024;
constexpr u32 SPIN_CAP = 1u << 17;

#define BAR() do { asm volatile("s_waitcnt lgkmcnt(0)" ::: "memory"); __builtin_amdgcn_s_barrier(); asm volatile("" ::: "memory"); } while (0)
#define RAWBAR() do { asm volatile("" ::: "memory"); __builtin_amdgcn_s_barrier(); asm volatile("" ::: "memory"); } while (0)
#define FRESH() do { asm volatile("" : "+s"(ws), "+s"(P), "+s"(j), "+s"(w), "+s"(x), "+s"(wqkv), "+s"(wo), "+s"(w1), "+s"(w2)); asm volatile("" : "+v"(lane)); th = j >> 3; cg = j & 7; cc = w & 1; kh = (w >> 1) & 1; \
  hh = lane >> 5; tk = lane & 31; g = lane >> 4; t16 = lane & 15; q = (lane >> 2) & 3; p = lane & 3; l16 = lane * 16u; } while (0)
#define TRB0 ((4 * hh + q) * 128 + 64 * (q >> 1) + 32 * (g & 1) + 8 * p)
#define TRB1 (TRB0 ^ 64)
#define TRA ((4 * hh + q) * 64 + 32 * (g & 1) + 8 * p)
#define HEL_OWN ((u32)(P * 16 + j) * 2048u + 4u * tk)
#define HEL_PEER (tk < 16 ? (u32)(P * 16 + tk) * 2048u + 4u * j : (u32)((tk - 16) * 16 + j) * 2048u + 64u + 4u * P)
#define PCNT(l, n) ((u32)P * 32768u + (l) * 2048u + (n) * 128u)
#define KCNT(l) ((u32)((1 + (l)) * 16 + j) * 2048u + 896u)
#define DRAIN() asm volatile("s_waitcnt vmcnt(0)" ::: "memory")

#define STAMP(i) do {} while (0)

template <int N> DI void waitv(u32x4& a) { asm volatile("s_waitcnt vmcnt(%1)" : "+v"(a) : "i"(N)); }
template <int N> DI void waitv4(u32x4& a, u32x4& b, u32x4& c, u32x4& d) { asm volatile("s_waitcnt vmcnt(%4)" : "+v"(a), "+v"(b), "+v"(c), "+v"(d) : "i"(N)); }
DI void waitl8(u32x4* f) { asm volatile("" : "+v"(f[0]), "+v"(f[1]), "+v"(f[2]), "+v"(f[3]), "+v"(f[4]), "+v"(f[5]), "+v"(f[6]), "+v"(f[7])::"memory"); }
DI void pin(u32x4& a) { asm volatile("" : "+v"(a)); }
DI void pin2(u32x2& a) { asm volatile("" : "+v"(a)); }
DI void pin1(u32& a) { asm volatile("" : "+v"(a)); }

template <int OFF> DI void gld16_sc1(u32x4& d, const char* sb, u32 vo) { asm volatile("global_load_dwordx4 %0, %1, %2 offset:%3 sc1" : "=v"(d) : "v"(vo), "s"(sb), "i"(OFF) : "memory"); }
template <int OFF> DI void gld16(u32x4& d, const char* sb, u32 vo) { asm volatile("global_load_dwordx4 %0, %1, %2 offset:%3" : "=v"(d) : "v"(vo), "s"(sb), "i"(OFF) : "memory"); }
template <int OFF> DI void gld8(u32x2& d, const char* sb, u32 vo) { asm volatile("global_load_dwordx2 %0, %1, %2 offset:%3" : "=v"(d) : "v"(vo), "s"(sb), "i"(OFF) : "memory"); }
DI void gld4_sc1(u32& d, const char* sb, u32 vo) { asm volatile("global_load_dword %0, %1, %2 sc1" : "=v"(d) : "v"(vo), "s"(sb) : "memory"); }
template <int OFF> DI void gst16_sc1(char* sb, u32 vo, u32x4 d) { asm volatile("global_store_dwordx4 %0, %1, %2 offset:%3 sc1\n\ts_nop 1" ::"v"(vo), "v"(d), "s"(sb), "i"(OFF) : "memory"); }
DI void gst8_sc1(char* sb, u32 vo, u32x2 d) { asm volatile("global_store_dwordx2 %0, %1, %2 sc1" ::"v"(vo), "v"(d), "s"(sb) : "memory"); }
template <int OFF> DI void gst8(char* sb, u32 vo, u32x2 d) { asm volatile("global_store_dwordx2 %0, %1, %2 offset:%3" ::"v"(vo), "v"(d), "s"(sb), "i"(OFF) : "memory"); }
DI void gadd1(char* sb, u32 vo) { asm volatile("global_atomic_add %0, %1, %2 sc1" ::"v"(vo), "v"(1u), "s"(sb) : "memory"); }
template <int OFF> DI void gst16(char* sb, u32 vo, u32x4 d) { asm volatile("global_store_dwordx4 %0, %1, %2 offset:%3\n\ts_nop 1" ::"v"(vo), "v"(d), "s"(sb), "i"(OFF) : "memory"); }
DI void gst4_sc1(char* sb, u32 vo, u32 d) { asm volatile("global_store_dword %0, %1, %2 sc1" ::"v"(vo), "v"(d), "s"(sb) : "memory"); }
DI void glds(u32 ldsaddr, const char* sb, u32 vo) { asm volatile("s_mov_b32 m0, %0\n\ts_nop 0\n\tglobal_load_lds_dwordx4 %1, %2" ::"s"(ldsaddr), "v"(vo), "s"(sb) : "memory"); }

DI bf16x8 trfrag(LAS char* p, int second) {
  s16x4 a = __builtin_amdgcn_ds_read_tr16_b64_v4i16((LAS s16x4*)p);
  s16x4 b = __builtin_amdgcn_ds_read_tr16_b64_v4i16((LAS s16x4*)(p + second));
  return __builtin_bit_cast(bf16x8, __builtin_shufflevector(a, b, 0, 1, 2, 3, 4, 5, 6, 7));
}
DI bf16x8 asb(u32x4 v) { return __builtin_bit_cast(bf16x8, v); }
DI f32x4 mfma16(bf16x8 a, bf16x8 b, f32x4 c) { return __builtin_amdgcn_mfma_f32_16x16x32_bf16(a, b, c, 0, 0, 0); }
DI f32x16 mfma(bf16x8 a, bf16x8 b, f32x16 c) { return __builtin_amdgcn_mfma_f32_32x32x16_bf16(a, b, c, 0, 0, 0); }
DI u32 pk2(float a, float b) { f32x2 v = {a, b}; return __builtin_bit_cast(u32, __builtin_convertvector(v, bf16x2)); }
DI u32 pk2v(f32x2 v) { return __builtin_bit_cast(u32, __builtin_convertvector(v, bf16x2)); }
DI u32x2 pk4(f32x4 v) { u32x2 r; r.x = pk2(v[0], v[1]); r.y = pk2(v[2], v[3]); return r; }
DI float bflo(u32 u) { return __uint_as_float(u << 16); }
DI float bfhi(u32 u) { return __uint_as_float(u & 0xffff0000u); }
DI float dot2(u32 a, u32 b, float c) { return __builtin_amdgcn_fdot2_f32_bf16(__builtin_bit_cast(bf16x2, a), __builtin_bit_cast(bf16x2, b), c, false); }
DI void swap32(float& a, float& b) { auto r = __builtin_amdgcn_permlane32_swap(__float_as_uint(a), __float_as_uint(b), false, false); a = __uint_as_float(r[0]); b = __uint_as_float(r[1]); }
DI void swap16(float& a, float& b) { auto r = __builtin_amdgcn_permlane16_swap(__float_as_uint(a), __float_as_uint(b), false, false); a = __uint_as_float(r[0]); b = __uint_as_float(r[1]); }
DI float mx2(float a, float b) { return __builtin_amdgcn_fmed3f(a, b, __builtin_inff()); }
DI float mx3(float a, float b, float c) { return __builtin_fmaxf(__builtin_fmaxf(a, b), c); }
DI void red2max(float& a, float& b) { swap32(a, b); float u = mx2(a, b); a = u; b = u; swap32(a, b); }
DI void red2sum(float& a, float& b) { swap32(a, b); float u = a + b; a = u; b = u; swap32(a, b); }
template <int CTRL> DI float dppadd(float v) { return v + __uint_as_float(__builtin_amdgcn_update_dpp(0u, __float_as_uint(v), CTRL, 0xf, 0xf, true)); }
DI float rowsum16(float v) { v = dppadd<0xB1>(v); v = dppadd<0x4E>(v); v = dppadd<0x141>(v); v = dppadd<0x140>(v); return v; }
DI f32x2 pr(const f32x16& a, int i) { return f32x2{a[2 * i], a[2 * i + 1]}; }

DI void softmax2(const f32x16& p, const float L2E  , u32x4 (&f)[2][2]) {
  float mx[2], mn[2];
#pragma unroll
  for (int hd = 0; hd < 2; ++hd) {
    const int b = 8 * hd;
    mx[hd] = mx2(mx3(mx3(mx3(p[b], p[b + 1], p[b + 2]), p[b + 3], p[b + 4]), p[b + 5], p[b + 6]), p[b + 7]);
    mn[hd] = mx2(mx3(mx3(mx3(-p[b], -p[b + 1], -p[b + 2]), -p[b + 3], -p[b + 4]), -p[b + 5], -p[b + 6]), -p[b + 7]);
    red2max(mx[hd], mn[hd]);
  }
  f32x2 e[2][2][4]; float s[2][2];
#pragma unroll
  for (int hd = 0; hd < 2; ++hd) {
    const float cp = -mx[hd] * L2E, cn = -mn[hd] * L2E;
#pragma unroll
    for (int i = 0; i < 4; ++i) {
      const f32x2 v = pr(p, 4 * hd + i), a = v * L2E + cp, b = v * -L2E + cn;
      e[hd][0][i] = f32x2{__builtin_amdgcn_exp2f(a.x), __builtin_amdgcn_exp2f(a.y)};
      e[hd][1][i] = f32x2{__builtin_amdgcn_exp2f(b.x), __builtin_amdgcn_exp2f(b.y)};
    }
#pragma unroll
    for (int sg = 0; sg < 2; ++sg) { const f32x2 t = (e[hd][sg][0] + e[hd][sg][1]) + (e[hd][sg][2] + e[hd][sg][3]); s[hd][sg] = t.x + t.y; }
    red2sum(s[hd][0], s[hd][1]);
#pragma unroll
    for (int sg = 0; sg < 2; ++sg) {
      const float r = __builtin_amdgcn_rcpf(s[hd][sg]);
#pragma unroll
      for (int i = 0; i < 4; ++i) f[hd][sg][i] = pk2v(e[hd][sg][i] * r);
    }
  }
}
DI void pack16(const f32x16& a, u32x4 (&y)[2]) {
#pragma unroll
  for (int i = 0; i < 8; ++i) y[i >> 2][i & 3] = pk2v(pr(a, i));
}
DI void lnfix(f32x16& a, LAS char* Sp, u32 negmean) { a = mfma(asb(u32x4{*(LAS u32*)Sp, 0u, 0u, 0u}), asb(u32x4{negmean, 0u, 0u, 0u}), a); }
DI u32 split2(float v) { const u32 hi = pk2(v, v) & 0xffffu; return hi | (pk2(v - bflo(hi), 0.f) << 16); }

template <int V> struct IC { static constexpr int v = V; };
template <int B, int E, class F> DI void sfor(F&& f) { if constexpr (B < E) { f(IC<B>{}); sfor<B + 1, E>(f); } }
#define SB() __builtin_amdgcn_sched_barrier(0)
template <int NT, int K0, int K1, int D, int XM, class LD, class PRE, class POST> DI void gemm(f32x16* acc, u32x4* xf, LD&& ld, PRE&& pre, POST&& post) {
  bf16x8 wf[D + 1];
  constexpr int S0 = K0 * NT, S1 = K1 * NT;
  sfor<S0, S0 + D>([&](auto I) { constexpr int s = I.v; wf[s % (D + 1)] = ld(IC<s / NT>{}, IC<s % NT>{}); });
  SB();
  sfor<S0, S1>([&](auto I) {
    constexpr int s = I.v;
    pre(I);
    if constexpr (s + D < S1) wf[(s + D) % (D + 1)] = ld(IC<(s + D) / NT>{}, IC<(s + D) % NT>{});
    SB();
    post(I);
    acc[s % NT] = mfma(wf[s % (D + 1)], asb(xf[(s / NT) % XM]), acc[s % NT]);
    asm volatile("" : "+v"(acc[s % NT]));
    SB();
  });
}
constexpr int b2slot(int t) { return t < 4 ? t * 16384 : t == 4 || t == 13 ? 131072 : t < 9 ? 65536 + (t - 5) * 16384 : (t - 9) * 16384; }
DI LAS char* opaque(LAS char* p) { asm volatile("" : "+v"(p)); return p; }

DI void cnt_wait(const char* ctl, u32 off, u32 val) {
  for (u32 sp = 0; sp < SPIN_CAP; ++sp) {
    u32 v; gld4_sc1(v, ctl, off); asm volatile("s_waitcnt vmcnt(0)" : "+v"(v)::"memory");
    if (__builtin_amdgcn_readfirstlane(v) >= val) break;
  }
}
#define HANDOFF(n) do { RAWBAR(); if (w == 0) { if (lane == 0) gadd1(ws, PCNT(layer, n)); cnt_wait(ws, PCNT(layer, n), 16u); } RAWBAR(); } while (0)

__global__ __launch_bounds__(512, 2) void mega(const char* x, const char* wqkv, const char* wphi, const char* wo, const char* w1, const char* w2, char* out, char* ws) {
  __shared__ __attribute__((aligned(16))) char smem[163840];
  LAS char* lds = (LAS char*)smem;
  const u32 lb = (u32)(size_t)lds;
  int lane = threadIdx.x & 63; int w = __builtin_amdgcn_readfirstlane(threadIdx.x >> 6);
  int hh = lane >> 5, tk = lane & 31, g = lane >> 4, t16 = lane & 15, q = (lane >> 2) & 3, p = lane & 3;
  const int bid = blockIdx.x, loc = bid >> 3;
  int P = (bid & 7) * 2 + (loc >> 4), j = loc & 15;
  int th = j >> 3, cg = j & 7, cc = w & 1, kh = (w >> 1) & 1;
  u32 l16 = lane * 16u;
  const u32x4 ones4 = {0x3f803f80u, 0x3f803f80u, 0x3f803f80u, 0x3f803f80u};
  const f32x16 z16 = {0.f, 0.f, 0.f, 0.f, 0.f, 0.f, 0.f, 0.f, 0.f, 0.f, 0.f, 0.f, 0.f, 0.f, 0.f, 0.f};
  STAMP(0);
  if (w >= 4) {
    const u32 swz = 16u * ((lane & 7) ^ ((g & 1) << 2));
    const u32 vo1024 = (lane >> 3) * 1024u + swz, vo3584 = (lane >> 3) * 3584u + swz, voA1 = (lane >> 2) * 3072u + 16u * (lane & 3);
    u32x4 wa1[16];
    u32x4 wb2[36];
    LAS char* wl = lds + l16;
#define HW (w & 3)
#define GLDS_Q(n) glds(lb + 65536 + (n) * 1024, wqkv + 64 * j + (size_t)((n) * 16) * 3072, voA1)
#define GLDS_B1(n) glds(lb + (n) * 1024, w1 + (size_t)(((n) & 63) * 8) * 3584 + j * 256 + ((n) >> 6) * 128, vo3584)
#define B2SRC(n) (w2 + cg * 128 + (size_t)(896 * (((n) >> 3) & 1) + (((n) >> 4) < 7 ? 128 * ((n) >> 4) : 128 * ((n) >> 4) - 832) + 8 * ((n) & 7)) * 1024)
#define GLDS_B2(n) glds(lb + ((n) < 64 ? (n) : (n) + 64) * 1024, B2SRC(n), vo1024)
#define GLDS_A3(n) glds(lb + LD_WA3 + (n) * 1024, wo + (size_t)(n) * 8192 + cg * 128, vo1024)
    auto putA1 = [&](auto R, auto M) { *(LAS u32x4*)(wl + R.v * 32768 + (4 * M.v + HW) * 1024) = wa1[R.v * 8 + M.v]; };
    auto putB2 = [&](auto T) { sfor<0, 4>([&](auto I) { *(LAS u32x4*)(wl + b2slot(T.v) + (4 * HW + I.v) * 1024) = wb2[(T.v - 5) * 4 + I.v]; }); };
    auto ldB2 = [&](auto T) { sfor<0, 4>([&](auto I) { gld16<0>(wb2[(T.v - 5) * 4 + I.v], B2SRC(T.v * 16 + 4 * HW + I.v), vo1024); }); };
    auto ldB1b = [&](u32x4* r) { const char* b = w1 + (size_t)(HW * 128) * 3584 + (j < 14 ? j : 13) * 256 + 128; sfor<0, 16>([&](auto I) { gld16<0>(r[I.v], b + (size_t)(I.v * 8) * 3584, vo3584); }); };
    auto putB1b = [&](u32x4* r) { sfor<0, 16>([&](auto I) { pin(r[I.v]); *(LAS u32x4*)(wl + 65536 + (HW * 16 + I.v) * 1024) = r[I.v]; }); };
    auto colsum = [&](LAS char* sp, int kstride, int second, auto K0, auto K1, f32x16& a) {
      bf16x8 wf[3];
      wf[K0.v % 3] = trfrag(sp + K0.v * kstride, second); wf[(K0.v + 1) % 3] = trfrag(sp + (K0.v + 1) * kstride, second);
      sfor<K0.v, K1.v>([&](auto K) {
        if constexpr (K.v + 2 < K1.v) wf[(K.v + 2) % 3] = trfrag(sp + (K.v + 2) * kstride, second);
        a = mfma(asb(ones4), wf[K.v % 3], a); asm volatile("" : "+v"(a)); SB();
      });
    };
    auto putS = [&](int tile, const f32x16& a) { const u32 sp = split2(a[0]); *(LAS u32*)(lds + LD_S + tile * 256 + lane * 4) = hh ? (sp >> 16) * 0x10001u : (sp & 0xffffu) * 0x10001u; };
    {
      u32 a0 = 0, sent = 0;
      if (HW == 3) {
        gld4_sc1(a0, ws, HEL_OWN + C_IN1); gst16_sc1<0>(ws + (size_t)(P * 16 + j) * 2048, l16, u32x4{0u, 0u, 0u, 0u});
        asm volatile("s_waitcnt vmcnt(0)" : "+v"(a0)::"memory");
        if (lane < 32) gst4_sc1(ws, HEL_PEER + C_REQ, a0);
      }
      const char* wb = wqkv + 64 * j + (size_t)(16 * HW) * 3072;
      sfor<0, 8>([&](auto M) { gld16<1024>(wa1[M.v], wb + (size_t)(64 * M.v) * 3072, voA1); gld16<2048>(wa1[8 + M.v], wb + (size_t)(64 * M.v) * 3072, voA1); });
      sfor<0, 4>([&](auto C) {
        constexpr int c = C.v;
        waitv4<12 - 4 * c>(wa1[2 * c], wa1[2 * c + 1], wa1[8 + 2 * c], wa1[8 + 2 * c + 1]);
        putA1(IC<0>{}, IC<2 * c>{}); putA1(IC<0>{}, IC<2 * c + 1>{}); putA1(IC<1>{}, IC<2 * c>{}); putA1(IC<1>{}, IC<2 * c + 1>{});
      });
      if (HW == 3) {
        gld4_sc1(sent, ws, HEL_OWN + C_REQ); asm volatile("s_waitcnt vmcnt(0)" : "+v"(sent)::"memory");
        if (lane < 32) gst4_sc1(ws, HEL_PEER + C_IN1, sent + 1u);
      }
      STAMP(33);
      BAR();
      if (HW >= 2) { _Pragma("unroll 4") for (int i = 0; i < 16; ++i) GLDS_Q((HW - 2) * 16 + i); }
      else { f32x16 aS = z16; colsum(lds + HW * 32768 + TRA, 1024, 512, IC<0>{}, IC<32>{}, aS); putS(HW, aS); }
      if (HW == 3) {
        for (u32 sp_ = 0; sp_ < SPIN_CAP; ++sp_) {
          u32 v, r; gld4_sc1(v, ws, HEL_OWN + C_IN1); gld4_sc1(r, ws, HEL_OWN + C_REQ);
          asm volatile("s_waitcnt vmcnt(0)" : "+v"(v), "+v"(r)::"memory");
          if (r != sent) { sent = r; if (lane < 32) gst4_sc1(ws, HEL_PEER + C_IN1, r + 1u); }
          if (__all(v != a0)) break;
        }
        u32 r; gld4_sc1(r, ws, HEL_OWN + C_REQ); asm volatile("s_waitcnt vmcnt(0)" : "+v"(r)::"memory");
        if (r != sent && lane < 32) gst4_sc1(ws, HEL_PEER + C_IN1, r + 1u);
      }
      DRAIN();
      STAMP(39);
      BAR();
      ldB1b(wb2 + 20);
      if (HW == 2) { f32x16 aS = z16; colsum(lds + 65536 + TRA, 1024, 512, IC<0>{}, IC<32>{}, aS); putS(2, aS); }
      BAR();
      RAWBAR();
    }
    sfor<0, 2>([&](auto L) {
      constexpr int layer = L.v;
      FRESH();
      if constexpr (layer == 1) {
        RAWBAR();
        _Pragma("unroll 4") for (int i = 0; i < 8; ++i) GLDS_Q(HW * 8 + i);
        ldB1b(wa1);
        asm volatile("s_waitcnt vmcnt(16)" ::: "memory");
        STAMP(42);
        RAWBAR();
        RAWBAR();
      }
      _Pragma("unroll 4") for (int i = 0; i < 8; ++i) GLDS_A3(32 + HW * 8 + i);
      if (j < 14) { _Pragma("unroll 4") for (int i = 0; i < 8; ++i) GLDS_B1(HW * 16 + i); }
      STAMP(36);
      RAWBAR();
      _Pragma("unroll 4") for (int i = 0; i < 8; ++i) GLDS_A3(HW * 8 + i);
      RAWBAR();
      RAWBAR();
      DRAIN();
      RAWBAR(); RAWBAR();
      if (j < 14) { _Pragma("unroll 4") for (int i = 8; i < 16; ++i) GLDS_B1(HW * 16 + i); }
      RAWBAR();
      putB1b(layer == 0 ? wb2 + 20 : wa1);
      DRAIN();
      RAWBAR(); BAR();
      FRESH();
      if (layer == 0 && j < 14) { f32x16 aS = z16; colsum(lds + (HW >> 1) * 65536 + ((HW & 1) ? TRB1 : TRB0), 2048, 1024, IC<0>{}, IC<32>{}, aS); putS(3 + HW, aS); }
      BAR();
      if constexpr (layer == 0) { ldB2(IC<5>{}); ldB2(IC<6>{}); ldB2(IC<7>{}); ldB2(IC<8>{}); ldB2(IC<9>{}); }
      STAMP(25);
      RAWBAR();
      _Pragma("unroll 4") for (int i = 0; i < 20; ++i) GLDS_B2(HW * 20 + i);
      STAMP(34);
      RAWBAR();
      if (HW == 0) { if (lane == 0) gadd1(ws, PCNT(layer, 2)); cnt_wait(ws, PCNT(layer, 2), 16u); }
      STAMP(35);
      RAWBAR();
      FRESH();
      DRAIN();
      sfor<0, 16>([&](auto I) { pin(wb2[I.v]); });
      putB2(IC<5>{}); putB2(IC<6>{});
      STAMP(38);
      RAWBAR();
      if (HW == 0 && lane == 0) gadd1(ws, PCNT(layer, 3));
      putB2(IC<7>{}); putB2(IC<8>{});
      BAR();
      if constexpr (layer == 0) { ldB2(IC<10>{}); ldB2(IC<11>{}); ldB2(IC<12>{}); ldB2(IC<13>{}); }
      if (HW == 0) cnt_wait(ws, PCNT(layer, 3), 16u);
      STAMP(57);
      RAWBAR();
      DRAIN();
      sfor<16, 36>([&](auto I) { pin(wb2[I.v]); });
      putB2(IC<9>{}); putB2(IC<10>{}); putB2(IC<11>{}); putB2(IC<12>{});
      STAMP(59);
      BAR();
      putB2(IC<13>{});
      BAR();
      if constexpr (layer == 0) sfor<0, 8>([&](auto M) { putA1(IC<0>{}, M); });
      RAWBAR();
      if constexpr (layer == 0) {
        sfor<0, 8>([&](auto M) { putA1(IC<1>{}, M); });
        RAWBAR(); BAR();
      }
    });
  } else {
    u32x4 resid[2];
    u32x4 wph[2];
    u32x4 xf[32];
    f32x16 acc[4];
    float rstd; u32 nmean;
    auto nop = [&](auto) {};
#define LDF(i, n) gld16_sc1<((n) & 3) * 1024>(xf[i], xb + ((n) >> 2) * 4096, l16)
#define FINSTATS(sum, ssq) { red2sum(sum, ssq); const float mean = sum * (1.f / 512.f); rstd = __builtin_amdgcn_rsqf(mx2(ssq * (1.f / 512.f) - mean * mean, 0.f) + 1e-5f); nmean = split2(-mean); }
#define STATS() { _Pragma("unroll") for (int i = 0; i < 4; ++i) pin(st[i]); \
      const f32x4 v = (__builtin_bit_cast(f32x4, st[0]) + __builtin_bit_cast(f32x4, st[1])) + (__builtin_bit_cast(f32x4, st[2]) + __builtin_bit_cast(f32x4, st[3])); \
      float a = v[0], b = v[1], c = v[2], d = v[3]; swap16(a, c); swap16(b, d); float sum = a + c, ssq = b + d; FINSTATS(sum, ssq) }
#define LDSTATS(SB_) { const char* sb = (SB_) + (size_t)(4 * P + w) * 4096; \
      gld16_sc1<0>(st[0], sb, l16); gld16_sc1<1024>(st[1], sb, l16); gld16_sc1<2048>(st[2], sb, l16); gld16_sc1<3072>(st[3], sb, l16); }
    constexpr int N0 = 10;
#define STREAM32(SB_) { if constexpr (s % 2 == 0) waitv<(N0 + s <= 32 ? N0 + s : s > 32 - N0 ? 36 : 32) - s / 2 - 1>(xf[s / 2]); \
      if constexpr (N0 + s < 32) LDF(N0 + s, N0 + s); if constexpr (N0 + s == 32) LDSTATS(SB_) }
#define RES2ACC() { _Pragma("unroll") for (int i = 0; i < 8; ++i) { const u32 r = resid[i >> 2][i & 3]; acc[0][2 * i] = bflo(r); acc[0][2 * i + 1] = bfhi(r); } acc[1] = z16; asm volatile("" : "+v"(acc[0])); }
    for (int layer = 0; layer < 2; ++layer) {
      FRESH();
      acc[0] = z16; acc[1] = z16; acc[2] = z16;
      LAS char* wa1p = opaque(lds + TRA);
      auto ldkv = [&](auto KB, auto T) { return trfrag(wa1p + T.v * 32768 + KB.v * 1024, 512); };
      if (layer == 0) {
        const char* xb = x + (size_t)(128 * P + 32 * w) * 1024;
        u32 xvo[4];
#pragma unroll
        for (int i = 0; i < 4; ++i) xvo[i] = g * 1024u + ((t16 ^ (4 * i + g)) * 16u);
        const u32 stg = lb + 65536 + w * 16384;
        auto ldx = [&](auto R) { sfor<0, 8>([&](auto I) { glds(stg + (R.v & 1) * 8192 + I.v * 1024, xb + R.v * 256 + I.v * 4096, xvo[I.v & 3]); }); };
        ldx(IC<0>{}); ldx(IC<1>{});
        u32 wp[8];
        STAMP(32);
        const u32 str = 65536 + w * 16384 + tk * 256 + t16 * 16 + 8 * hh;
        float sq[4] = {0.f, 0.f, 0.f, 0.f};
        f32x16 asum = z16;
        sfor<0, 4>([&](auto R) {
          constexpr int b = 8 * R.v;
          asm volatile("s_waitcnt vmcnt(%0)" ::"i"(8) : "memory");
#pragma unroll
          for (int i = 0; i < 8; ++i) {
            u32x2 lo = *(LAS u32x2*)(lds + ((str ^ (32 * i)) + (R.v & 1) * 8192)), hi = *(LAS u32x2*)(lds + ((str ^ (32 * i + 16)) + (R.v & 1) * 8192));
            xf[b + i] = u32x4{lo.x, lo.y, hi.x, hi.y};
          }
          if ((cg >> 1) == R.v && (w >> 1) == th) {
            LAS char* rx = lds + LD_SC + cc * 4096 + l16;
            if (cg & 1) { _Pragma("unroll") for (int i = 0; i < 4; ++i) *(LAS u32x4*)(rx + i * 1024) = xf[b + 4 + i]; }
            else { _Pragma("unroll") for (int i = 0; i < 4; ++i) *(LAS u32x4*)(rx + i * 1024) = xf[b + i]; }
          }
          if constexpr (R.v < 2) { waitl8(xf + b); ldx(IC<R.v + 2>{}); }
          if constexpr (R.v == 2) {
            const u16* wsrc = (const u16*)wphi + (2 * j + (tk >> 4)) * 256 + (4 * hh) * 16 + t16;
#pragma unroll
            for (int i = 0; i < 8; ++i) asm volatile("global_load_ushort %0, %1, off offset:%2" : "=v"(wp[i]) : "v"(wsrc), "i"((i >> 2) * 256 + (i & 3) * 32) : "memory");
          }
          SB();
          sfor<0, 8>([&](auto I) {
            asum = mfma(asb(ones4), asb(xf[b + I.v]), asum); asm volatile("" : "+v"(asum));
#pragma unroll
            for (int e = 0; e < 4; ++e) sq[e] = dot2(xf[b + I.v][e], xf[b + I.v][e], sq[e]);
            asm volatile("" : "+v"(sq[0]), "+v"(sq[1]), "+v"(sq[2]), "+v"(sq[3]));
            SB();
          });
        });
        float sum = asum[0] * 0.5f, ssq = (sq[0] + sq[1]) + (sq[2] + sq[3]);
        FINSTATS(sum, ssq)
        STAMP(33);
        BAR();
        STAMP(54);
        resid[0] = *(LAS u32x4*)(lds + LD_SC + cc * 4096 + kh * 2048 + l16); resid[1] = *(LAS u32x4*)(lds + LD_SC + cc * 4096 + kh * 2048 + 1024 + l16);
        gemm<2, 0, 32, 4, 32>(acc, xf, ldkv, nop, nop);
        STAMP(39);
        DRAIN();
#pragma unroll
        for (int i = 0; i < 8; ++i) pin1(wp[i]);
        const u32x4 wv = {wp[0] | (wp[1] << 16), wp[2] | (wp[3] << 16), wp[4] | (wp[5] << 16), wp[6] | (wp[7] << 16)};
        const u32x4 zz = {0u, 0u, 0u, 0u};
        wph[0] = tk < 16 ? wv : zz; wph[1] = tk < 16 ? zz : wv;
        pin(resid[0]); pin(resid[1]);
        RAWBAR();
      } else {
        STAMP(40);
        u32x4 st[4];
        const char* xb = ws + WS_X2 + (size_t)(4 * P + w) * 32768;
        sfor<0, N0>([&](auto I) { LDF(I.v, I.v); });
        STAMP(41);
        gemm<2, 0, 32, 4, 32>(acc, xf, ldkv, [&](auto I) {
          constexpr int s = I.v;
          if constexpr (s == 22) { STAMP(44); RAWBAR(); STAMP(45); }
          STREAM32(ws + WS_ST2)
        }, nop);
        DRAIN();
        STATS()
      }
      STAMP(1 + 12 * layer);
      FRESH();
      {
        u32x4 yk[2], yv[2], f[2][2];
        lnfix(acc[0], lds + LD_S + lane * 4, nmean); lnfix(acc[1], lds + LD_S + 256 + lane * 4, nmean);
        pack16(acc[0], yk);
        f32x16 pp = mfma(asb(wph[0]), asb(yk[0]), z16); pp = mfma(asb(wph[1]), asb(yk[1]), pp);
        pack16(acc[1] * rstd, yv);
        LAS char* kt = lds + LD_SC + (32 * w + tk) * 200 + 8 * hh;
#pragma unroll
        for (int hd = 0; hd < 2; ++hd) { *(LAS u32x2*)(kt + hd * 96 + 64) = u32x2{yv[hd].x, yv[hd].y}; *(LAS u32x2*)(kt + hd * 96 + 80) = u32x2{yv[hd].z, yv[hd].w}; }
        softmax2(pp, rstd * 1.4426950408889634f, f);
#pragma unroll
        for (int hd = 0; hd < 2; ++hd) {
          *(LAS u32x2*)(kt + hd * 96) = u32x2{f[hd][0].x, f[hd][0].y}; *(LAS u32x2*)(kt + hd * 96 + 16) = u32x2{f[hd][0].z, f[hd][0].w};
          *(LAS u32x2*)(kt + hd * 96 + 32) = u32x2{f[hd][1].x, f[hd][1].y}; *(LAS u32x2*)(kt + hd * 96 + 48) = u32x2{f[hd][1].z, f[hd][1].w};
        }
      }
      {
        LAS char* ka = lds + LD_SC + (4 * g + q) * 200 + 8 * p + (w >> 1) * 96;
        char* kb_ = ws + WS_KVP + (size_t)((layer * 16 + j) * 4 + w) * KVP_UNIT;
        asm volatile("" : "+v"(ka)); asm volatile("" : "+s"(kb_));
        STAMP(42);
        BAR();
        STAMP(43);
        f32x4 akv = {0.f, 0.f, 0.f, 0.f}, az = {0.f, 0.f, 0.f, 0.f};
        bf16x8 A[4], B[4];
#pragma unroll
        for (int ks = 0; ks < 4; ++ks) { A[ks] = trfrag(ka + ks * 6400 + (w & 1) * 32, 3200); B[ks] = trfrag(ka + ks * 6400 + 64, 3200); }
        SB();
#pragma unroll
        for (int ks = 0; ks < 4; ++ks) { akv = mfma16(A[ks], B[ks], akv); az = mfma16(A[ks], asb(ones4), az); }
        {
          const u32x2 kp = pk4(akv);
          auto r0 = __builtin_amdgcn_permlane32_swap(kp.x, kp.x, false, false), r1 = __builtin_amdgcn_permlane32_swap(kp.y, kp.y, false, false);
          if (lane < 32) gst16_sc1<0>(kb_ + P * 512, l16, u32x4{r0[0], r1[0], r0[1], r1[1]});
        }
        asm volatile("s_nop 7" : "+v"(az));
        if (t16 == 0) gst16_sc1<0>(kb_ + 8192 + P * 64, g * 16u, __builtin_bit_cast(u32x4, az));
        DRAIN();
      }
      STAMP(37);
      RAWBAR();
      if (w == 0 && lane == 0) gadd1(ws, KCNT(layer));
      STAMP(2 + 12 * layer);
      u32x4 phq[2][2];
      {
        LAS char* wq = opaque(lds + 65536 + TRA);
        gemm<1, 0, 32, 4, 32>(acc + 2, xf, [&](auto KB, auto) { return trfrag(wq + KB.v * 1024, 512); }, nop, nop);
        STAMP(36);
        RAWBAR();
        STAMP(46);
        u32x4 yq[2];
        lnfix(acc[2], lds + LD_S + 512 + lane * 4, nmean);
        pack16(acc[2], yq);
        f32x16 pp = mfma(asb(wph[0]), asb(yq[0]), z16); pp = mfma(asb(wph[1]), asb(yq[1]), pp);
        softmax2(pp, rstd * 1.4426950408889634f, phq);
      }
      STAMP(47);
      FRESH();
      if (w == 0) cnt_wait(ws, KCNT(layer), 16u);
      RAWBAR();
      STAMP(3 + 12 * layer);
      {
        const char* xb = ws + WS_KVP + (size_t)((layer * 16 + j) * 4 + w) * KVP_UNIT;
        u32x4 zz;
        gld16_sc1<0>(zz, xb + 8192, t16 * 64u + g * 16u);
        sfor<0, 8>([&](auto I) { LDF(I.v, I.v); });
        RES2ACC()
        LAS char* gi = lds + LD_SC + 16384 + w * 680 + 8 * g;
        LAS char* gr = lds + LD_SC + 16384 + (tk < 16 ? tk : 16) * 40 + 8 * hh;
        char* ob = ws + WS_O + (size_t)layer * (2u << 20) + ((size_t)(4 * P + w) * 32 + 2 * j) * 1024;
        asm volatile("" : "+v"(gi), "+v"(gr)); asm volatile("" : "+s"(ob));
        waitv<8>(zz);
        f32x4 sz = __builtin_bit_cast(f32x4, zz);
#pragma unroll
        for (int e = 0; e < 4; ++e) sz[e] = rowsum16(sz[e]);
        if (t16 == 0) *(LAS u32x2*)(gi + 640) = pk4(sz);
        float sa[8] = {0.f, 0.f, 0.f, 0.f, 0.f, 0.f, 0.f, 0.f};
        u32 c_lo = 0x00003f80u, c_hi = 0x3f800000u; asm volatile("" : "+s"(c_lo), "+s"(c_hi));
        sfor<0, 8>([&](auto I) {
          waitv<7 - I.v>(xf[I.v]);
#pragma unroll
          for (int e = 0; e < 4; ++e) { sa[2 * e] = dot2(xf[I.v][e], c_lo, sa[2 * e]); sa[2 * e + 1] = dot2(xf[I.v][e], c_hi, sa[2 * e + 1]); }
        });
        f32x4 s0;
#pragma unroll
        for (int e = 0; e < 4; ++e) { swap32(sa[e], sa[4 + e]); s0[e] = sa[e] + sa[4 + e]; }
        *(LAS u32x2*)(gi + t16 * 40) = pk4(s0);
        STAMP(48);
        BAR();
        STAMP(49);
        f32x16 o[2];
        sfor<0, 2>([&](auto HD) {
          constexpr int hd = HD.v;
          u32x2 a = *(LAS u32x2*)(gr + hd * 1360), b = *(LAS u32x2*)(gr + hd * 1360 + 16), c = *(LAS u32x2*)(gr + hd * 1360 + 680), d = *(LAS u32x2*)(gr + hd * 1360 + 696);
          o[hd] = mfma(asb(u32x4{a.x, a.y, b.x, b.y}), asb(phq[hd][0]), z16);
          o[hd] = mfma(asb(u32x4{c.x, c.y, d.x, d.y}), asb(phq[hd][1]), o[hd]);
        });
        sfor<0, 2>([&](auto HD) {
          constexpr int hd = HD.v;
          const float r = __builtin_amdgcn_rcpf(o[hd][8] + 1e-6f);
          gst16_sc1<hd * 1024>(ob, l16, u32x4{pk2v(pr(o[hd], 0) * r), pk2v(pr(o[hd], 1) * r), pk2v(pr(o[hd], 2) * r), pk2v(pr(o[hd], 3) * r)});
        });
        STAMP(50);
        DRAIN();
        STAMP(4 + 12 * layer);
      }
#define TILE_REDUCE(EXOFF) { LAS char* ex = lds + (EXOFF) + l16; \
        _Pragma("unroll") for (int i = 0; i < 4; ++i) *(LAS f32x4*)(ex + w * 4096 + i * 1024) = f32x4{acc[1][4 * i], acc[1][4 * i + 1], acc[1][4 * i + 2], acc[1][4 * i + 3]}; \
        BAR(); \
        _Pragma("unroll") for (int i = 0; i < 4; ++i) { const f32x4 v = *(LAS f32x4*)(ex + (w ^ 2) * 4096 + i * 1024) + f32x4{acc[0][4 * i], acc[0][4 * i + 1], acc[0][4 * i + 2], acc[0][4 * i + 3]}; \
          resid[i >> 1][2 * (i & 1)] = pk2(v[0], v[1]); resid[i >> 1][2 * (i & 1) + 1] = pk2(v[2], v[3]); } }
#define TILE_PUBLISH(xo, so) { gst16_sc1<0>(xo, l16, resid[0]); gst16_sc1<1024>(xo, l16, resid[1]); \
        float sum = 0.f, ssq = 0.f, sum2 = 0.f, ssq2 = 0.f; \
        _Pragma("unroll") for (int e = 0; e < 4; ++e) { sum = dot2(resid[0][e], 0x3f803f80u, sum); ssq = dot2(resid[0][e], resid[0][e], ssq); sum2 = dot2(resid[1][e], 0x3f803f80u, sum2); ssq2 = dot2(resid[1][e], resid[1][e], ssq2); } \
        sum += sum2; ssq += ssq2; red2sum(sum, ssq); sum2 = sum; ssq2 = ssq; swap16(sum, sum2); swap16(ssq, ssq2); \
        if (lane < 16) gst16_sc1<0>(so, l16, u32x4{__float_as_uint(sum), __float_as_uint(ssq), __float_as_uint(sum2), __float_as_uint(ssq2)}); \
        DRAIN(); }
      FRESH();
      {
        const size_t ch = (size_t)(4 * P + 2 * th + cc);
        const char* xb = ws + WS_O + (size_t)layer * (2u << 20) + (ch * 32 + 16 * kh) * 1024;
        char* xo = ws + WS_X1 + (size_t)layer * (2u << 20) + (ch * 32 + 4 * cg + 2 * kh) * 1024;
        char* so = ws + WS_ST1 + (size_t)layer * (256u << 10) + (ch * 16 + 2 * cg + kh) * 256;
        LAS char* wa[2] = {opaque(lds + LD_WA3 + kh * 32768 + (TRB0 ^ (64 * kh))), opaque(lds + LD_WA3 + kh * 32768 + (TRB1 ^ (64 * kh)))};
        asm volatile("" : "+s"(xb), "+s"(xo), "+s"(so));
        HANDOFF(0);
        STAMP(5 + 12 * layer);
        sfor<0, N0>([&](auto I) { LDF(I.v, I.v); });
        STAMP(51);
        gemm<2, 0, 16, 4, 32>(acc, xf, [&](auto KB, auto T) { return trfrag(wa[T.v] + KB.v * 2048, 1024); }, [&](auto I) {
          constexpr int s = I.v;
          if constexpr (s % 2 == 0) waitv<(N0 + s < 16 ? N0 + s : 16) - s / 2 - 1>(xf[s / 2]);
          if constexpr (N0 + s < 16) LDF(N0 + s, N0 + s);
        }, nop);
        STAMP(52);
        TILE_REDUCE(LD_SC)
        STAMP(53);
        TILE_PUBLISH(xo, so)
        STAMP(6 + 12 * layer);
      }
      FRESH();
      {
        const char* xb = ws + WS_X1 + (size_t)layer * (2u << 20) + (size_t)(4 * P + w) * 32768;
        LAS char* wa[2] = {opaque(lds + TRB0), opaque(lds + TRB1)};
        asm volatile("" : "+s"(xb));
        HANDOFF(1);
        STAMP(7 + 12 * layer);
        if (j < 14) {
          u32x4 st[4];
          sfor<0, N0>([&](auto I) { LDF(I.v, I.v); });
          STAMP(28);
          acc[0] = z16; acc[1] = z16; acc[2] = z16; acc[3] = z16;
          gemm<2, 0, 32, 4, 32>(acc, xf, [&](auto KB, auto T) { return trfrag(wa[T.v] + KB.v * 2048, 1024); }, [&](auto I) {
            constexpr int s = I.v;
            if constexpr (s == 36) RAWBAR();
            STREAM32(ws + WS_ST1 + (size_t)layer * (256u << 10))
          }, nop);
          DRAIN();
          STATS()
          LAS char* Sp = lds + LD_S + 768 + lane * 4;
          lnfix(acc[0], Sp, nmean); lnfix(acc[1], Sp + 256, nmean);
          STAMP(25);
          RAWBAR();
          STAMP(26);
          char* hb = ws + WS_H + (size_t)layer * (7u << 20) + ((size_t)(4 * P + w) * 112 + 8 * j) * 1024;
          const float c1 = rstd * -1.4426950408889634f, sigma = __builtin_amdgcn_rcpf(rstd);
          float tt[8]; u32x4 hp;
          auto uop = [&](auto M) {
            constexpr int st_ = M.v % 6, e = M.v / 6 - st_, t = e / 16, r = e % 16;
            if constexpr (e >= 0 && e < 64) {
              if constexpr (st_ == 0) tt[e & 7] = acc[t][r] * c1;
              if constexpr (st_ == 1) tt[e & 7] = __builtin_amdgcn_exp2f(tt[e & 7]);
              if constexpr (st_ == 2) tt[e & 7] = __builtin_fmaf(tt[e & 7], sigma, sigma);
              if constexpr (st_ == 3) tt[e & 7] = __builtin_amdgcn_rcpf(tt[e & 7]);
              if constexpr (st_ == 4) tt[e & 7] = acc[t][r] * tt[e & 7];
              if constexpr (st_ < 5) asm volatile("" : "+v"(tt[e & 7]));
              if constexpr (st_ == 5 && (e & 1)) {
                u32 pkd = pk2(tt[(e - 1) & 7], tt[e & 7]); pin1(pkd); hp[(e >> 1) & 3] = pkd;
                if constexpr ((e & 7) == 7) gst16_sc1<((e >> 3) & 3) * 1024>(hb + (e >> 5) * 4096, l16, hp);
              }
            }
          };
          constexpr int UB = 32 * 6, UC = 48 * 6;
          wa[0] = opaque(lds + 65536 + TRB0); wa[1] = opaque(lds + 65536 + TRB1);
          gemm<1, 0, 32, 4, 32>(acc + 2, xf, [&](auto KB, auto) { return trfrag(wa[0] + KB.v * 2048, 1024); }, nop, [&](auto I) { if constexpr (I.v >= 1) sfor<(I.v - 1) * UB / 31, I.v * UB / 31>(uop); });
          lnfix(acc[2], Sp + 512, nmean);
          STAMP(27);
          gemm<1, 0, 32, 4, 32>(acc + 3, xf, [&](auto KB, auto) { return trfrag(wa[1] + KB.v * 2048, 1024); },
                                [&](auto I) { if constexpr (I.v == 18) { STAMP(34); DRAIN(); RAWBAR();   } },
                                [&](auto I) { constexpr int i = I.v, u0 = i < 6 ? 5 * i : 30 + (i - 6) * (UC - UB - 30) / 26, u1 = i + 1 < 6 ? 5 * (i + 1) : 30 + (i - 5) * (UC - UB - 30) / 26; sfor<UB + u0, UB + u1>(uop); });
          lnfix(acc[3], Sp + 768, nmean);
          STAMP(8 + 12 * layer);
          sfor<UC, 70 * 6>(uop);
          STAMP(35);
          RAWBAR();
          STAMP(55);
        } else { RAWBAR(); RAWBAR(); RAWBAR(); RAWBAR(); }
      }
      FRESH();
      {
        const size_t ch = (size_t)(4 * P + 2 * th + cc);
        const char* xb = ws + WS_H + (size_t)layer * (7u << 20) + (ch * 112 + 56 * kh) * 1024;
        const int t0 = kh * 8192 + (TRB0 ^ (64 * kh)), t1 = t0 ^ 64;
        LAS char* wa[6] = {opaque(lds + t0), opaque(lds + t1), opaque(lds + 65536 + t0), opaque(lds + 65536 + t1), opaque(lds + 131072 + t0), opaque(lds + 131072 + t1)};
        asm volatile("" : "+s"(xb));
        RES2ACC()
        DRAIN();
        STAMP(9 + 12 * layer);
        RAWBAR();
        STAMP(10 + 12 * layer);
#define LDH(i) LDF((i) % 32, (i) < 28 ? 8 * ((i) / 4) + (i) % 4 : 8 * (((i) - 28) / 4) + 4 + (i) % 4)
        constexpr int XB = 44;
        sfor<0, N0>([&](auto I) { LDH(I.v); });
        STAMP(56);
        gemm<2, 0, 56, 4, 32>(acc, xf, [&](auto KB, auto T) { constexpr int so = b2slot(KB.v >> 2); return trfrag(wa[(so >> 16) * 2 + T.v] + (so & 65535) + (KB.v & 3) * 2048, 1024); }, [&](auto I) {
          constexpr int s = I.v, kb = s / 2;
          if constexpr (s == XB) STAMP(57);
          if constexpr (s == 68) STAMP(59);
          if constexpr (s == 100) STAMP(61);
          if constexpr (s == 18 || s == XB || s == 68 || s == 100) RAWBAR();
          if constexpr (s == XB) STAMP(58);
          if constexpr (s == 68) STAMP(60);
          constexpr int iss = s <= XB ? (N0 + s < 28 ? N0 + s : 28) : (36 + (s - XB - 1) < 56 ? 36 + (s - XB - 1) : 56);
          if constexpr (s % 2 == 0) waitv<iss - kb - 1>(xf[kb % 32]);
          if constexpr (s < XB && N0 + s < 28) LDH(N0 + s);
          if constexpr (s == XB) sfor<28, 36>([&](auto J) { LDH(J.v); });
          if constexpr (s > XB && 36 + (s - XB - 1) < 56) LDH(36 + (s - XB - 1));
        }, nop);
        STAMP(11 + 12 * layer);
        TILE_REDUCE(98304)
        if (layer == 0) {
          char* xo = ws + WS_X2 + (ch * 32 + 4 * cg + 2 * kh) * 1024;
          char* so = ws + WS_ST2 + (ch * 16 + 2 * cg + kh) * 256;
          TILE_PUBLISH(xo, so)
          STAMP(12);
          HANDOFF(4);
        } else {
          char* ob = out + (size_t)(128 * P + 64 * th + 32 * cc) * 1024 + 128 * cg + 64 * kh;
          sfor<0, 2>([&](auto S) {
            auto r0 = __builtin_amdgcn_permlane32_swap(resid[S.v].x, resid[S.v].z, false, false);
            auto r1 = __builtin_amdgcn_permlane32_swap(resid[S.v].y, resid[S.v].w, false, false);
            gst16<32 * S.v>(ob, tk * 1024u + 16u * hh, u32x4{r0[0], r1[0], r0[1], r1[1]});
          });
        }
      }
    }
  }
}

extern "C" void kernel_launch(void* const* d_in, const int* in_sizes, int n_in, void* d_out, int out_size, void* d_ws, size_t ws_size, hipStream_t stream) {
  mega<<<256, 512, 0, stream>>>((const char*)d_in[0], (const char*)d_in[1], (const char*)d_in[2], (const char*)d_in[3], (const char*)d_in[5], (const char*)d_in[6], (char*)d_out, (char*)d_ws);
}
```

```cpp
#include <hip/hip_runtime.h>
#include <hip/hip_bf16.h>

typedef unsigned u32;
typedef unsigned short u16;
typedef u32 u32x4 __attribute__((ext_vector_type(4)));
typedef u32 u32x2 __attribute__((ext_vector_type(2)));
typedef float f32x16 __attribute__((ext_vector_type(16)));
typedef float f32x4 __attribute__((ext_vector_type(4)));
typedef float f32x2 __attribute__((ext_vector_type(2)));
typedef short s16x4 __attribute__((ext_vector_type(4)));
typedef __bf16 bf16x2 __attribute__((ext_vector_type(2)));
typedef __bf16 bf16x8 __attribute__((ext_vector_type(8)));
#define LAS __attribute__((address_space(3)))
#define DI __device__ __forceinline__

constexpr size_t WS_CTL = 0;
constexpr size_t WS_ST1 = 512u << 10;
constexpr size_t WS_ST2 = 1024u << 10;
constexpr size_t WS_KVP = 1280u << 10;
constexpr u32 KVP_UNIT = 9216;
constexpr size_t WS_O = 4u << 20;
constexpr size_t WS_X1 = 8u << 20;
constexpr size_t WS_X2 = 12u << 20;
constexpr size_t WS_H = 14u << 20;
constexpr size_t WS_STAMP = 40u << 20;
constexpr u32 C_IN1 = 1024, C_REQ = 1152;

constexpr u32 LD_STG = 96 * 1024;
constexpr u32 LD_WA3 = 64 * 1024;
constexpr u32 LD_SC = 128 * 1024;
constexpr u32 LD_HX2 = 144 * 1024;
constexpr u32 LD_RS = 153 * 1024;
constexpr u32 LD_STAMP = 154 * 1024;
constexpr u32 LD_S = 158 * 1024;
#define TOUCH 1
#define N0V 12
constexpr u32 SPIN_CAP = 1u << 17;

#define BAR() do { asm volatile("s_waitcnt lgkmcnt(0)" ::: "memory"); __builtin_amdgcn_s_barrier(); asm volatile("" ::: "memory"); } while (0)
#define RAWBAR() do { asm volatile("" ::: "memory"); __builtin_amdgcn_s_barrier(); asm volatile("" ::: "memory"); } while (0)
#define FRESH() do { asm volatile("" : "+s"(ws), "+s"(P), "+s"(j), "+s"(w), "+s"(x), "+s"(wqkv), "+s"(wo), "+s"(w1), "+s"(w2)); asm volatile("" : "+v"(lane)); th = j >> 3; cg = j & 7; cc = w & 1; kh = (w >> 1) & 1; \
  hh = lane >> 5; tk = lane & 31; g = lane >> 4; t16 = lane & 15; q = (lane >> 2) & 3; p = lane & 3; l16 = lane * 16u; } while (0)
#define TRB0 ((4 * hh + q) * 128 + 64 * (q >> 1) + 32 * (g & 1) + 8 * p)
#define TRB1 (TRB0 ^ 64)
#define TRA ((4 * hh + q) * 64 + 32 * (g & 1) + 8 * p)
#define HEL_OWN ((u32)(P * 16 + j) * 2048u + 4u * tk)
#define HEL_PEER (tk < 16 ? (u32)(P * 16 + tk) * 2048u + 4u * j : (u32)((tk - 16) * 16 + j) * 2048u + 64u + 4u * P)
#define PCNT(l, n) ((u32)P * 32768u + (l) * 2048u + (n) * 128u)
#define KCNT(l) ((u32)((1 + (l)) * 16 + j) * 2048u + 896u)
#define DRAIN() asm volatile("s_waitcnt vmcnt(0)" ::: "memory")

#define STAMP(i) do {} while (0)

template <int N> DI void waitv(u32x4& a) { asm volatile("s_waitcnt vmcnt(%1)" : "+v"(a) : "i"(N)); }
template <int N> DI void waitv4(u32x4& a, u32x4& b, u32x4& c, u32x4& d) { asm volatile("s_waitcnt vmcnt(%4)" : "+v"(a), "+v"(b), "+v"(c), "+v"(d) : "i"(N)); }
DI void waitl8(u32x4* f) { asm volatile("" : "+v"(f[0]), "+v"(f[1]), "+v"(f[2]), "+v"(f[3]), "+v"(f[4]), "+v"(f[5]), "+v"(f[6]), "+v"(f[7])::"memory"); }
DI void pin(u32x4& a) { asm volatile("" : "+v"(a)); }
DI void pin2(u32x2& a) { asm volatile("" : "+v"(a)); }
DI void pin1(u32& a) { asm volatile("" : "+v"(a)); }

template <int OFF> DI void gld16_sc1(u32x4& d, const char* sb, u32 vo) { asm volatile("global_load_dwordx4 %0, %1, %2 offset:%3 sc1" : "=v"(d) : "v"(vo), "s"(sb), "i"(OFF) : "memory"); }
template <int OFF> DI void gld16(u32x4& d, const char* sb, u32 vo) { asm volatile("global_load_dwordx4 %0, %1, %2 offset:%3" : "=v"(d) : "v"(vo), "s"(sb), "i"(OFF) : "memory"); }
template <int OFF> DI void gld8(u32x2& d, const char* sb, u32 vo) { asm volatile("global_load_dwordx2 %0, %1, %2 offset:%3" : "=v"(d) : "v"(vo), "s"(sb), "i"(OFF) : "memory"); }
DI void gld4_sc1(u32& d, const char* sb, u32 vo) { asm volatile("global_load_dword %0, %1, %2 sc1" : "=v"(d) : "v"(vo), "s"(sb) : "memory"); }
template <int OFF> DI void gst16_sc1(char* sb, u32 vo, u32x4 d) { asm volatile("global_store_dwordx4 %0, %1, %2 offset:%3 sc1\n\ts_nop 1" ::"v"(vo), "v"(d), "s"(sb), "i"(OFF) : "memory"); }
DI void gst8_sc1(char* sb, u32 vo, u32x2 d) { asm volatile("global_store_dwordx2 %0, %1, %2 sc1" ::"v"(vo), "v"(d), "s"(sb) : "memory"); }
template <int OFF> DI void gst8(char* sb, u32 vo, u32x2 d) { asm volatile("global_store_dwordx2 %0, %1, %2 offset:%3" ::"v"(vo), "v"(d), "s"(sb), "i"(OFF) : "memory"); }
DI void gadd1(char* sb, u32 vo) { asm volatile("global_atomic_add %0, %1, %2 sc1" ::"v"(vo), "v"(1u), "s"(sb) : "memory"); }
template <int OFF> DI void gst16(char* sb, u32 vo, u32x4 d) { asm volatile("global_store_dwordx4 %0, %1, %2 offset:%3\n\ts_nop 1" ::"v"(vo), "v"(d), "s"(sb), "i"(OFF) : "memory"); }
DI void gst4_sc1(char* sb, u32 vo, u32 d) { asm volatile("global_store_dword %0, %1, %2 sc1" ::"v"(vo), "v"(d), "s"(sb) : "memory"); }
DI void glds(u32 ldsaddr, const char* sb, u32 vo) { asm volatile("s_mov_b32 m0, %0\n\ts_nop 0\n\tglobal_load_lds_dwordx4 %1, %2" ::"s"(ldsaddr), "v"(vo), "s"(sb) : "memory"); }

DI bf16x8 trfrag(LAS char* p, int second) {
  s16x4 a = __builtin_amdgcn_ds_read_tr16_b64_v4i16((LAS s16x4*)p);
  s16x4 b = __builtin_amdgcn_ds_read_tr16_b64_v4i16((LAS s16x4*)(p + second));
  return __builtin_bit_cast(bf16x8, __builtin_shufflevector(a, b, 0, 1, 2, 3, 4, 5, 6, 7));
}
DI bf16x8 asb(u32x4 v) { return __builtin_bit_cast(bf16x8, v); }
DI f32x4 mfma16(bf16x8 a, bf16x8 b, f32x4 c) { return __builtin_amdgcn_mfma_f32_16x16x32_bf16(a, b, c, 0, 0, 0); }
DI f32x16 mfma(bf16x8 a, bf16x8 b, f32x16 c) { return __builtin_amdgcn_mfma_f32_32x32x16_bf16(a, b, c, 0, 0, 0); }
DI u32 pk2(float a, float b) { f32x2 v = {a, b}; return __builtin_bit_cast(u32, __builtin_convertvector(v, bf16x2)); }
DI u32 pk2v(f32x2 v) { return __builtin_bit_cast(u32, __builtin_convertvector(v, bf16x2)); }
DI u32x2 pk4(f32x4 v) { u32x2 r; r.x = pk2(v[0], v[1]); r.y = pk2(v[2], v[3]); return r; }
DI float bflo(u32 u) { return __uint_as_float(u << 16); }
DI float bfhi(u32 u) { return __uint_as_float(u & 0xffff0000u); }
DI float dot2(u32 a, u32 b, float c) { return __builtin_amdgcn_fdot2_f32_bf16(__builtin_bit_cast(bf16x2, a), __builtin_bit_cast(bf16x2, b), c, false); }
DI void swap32(float& a, float& b) { auto r = __builtin_amdgcn_permlane32_swap(__float_as_uint(a), __float_as_uint(b), false, false); a = __uint_as_float(r[0]); b = __uint_as_float(r[1]); }
DI void swap16(float& a, float& b) { auto r = __builtin_amdgcn_permlane16_swap(__float_as_uint(a), __float_as_uint(b), false, false); a = __uint_as_float(r[0]); b = __uint_as_float(r[1]); }
DI float mx2(float a, float b) { return __builtin_amdgcn_fmed3f(a, b, __builtin_inff()); }
DI float mx3(float a, float b, float c) { return __builtin_fmaxf(__builtin_fmaxf(a, b), c); }
DI void red2max(float& a, float& b) { swap32(a, b); float u = mx2(a, b); a = u; b = u; swap32(a, b); }
DI void red2sum(float& a, float& b) { swap32(a, b); float u = a + b; a = u; b = u; swap32(a, b); }
template <int CTRL> DI float dppadd(float v) { return v + __uint_as_float(__builtin_amdgcn_update_dpp(0u, __float_as_uint(v), CTRL, 0xf, 0xf, true)); }
DI float rowsum16(float v) { v = dppadd<0xB1>(v); v = dppadd<0x4E>(v); v = dppadd<0x141>(v); v = dppadd<0x140>(v); return v; }
DI f32x2 pr(const f32x16& a, int i) { return f32x2{a[2 * i], a[2 * i + 1]}; }

DI void softmax2(const f32x16& p, const float L2E  , u32x4 (&f)[2][2]) {
  float mx[2], mn[2];
#pragma unroll
  for (int hd = 0; hd < 2; ++hd) {
    const int b = 8 * hd;
    mx[hd] = mx2(mx3(mx3(mx3(p[b], p[b + 1], p[b + 2]), p[b + 3], p[b + 4]), p[b + 5], p[b + 6]), p[b + 7]);
    mn[hd] = mx2(mx3(mx3(mx3(-p[b], -p[b + 1], -p[b + 2]), -p[b + 3], -p[b + 4]), -p[b + 5], -p[b + 6]), -p[b + 7]);
    red2max(mx[hd], mn[hd]);
  }
  f32x2 e[2][2][4]; float s[2][2];
#pragma unroll
  for (int hd = 0; hd < 2; ++hd) {
    const float cp = -mx[hd] * L2E, cn = -mn[hd] * L2E;
#pragma unroll
    for (int i = 0; i < 4; ++i) {
      const f32x2 v = pr(p, 4 * hd + i), a = v * L2E + cp, b = v * -L2E + cn;
      e[hd][0][i] = f32x2{__builtin_amdgcn_exp2f(a.x), __builtin_amdgcn_exp2f(a.y)};
      e[hd][1][i] = f32x2{__builtin_amdgcn_exp2f(b.x), __builtin_amdgcn_exp2f(b.y)};
    }
#pragma unroll
    for (int sg = 0; sg < 2; ++sg) { const f32x2 t = (e[hd][sg][0] + e[hd][sg][1]) + (e[hd][sg][2] + e[hd][sg][3]); s[hd][sg] = t.x + t.y; }
    red2sum(s[hd][0], s[hd][1]);
#pragma unroll
    for (int sg = 0; sg < 2; ++sg) {
      const float r = __builtin_amdgcn_rcpf(s[hd][sg]);
#pragma unroll
      for (int i = 0; i < 4; ++i) f[hd][sg][i] = pk2v(e[hd][sg][i] * r);
    }
  }
}
DI void pack16(const f32x16& a, u32x4 (&y)[2]) {
#pragma unroll
  for (int i = 0; i < 8; ++i) y[i >> 2][i & 3] = pk2v(pr(a, i));
}
DI void lnfix(f32x16& a, LAS char* Sp, u32 negmean) { a = mfma(asb(u32x4{*(LAS u32*)Sp, 0u, 0u, 0u}), asb(u32x4{negmean, 0u, 0u, 0u}), a); }
DI u32 split2(float v) { const u32 hi = pk2(v, v) & 0xffffu; return hi | (pk2(v - bflo(hi), 0.f) << 16); }

template <int V> struct IC { static constexpr int v = V; };
template <int B, int E, class F> DI void sfor(F&& f) { if constexpr (B < E) { f(IC<B>{}); sfor<B + 1, E>(f); } }
#define SB() __builtin_amdgcn_sched_barrier(0)
template <int NT, int K0, int K1, int D, int XM, class LD, class PRE, class POST> DI void gemm(f32x16* acc, u32x4* xf, LD&& ld, PRE&& pre, POST&& post) {
  bf16x8 wf[D + 1];
  constexpr int S0 = K0 * NT, S1 = K1 * NT;
  sfor<S0, S0 + D>([&](auto I) { constexpr int s = I.v; wf[s % (D + 1)] = ld(IC<s / NT>{}, IC<s % NT>{}); });
  SB();
  sfor<S0, S1>([&](auto I) {
    constexpr int s = I.v;
    pre(I);
    if constexpr (s + D < S1) wf[(s + D) % (D + 1)] = ld(IC<(s + D) / NT>{}, IC<(s + D) % NT>{});
    SB();
    post(I);
    acc[s % NT] = mfma(wf[s % (D + 1)], asb(xf[(s / NT) % XM]), acc[s % NT]);
    asm volatile("" : "+v"(acc[s % NT]));
    SB();
  });
}
constexpr int b2slot(int t) { return t < 4 ? t * 16384 : t == 4 || t == 13 ? 131072 : t < 9 ? 65536 + (t - 5) * 16384 : (t - 9) * 16384; }
DI LAS char* opaque(LAS char* p) { asm volatile("" : "+v"(p)); return p; }

DI void cnt_wait(const char* ctl, u32 off, u32 val) {
  for (u32 sp = 0; sp < SPIN_CAP; ++sp) {
    u32 v; gld4_sc1(v, ctl, off); asm volatile("s_waitcnt vmcnt(0)" : "+v"(v)::"memory");
    if (__builtin_amdgcn_readfirstlane(v) >= val) break;
  }
}
#define HANDOFF(n) do { RAWBAR(); if (w == 0) { if (lane == 0) gadd1(ws, PCNT(layer, n)); cnt_wait(ws, PCNT(layer, n), 16u); } RAWBAR(); } while (0)

__global__ __launch_bounds__(512, 2) void mega(const char* x, const char* wqkv, const char* wphi, const char* wo, const char* w1, const char* w2, char* out, char* ws) {
  __shared__ __attribute__((aligned(16))) char smem[163840];
  LAS char* lds = (LAS char*)smem;
  const u32 lb = (u32)(size_t)lds;
  int lane = threadIdx.x & 63; int w = __builtin_amdgcn_readfirstlane(threadIdx.x >> 6);
  int hh = lane >> 5, tk = lane & 31, g = lane >> 4, t16 = lane & 15, q = (lane >> 2) & 3, p = lane & 3;
  const int bid = blockIdx.x, loc = bid >> 3;
  int P = (bid & 7) * 2 + (loc >> 4), j = loc & 15;
  int th = j >> 3, cg = j & 7, cc = w & 1, kh = (w >> 1) & 1;
  u32 l16 = lane * 16u;
  const u32x4 ones4 = {0x3f803f80u, 0x3f803f80u, 0x3f803f80u, 0x3f803f80u};
  const f32x16 z16 = {0.f, 0.f, 0.f, 0.f, 0.f, 0.f, 0.f, 0.f, 0.f, 0.f, 0.f, 0.f, 0.f, 0.f, 0.f, 0.f};
  STAMP(0);
  if (w >= 4) {
    const u32 swz = 16u * ((lane & 7) ^ ((g & 1) << 2));
    const u32 vo1024 = (lane >> 3) * 1024u + swz, vo3584 = (lane >> 3) * 3584u + swz, voA1 = (lane >> 2) * 3072u + 16u * (lane & 3);
    u32x4 wa1[16];
    u32x4 wb2[36];
    LAS char* wl = lds + l16;
#define HW (w & 3)
#define GLDS_Q(n) glds(lb + 65536 + (n) * 1024, wqkv + 64 * j + (size_t)((n) * 16) * 3072, voA1)
#define GLDS_B1(n) glds(lb + (n) * 1024, w1 + (size_t)(((n) & 63) * 8) * 3584 + j * 256 + ((n) >> 6) * 128, vo3584)
#define B2SRC(n) (w2 + cg * 128 + (size_t)(896 * (((n) >> 3) & 1) + (((n) >> 4) < 7 ? 128 * ((n) >> 4) : 128 * ((n) >> 4) - 832) + 8 * ((n) & 7)) * 1024)
#define GLDS_B2(n) glds(lb + ((n) < 64 ? (n) : (n) + 64) * 1024, B2SRC(n), vo1024)
#define GLDS_A3(n) glds(lb + LD_WA3 + (n) * 1024, wo + (size_t)(n) * 8192 + cg * 128, vo1024)
    auto putA1 = [&](auto R, auto M) { *(LAS u32x4*)(wl + R.v * 32768 + (4 * M.v + HW) * 1024) = wa1[R.v * 8 + M.v]; };
    auto putB2 = [&](auto T) { sfor<0, 4>([&](auto I) { *(LAS u32x4*)(wl + b2slot(T.v) + (4 * HW + I.v) * 1024) = wb2[(T.v - 5) * 4 + I.v]; }); };
    auto ldB2 = [&](auto T) { sfor<0, 4>([&](auto I) { gld16<0>(wb2[(T.v - 5) * 4 + I.v], B2SRC(T.v * 16 + 4 * HW + I.v), vo1024); }); };
    auto ldB1b = [&](u32x4* r) { const char* b = w1 + (size_t)(HW * 128) * 3584 + (j < 14 ? j : 13) * 256 + 128; sfor<0, 16>([&](auto I) { gld16<0>(r[I.v], b + (size_t)(I.v * 8) * 3584, vo3584); }); };
    auto putB1b = [&](u32x4* r) { sfor<0, 16>([&](auto I) { pin(r[I.v]); *(LAS u32x4*)(wl + 65536 + (HW * 16 + I.v) * 1024) = r[I.v]; }); };
    auto colsum = [&](LAS char* sp, int kstride, int second, auto K0, auto K1, f32x16& a) {
      bf16x8 wf[3];
      wf[K0.v % 3] = trfrag(sp + K0.v * kstride, second); wf[(K0.v + 1) % 3] = trfrag(sp + (K0.v + 1) * kstride, second);
      sfor<K0.v, K1.v>([&](auto K) {
        if constexpr (K.v + 2 < K1.v) wf[(K.v + 2) % 3] = trfrag(sp + (K.v + 2) * kstride, second);
        a = mfma(asb(ones4), wf[K.v % 3], a); asm volatile("" : "+v"(a)); SB();
      });
    };
    auto putS = [&](int tile, float a) { const u32 sp = split2(a); *(LAS u32*)(lds + LD_S + tile * 256 + lane * 4) = hh ? (sp >> 16) * 0x10001u : (sp & 0xffffu) * 0x10001u; };
    {
      u32 a0 = 0, sent = 0;
      if (HW == 3) {
        gld4_sc1(a0, ws, HEL_OWN + C_IN1); gst16_sc1<0>(ws + (size_t)(P * 16 + j) * 2048, l16, u32x4{0u, 0u, 0u, 0u});
        asm volatile("s_waitcnt vmcnt(0)" : "+v"(a0)::"memory");
        if (lane < 32) gst4_sc1(ws, HEL_PEER + C_REQ, a0);
      }
      const char* wb = wqkv + 64 * j + (size_t)(16 * HW) * 3072;
      RAWBAR();
      sfor<0, 8>([&](auto M) { gld16<1024>(wa1[M.v], wb + (size_t)(64 * M.v) * 3072, voA1); gld16<2048>(wa1[8 + M.v], wb + (size_t)(64 * M.v) * 3072, voA1); });
      sfor<0, 4>([&](auto C) {
        constexpr int c = C.v;
        waitv4<12 - 4 * c>(wa1[2 * c], wa1[2 * c + 1], wa1[8 + 2 * c], wa1[8 + 2 * c + 1]);
        putA1(IC<0>{}, IC<2 * c>{}); putA1(IC<0>{}, IC<2 * c + 1>{}); putA1(IC<1>{}, IC<2 * c>{}); putA1(IC<1>{}, IC<2 * c + 1>{});
      });
      {
        asm volatile("s_waitcnt lgkmcnt(0)" ::: "memory");
        f32x16 a0_ = z16, a1_ = z16;
        colsum(lds + HW * 1024 + TRA, 4096, 512, IC<0>{}, IC<8>{}, a0_); colsum(lds + 32768 + HW * 1024 + TRA, 4096, 512, IC<0>{}, IC<8>{}, a1_);
        *(LAS float*)(lds + LD_RS + HW * 256 + lane * 4) = hh ? a1_[0] : a0_[0];
      }
      STAMP(33);
      BAR();
      if (HW == 3) {
        gld4_sc1(sent, ws, HEL_OWN + C_REQ); asm volatile("s_waitcnt vmcnt(0)" : "+v"(sent)::"memory");
        if (lane < 32) gst4_sc1(ws, HEL_PEER + C_IN1, sent + 1u);
      }
      u32 tch[4];
      {
        const u32 l4k = lane * 4096u;
        const int ly = HW & 1;
        auto touch = [&](u32& d, const char* b, u32 vo) { asm volatile("global_load_dword %0, %1, %2" : "=v"(d) : "v"(vo), "s"(b) : "memory"); };
        if (HW < 2) {
          touch(tch[0], ws + WS_H + (size_t)ly * (7u << 20) + (size_t)P * 458752, l4k);
          touch(tch[1], ws + WS_H + (size_t)ly * (7u << 20) + (size_t)P * 458752 + 196608, l4k);
          touch(tch[2], ws + WS_O + (size_t)ly * (2u << 20) + (size_t)P * 131072, (lane < 32 ? 0u : (4u << 20) - 131072u) + l4k);
          touch(tch[3], ws + WS_KVP + (size_t)(ly * 16 + j) * 4 * KVP_UNIT, (lane & 7) * 4096u);
        } else {
          touch(tch[0], ly ? out + (size_t)P * 131072 : ws + WS_X2 + (size_t)P * 131072, tk * 4096u);
          touch(tch[1], ws + WS_ST1 + (size_t)P * 16384, (lane & 3) * 4096u + (lane >> 2 < 3 ? (lane >> 2) : 0) * (256u << 10));
          touch(tch[2], w2 + (size_t)ly * 917504, lane * 14336u);
          touch(tch[3], ly ? wo : w1, ly ? lane * 8192u : lane * 28672u);
        }
      }
      _Pragma("unroll 4") for (int i = 0; i < 8; ++i) GLDS_Q(HW * 8 + i);
      ldB1b(wb2 + 20);
      if (HW < 2) { LAS float* pp = (LAS float*)(lds + LD_RS + HW * 128 + tk * 4); putS(HW, (pp[0] + pp[64]) + (pp[128] + pp[192])); }
      STAMP(39);
      BAR();
      if (HW == 3) {
        for (u32 sp_ = 0; sp_ < SPIN_CAP; ++sp_) {
          u32 v, r; gld4_sc1(v, ws, HEL_OWN + C_IN1); gld4_sc1(r, ws, HEL_OWN + C_REQ);
          asm volatile("s_waitcnt vmcnt(0)" : "+v"(v), "+v"(r)::"memory");
          if (r != sent) { sent = r; if (lane < 32) gst4_sc1(ws, HEL_PEER + C_IN1, r + 1u); }
          if (__all(v != a0)) break;
        }
        u32 r; gld4_sc1(r, ws, HEL_OWN + C_REQ); asm volatile("s_waitcnt vmcnt(0)" : "+v"(r)::"memory");
        if (r != sent && lane < 32) gst4_sc1(ws, HEL_PEER + C_IN1, r + 1u);
      }
      DRAIN();
      asm volatile("" ::"v"(tch[0]), "v"(tch[1]), "v"(tch[2]), "v"(tch[3]));
      { f32x16 aS = z16; colsum(lds + 65536 + HW * 8192 + TRA, 1024, 512, IC<0>{}, IC<8>{}, aS); *(LAS float*)(lds + LD_RS + HW * 256 + lane * 4) = aS[0]; }
      BAR();
      if (HW == 2) { LAS float* pp = (LAS float*)(lds + LD_RS + tk * 4); putS(2, (pp[0] + pp[64]) + (pp[128] + pp[192])); }
      BAR();
    }
    sfor<0, 2>([&](auto L) {
      constexpr int layer = L.v;
      FRESH();
      if constexpr (layer == 1) {
        RAWBAR();
        _Pragma("unroll 4") for (int i = 0; i < 8; ++i) GLDS_Q(HW * 8 + i);
        ldB1b(wa1);
        asm volatile("s_waitcnt vmcnt(16)" ::: "memory");
        STAMP(42);
        RAWBAR();
        RAWBAR();
      }
      _Pragma("unroll 4") for (int i = 0; i < 8; ++i) GLDS_A3(32 + HW * 8 + i);
      if (j < 14) { _Pragma("unroll 4") for (int i = 0; i < 8; ++i) GLDS_B1(HW * 16 + i); }
      STAMP(36);
      RAWBAR();
      _Pragma("unroll 4") for (int i = 0; i < 8; ++i) GLDS_A3(HW * 8 + i);
      RAWBAR();
      RAWBAR();
      DRAIN();
      RAWBAR(); RAWBAR();
      if (j < 14) { _Pragma("unroll 4") for (int i = 8; i < 16; ++i) GLDS_B1(HW * 16 + i); }
      RAWBAR();
      putB1b(layer == 0 ? wb2 + 20 : wa1);
      DRAIN();
      RAWBAR(); BAR();
      FRESH();
      if (layer == 0 && j < 14) { f32x16 aS = z16; colsum(lds + (HW >> 1) * 65536 + ((HW & 1) ? TRB1 : TRB0), 2048, 1024, IC<0>{}, IC<32>{}, aS); putS(3 + HW, aS[0]); }
      BAR();
      if constexpr (layer == 0) { ldB2(IC<5>{}); ldB2(IC<6>{}); ldB2(IC<7>{}); ldB2(IC<8>{}); ldB2(IC<9>{}); }
      STAMP(25);
      RAWBAR();
      _Pragma("unroll 4") for (int i = 0; i < 20; ++i) GLDS_B2(HW * 20 + i);
      STAMP(34);
      RAWBAR();
      if (HW == 0) { if (lane == 0) gadd1(ws, PCNT(layer, 2)); cnt_wait(ws, PCNT(layer, 2), 16u); }
      STAMP(35);
      RAWBAR();
      FRESH();
      DRAIN();
      sfor<0, 16>([&](auto I) { pin(wb2[I.v]); });
      putB2(IC<5>{}); putB2(IC<6>{});
      STAMP(38);
      RAWBAR();
      if (HW == 0 && lane == 0) gadd1(ws, PCNT(layer, 3));
      putB2(IC<7>{}); putB2(IC<8>{});
      BAR();
      if constexpr (layer == 0) { ldB2(IC<10>{}); ldB2(IC<11>{}); ldB2(IC<12>{}); ldB2(IC<13>{}); }
      if (HW == 0) cnt_wait(ws, PCNT(layer, 3), 16u);
      else if (layer == 1) { putB2(IC<9>{}); putB2(IC<10>{}); }
      STAMP(57);
      RAWBAR();
      DRAIN();
      sfor<16, 36>([&](auto I) { pin(wb2[I.v]); });
      if (layer == 0 || HW == 0) { putB2(IC<9>{}); putB2(IC<10>{}); }
      putB2(IC<11>{}); putB2(IC<12>{});
      STAMP(59);
      BAR();
      putB2(IC<13>{});
      BAR();
      if constexpr (layer == 0) sfor<0, 8>([&](auto M) { putA1(IC<0>{}, M); });
      RAWBAR();
      if constexpr (layer == 0) {
        sfor<0, 8>([&](auto M) { putA1(IC<1>{}, M); });
        RAWBAR(); BAR();
      }
    });
  } else {
    u32x4 resid[2];
    u32x4 wph[2];
    u32x4 xf[32];
    f32x16 acc[4];
    float rstd; u32 nmean;
    auto nop = [&](auto) {};
#define LDF(i, n) gld16_sc1<((n) & 3) * 1024>(xf[i], xb + ((n) >> 2) * 4096, l16)
#define FINSTATS(sum, ssq) { red2sum(sum, ssq); const float mean = sum * (1.f / 512.f); rstd = __builtin_amdgcn_rsqf(mx2(ssq * (1.f / 512.f) - mean * mean, 0.f) + 1e-5f); nmean = split2(-mean); }
#define STATS() { _Pragma("unroll") for (int i = 0; i < 4; ++i) pin(st[i]); \
      const f32x4 v = (__builtin_bit_cast(f32x4, st[0]) + __builtin_bit_cast(f32x4, st[1])) + (__builtin_bit_cast(f32x4, st[2]) + __builtin_bit_cast(f32x4, st[3])); \
      float a = v[0], b = v[1], c = v[2], d = v[3]; swap16(a, c); swap16(b, d); float sum = a + c, ssq = b + d; FINSTATS(sum, ssq) }
#define LDSTATS(SB_) { const char* sb = (SB_) + (size_t)(4 * P + w) * 4096; \
      gld16_sc1<0>(st[0], sb, l16); gld16_sc1<1024>(st[1], sb, l16); gld16_sc1<2048>(st[2], sb, l16); gld16_sc1<3072>(st[3], sb, l16); }
    constexpr int N0 = N0V;
#define STREAM32(NT, SB_) { constexpr int kb_ = s / NT; if constexpr (s % NT == 0) waitv<(N0 + kb_ < 36 ? N0 + kb_ : 36) - kb_ - 1>(xf[kb_]); \
      if constexpr (s % NT == 1 && N0 + kb_ < 32) LDF(N0 + kb_, N0 + kb_); \
      if constexpr (s % NT == 1 && N0 + kb_ >= 32 && N0 + kb_ < 36) gld16_sc1<(N0 + kb_ - 32) * 1024>(st[(N0 + kb_) & 3], (SB_) + (size_t)(4 * P + w) * 4096, l16); }
#define RES2ACC() { _Pragma("unroll") for (int i = 0; i < 8; ++i) { const u32 r = resid[i >> 2][i & 3]; acc[0][2 * i] = bflo(r); acc[0][2 * i + 1] = bfhi(r); } acc[1] = z16; asm volatile("" : "+v"(acc[0])); }
    for (int layer = 0; layer < 2; ++layer) {
      FRESH();
      acc[0] = z16; acc[1] = z16; acc[2] = z16;
      LAS char* wa1p = opaque(lds + TRA);
      auto ldkv = [&](auto KB, auto T) { return trfrag(wa1p + T.v * 32768 + KB.v * 1024, 512); };
      if (layer == 0) {
        const char* xb = x + (size_t)(128 * P + 32 * w) * 1024;
        u32 xvo[4];
#pragma unroll
        for (int i = 0; i < 4; ++i) xvo[i] = g * 1024u + ((t16 ^ (4 * i + g)) * 16u);
        const u32 stg = lb + 65536 + w * 16384;
        auto ldx = [&](auto R) { sfor<0, 8>([&](auto I) { glds(stg + (R.v & 1) * 8192 + I.v * 1024, xb + R.v * 256 + I.v * 4096, xvo[I.v & 3]); }); };
        ldx(IC<0>{}); ldx(IC<1>{});
        u32 wp[8];
        STAMP(32);
        const u32 str = 65536 + w * 16384 + tk * 256 + t16 * 16 + 8 * hh;
        float sm[4] = {0.f, 0.f, 0.f, 0.f}, sq[4] = {0.f, 0.f, 0.f, 0.f};
        sfor<0, 4>([&](auto R) {
          constexpr int b = 8 * R.v;
          asm volatile("s_waitcnt vmcnt(%0)" ::"i"(8) : "memory");
#pragma unroll
          for (int i = 0; i < 8; ++i) {
            u32x2 lo = *(LAS u32x2*)(lds + ((str ^ (32 * i)) + (R.v & 1) * 8192)), hi = *(LAS u32x2*)(lds + ((str ^ (32 * i + 16)) + (R.v & 1) * 8192));
            xf[b + i] = u32x4{lo.x, lo.y, hi.x, hi.y};
          }
          if ((cg >> 1) == R.v && (w >> 1) == th) {
            LAS char* rx = lds + LD_SC + cc * 4096 + l16;
            if (cg & 1) { _Pragma("unroll") for (int i = 0; i < 4; ++i) *(LAS u32x4*)(rx + i * 1024) = xf[b + 4 + i]; }
            else { _Pragma("unroll") for (int i = 0; i < 4; ++i) *(LAS u32x4*)(rx + i * 1024) = xf[b + i]; }
          }
          if constexpr (R.v < 2) { waitl8(xf + b); ldx(IC<R.v + 2>{}); }
          if constexpr (R.v == 0) RAWBAR();
          if constexpr (R.v == 2) {
            const u16* wsrc = (const u16*)wphi + (2 * j + (tk >> 4)) * 256 + (4 * hh) * 16 + t16;
#pragma unroll
            for (int i = 0; i < 8; ++i) asm volatile("global_load_ushort %0, %1, off offset:%2" : "=v"(wp[i]) : "v"(wsrc), "i"((i >> 2) * 256 + (i & 3) * 32) : "memory");
          }
          if constexpr (R.v >= 1) {
            sfor<(R.v == 1 ? 0 : b), b + 8>([&](auto I) {
#pragma unroll
              for (int e = 0; e < 4; ++e) { sm[e] = dot2(xf[I.v][e], 0x3f803f80u, sm[e]); sq[e] = dot2(xf[I.v][e], xf[I.v][e], sq[e]); }
            });
            asm volatile("" : "+v"(sm[0]), "+v"(sm[1]), "+v"(sm[2]), "+v"(sm[3]), "+v"(sq[0]), "+v"(sq[1]), "+v"(sq[2]), "+v"(sq[3]));
          }
          STAMP(29 + (R.v == 3 ? 2 : R.v));
        });
        float sum = (sm[0] + sm[1]) + (sm[2] + sm[3]), ssq = (sq[0] + sq[1]) + (sq[2] + sq[3]);
        FINSTATS(sum, ssq)
        STAMP(33);
        BAR();
        STAMP(54);
        resid[0] = *(LAS u32x4*)(lds + LD_SC + cc * 4096 + kh * 2048 + l16); resid[1] = *(LAS u32x4*)(lds + LD_SC + cc * 4096 + kh * 2048 + 1024 + l16);
        gemm<2, 0, 32, 4, 32>(acc, xf, ldkv, nop, nop);
        STAMP(39);
        DRAIN();
#pragma unroll
        for (int i = 0; i < 8; ++i) pin1(wp[i]);
        const u32x4 wv = {wp[0] | (wp[1] << 16), wp[2] | (wp[3] << 16), wp[4] | (wp[5] << 16), wp[6] | (wp[7] << 16)};
        const u32x4 zz = {0u, 0u, 0u, 0u};
        wph[0] = tk < 16 ? wv : zz; wph[1] = tk < 16 ? zz : wv;
        pin(resid[0]); pin(resid[1]);
        RAWBAR();
      } else {
        STAMP(40);
        u32x4 st[4];
        const char* xb = ws + WS_X2 + (size_t)(4 * P + w) * 32768;
        sfor<0, N0>([&](auto I) { LDF(I.v, I.v); });
        STAMP(41);
        gemm<2, 0, 32, 4, 32>(acc, xf, ldkv, [&](auto I) {
          constexpr int s = I.v;
          if constexpr (s == 2 * (36 - N0)) { STAMP(44); RAWBAR(); STAMP(45); }
          STREAM32(2, ws + WS_ST2)
        }, nop);
        DRAIN();
        STATS()
      }
      STAMP(1 + 12 * layer);
      FRESH();
      {
        u32x4 yk[2], yv[2], f[2][2];
        lnfix(acc[0], lds + LD_S + lane * 4, nmean); lnfix(acc[1], lds + LD_S + 256 + lane * 4, nmean);
        pack16(acc[0], yk);
        f32x16 pp = mfma(asb(wph[0]), asb(yk[0]), z16); pp = mfma(asb(wph[1]), asb(yk[1]), pp);
        pack16(acc[1] * rstd, yv);
        LAS char* kt = lds + LD_SC + (32 * w + tk) * 200 + 8 * hh;
#pragma unroll
        for (int hd = 0; hd < 2; ++hd) { *(LAS u32x2*)(kt + hd * 96 + 64) = u32x2{yv[hd].x, yv[hd].y}; *(LAS u32x2*)(kt + hd * 96 + 80) = u32x2{yv[hd].z, yv[hd].w}; }
        softmax2(pp, rstd * 1.4426950408889634f, f);
#pragma unroll
        for (int hd = 0; hd < 2; ++hd) {
          *(LAS u32x2*)(kt + hd * 96) = u32x2{f[hd][0].x, f[hd][0].y}; *(LAS u32x2*)(kt + hd * 96 + 16) = u32x2{f[hd][0].z, f[hd][0].w};
          *(LAS u32x2*)(kt + hd * 96 + 32) = u32x2{f[hd][1].x, f[hd][1].y}; *(LAS u32x2*)(kt + hd * 96 + 48) = u32x2{f[hd][1].z, f[hd][1].w};
        }
      }
      {
        LAS char* ka = lds + LD_SC + (4 * g + q) * 200 + 8 * p + (w >> 1) * 96;
        char* kb_ = ws + WS_KVP + (size_t)((layer * 16 + j) * 4 + w) * KVP_UNIT;
        asm volatile("" : "+v"(ka)); asm volatile("" : "+s"(kb_));
        STAMP(42);
        BAR();
        STAMP(43);
        f32x4 akv = {0.f, 0.f, 0.f, 0.f}, az = {0.f, 0.f, 0.f, 0.f};
        bf16x8 A[4], B[4];
#pragma unroll
        for (int ks = 0; ks < 4; ++ks) { A[ks] = trfrag(ka + ks * 6400 + (w & 1) * 32, 3200); B[ks] = trfrag(ka + ks * 6400 + 64, 3200); }
        SB();
#pragma unroll
        for (int ks = 0; ks < 4; ++ks) { akv = mfma16(A[ks], B[ks], akv); az = mfma16(A[ks], asb(ones4), az); }
        {
          const u32x2 kp = pk4(akv);
          auto r0 = __builtin_amdgcn_permlane32_swap(kp.x, kp.x, false, false), r1 = __builtin_amdgcn_permlane32_swap(kp.y, kp.y, false, false);
          if (lane < 32) gst16_sc1<0>(kb_ + P * 512, l16, u32x4{r0[0], r1[0], r0[1], r1[1]});
        }
        asm volatile("s_nop 7" : "+v"(az));
        if (t16 == 0) gst16_sc1<0>(kb_ + 8192 + P * 64, g * 16u, __builtin_bit_cast(u32x4, az));
        DRAIN();
      }
      STAMP(37);
      RAWBAR();
      if (w == 0 && lane == 0) gadd1(ws, KCNT(layer));
      STAMP(2 + 12 * layer);
      u32x4 phq[2][2];
      {
        LAS char* wq = opaque(lds + 65536 + TRA);
        gemm<1, 0, 32, 4, 32>(acc + 2, xf, [&](auto KB, auto) { return trfrag(wq + KB.v * 1024, 512); }, nop, nop);
        STAMP(36);
        RAWBAR();
        STAMP(46);
        u32x4 yq[2];
        lnfix(acc[2], lds + LD_S + 512 + lane * 4, nmean);
        pack16(acc[2], yq);
        f32x16 pp = mfma(asb(wph[0]), asb(yq[0]), z16); pp = mfma(asb(wph[1]), asb(yq[1]), pp);
        softmax2(pp, rstd * 1.4426950408889634f, phq);
      }
      STAMP(47);
      FRESH();
      if (w == 0) cnt_wait(ws, KCNT(layer), 16u);
      RAWBAR();
      STAMP(3 + 12 * layer);
      {
        const char* xb = ws + WS_KVP + (size_t)((layer * 16 + j) * 4 + w) * KVP_UNIT;
        u32x4 zz;
        gld16_sc1<0>(zz, xb + 8192, t16 * 64u + g * 16u);
        sfor<0, 8>([&](auto I) { LDF(I.v, I.v); });
        RES2ACC()
        LAS char* gi = lds + LD_SC + 16384 + w * 680 + 8 * g;
        LAS char* gr = lds + LD_SC + 16384 + (tk < 16 ? tk : 16) * 40 + 8 * hh;
        char* ob = ws + WS_O + (size_t)layer * (2u << 20) + ((size_t)(4 * P + w) * 32 + 2 * j) * 1024;
        asm volatile("" : "+v"(gi), "+v"(gr)); asm volatile("" : "+s"(ob));
        waitv<8>(zz);
        f32x4 sz = __builtin_bit_cast(f32x4, zz);
#pragma unroll
        for (int e = 0; e < 4; ++e) sz[e] = rowsum16(sz[e]);
        if (t16 == 0) *(LAS u32x2*)(gi + 640) = pk4(sz);
        float sa[8] = {0.f, 0.f, 0.f, 0.f, 0.f, 0.f, 0.f, 0.f};
        u32 c_lo = 0x00003f80u, c_hi = 0x3f800000u; asm volatile("" : "+s"(c_lo), "+s"(c_hi));
        sfor<0, 8>([&](auto I) {
          waitv<7 - I.v>(xf[I.v]);
#pragma unroll
          for (int e = 0; e < 4; ++e) { sa[2 * e] = dot2(xf[I.v][e], c_lo, sa[2 * e]); sa[2 * e + 1] = dot2(xf[I.v][e], c_hi, sa[2 * e + 1]); }
        });
        f32x4 s0;
#pragma unroll
        for (int e = 0; e < 4; ++e) { swap32(sa[e], sa[4 + e]); s0[e] = sa[e] + sa[4 + e]; }
        *(LAS u32x2*)(gi + t16 * 40) = pk4(s0);
        STAMP(48);
        BAR();
        STAMP(49);
        f32x16 o[2];
        sfor<0, 2>([&](auto HD) {
          constexpr int hd = HD.v;
          u32x2 a = *(LAS u32x2*)(gr + hd * 1360), b = *(LAS u32x2*)(gr + hd * 1360 + 16), c = *(LAS u32x2*)(gr + hd * 1360 + 680), d = *(LAS u32x2*)(gr + hd * 1360 + 696);
          o[hd] = mfma(asb(u32x4{a.x, a.y, b.x, b.y}), asb(phq[hd][0]), z16);
          o[hd] = mfma(asb(u32x4{c.x, c.y, d.x, d.y}), asb(phq[hd][1]), o[hd]);
        });
        sfor<0, 2>([&](auto HD) {
          constexpr int hd = HD.v;
          const float r = __builtin_amdgcn_rcpf(o[hd][8] + 1e-6f);
          gst16_sc1<hd * 1024>(ob, l16, u32x4{pk2v(pr(o[hd], 0) * r), pk2v(pr(o[hd], 1) * r), pk2v(pr(o[hd], 2) * r), pk2v(pr(o[hd], 3) * r)});
        });
        STAMP(50);
        DRAIN();
        STAMP(4 + 12 * layer);
      }
#define TILE_REDUCE(EXOFF) { LAS char* ex = lds + (EXOFF) + l16; \
        _Pragma("unroll") for (int i = 0; i < 4; ++i) *(LAS f32x4*)(ex + w * 4096 + i * 1024) = f32x4{acc[1][4 * i], acc[1][4 * i + 1], acc[1][4 * i + 2], acc[1][4 * i + 3]}; \
        BAR(); \
        _Pragma("unroll") for (int i = 0; i < 4; ++i) { const f32x4 v = *(LAS f32x4*)(ex + (w ^ 2) * 4096 + i * 1024) + f32x4{acc[0][4 * i], acc[0][4 * i + 1], acc[0][4 * i + 2], acc[0][4 * i + 3]}; \
          resid[i >> 1][2 * (i & 1)] = pk2(v[0], v[1]); resid[i >> 1][2 * (i & 1) + 1] = pk2(v[2], v[3]); } }
#define TILE_PUBLISH(xo, so) { gst16_sc1<0>(xo, l16, resid[0]); gst16_sc1<1024>(xo, l16, resid[1]); \
        float sum = 0.f, ssq = 0.f, sum2 = 0.f, ssq2 = 0.f; \
        _Pragma("unroll") for (int e = 0; e < 4; ++e) { sum = dot2(resid[0][e], 0x3f803f80u, sum); ssq = dot2(resid[0][e], resid[0][e], ssq); sum2 = dot2(resid[1][e], 0x3f803f80u, sum2); ssq2 = dot2(resid[1][e], resid[1][e], ssq2); } \
        sum += sum2; ssq += ssq2; red2sum(sum, ssq); sum2 = sum; ssq2 = ssq; swap16(sum, sum2); swap16(ssq, ssq2); \
        if (lane < 16) gst16_sc1<0>(so, l16, u32x4{__float_as_uint(sum), __float_as_uint(ssq), __float_as_uint(sum2), __float_as_uint(ssq2)}); \
        DRAIN(); }
      FRESH();
      {
        const size_t ch = (size_t)(4 * P + 2 * th + cc);
        const char* xb = ws + WS_O + (size_t)layer * (2u << 20) + (ch * 32 + 16 * kh) * 1024;
        char* xo = ws + WS_X1 + (size_t)layer * (2u << 20) + (ch * 32 + 4 * cg + 2 * kh) * 1024;
        char* so = ws + WS_ST1 + (size_t)layer * (256u << 10) + (ch * 16 + 2 * cg + kh) * 256;
        LAS char* wa[2] = {opaque(lds + LD_WA3 + kh * 32768 + (TRB0 ^ (64 * kh))), opaque(lds + LD_WA3 + kh * 32768 + (TRB1 ^ (64 * kh)))};
        asm volatile("" : "+s"(xb), "+s"(xo), "+s"(so));
        HANDOFF(0);
        STAMP(5 + 12 * layer);
        sfor<0, N0>([&](auto I) { LDF(I.v, I.v); });
        STAMP(51);
        gemm<2, 0, 16, 4, 32>(acc, xf, [&](auto KB, auto T) { return trfrag(wa[T.v] + KB.v * 2048, 1024); }, [&](auto I) {
          constexpr int s = I.v;
          if constexpr (s % 2 == 0) waitv<(N0 + s / 2 < 16 ? N0 + s / 2 : 16) - s / 2 - 1>(xf[s / 2]);
          if constexpr (s % 2 == 1 && N0 + s / 2 < 16) LDF(N0 + s / 2, N0 + s / 2);
        }, nop);
        STAMP(52);
        TILE_REDUCE(LD_SC)
        STAMP(53);
        TILE_PUBLISH(xo, so)
        STAMP(6 + 12 * layer);
      }
      FRESH();
      {
        const char* xb = ws + WS_X1 + (size_t)layer * (2u << 20) + (size_t)(4 * P + w) * 32768;
        LAS char* wa[4] = {opaque(lds + TRB0), opaque(lds + TRB1), opaque(lds + 65536 + TRB0), opaque(lds + 65536 + TRB1)};
        asm volatile("" : "+s"(xb));
        HANDOFF(1);
        STAMP(7 + 12 * layer);
        if (j < 14) {
          u32x4 st[4];
          sfor<0, N0>([&](auto I) { LDF(I.v, I.v); });
          STAMP(28);
          acc[0] = z16; acc[1] = z16; acc[2] = z16; acc[3] = z16;
          gemm<2, 0, 32, 4, 32>(acc, xf, [&](auto KB, auto T) { return trfrag(wa[T.v] + KB.v * 2048, 1024); }, [&](auto I) {
            constexpr int s = I.v;
            if constexpr (s == 2 * (36 - N0)) RAWBAR();
            STREAM32(2, ws + WS_ST1 + (size_t)layer * (256u << 10))
            if constexpr (s == 60) DRAIN();
          }, [&](auto I) { if constexpr (I.v == 60) { STATS() asm volatile("" : "+v"(rstd), "+v"(nmean)); } });
          LAS char* Sp = lds + LD_S + 768 + lane * 4;
          lnfix(acc[0], Sp, nmean); lnfix(acc[1], Sp + 256, nmean);
          STAMP(25);
          RAWBAR();
          STAMP(26);
          char* hb = ws + WS_H + (size_t)layer * (7u << 20) + ((size_t)(4 * P + w) * 112 + 8 * j) * 1024;
          const float c1 = rstd * -1.4426950408889634f, sigma = __builtin_amdgcn_rcpf(rstd);
          float tt[8]; u32x4 hp;
          auto uop = [&](auto M) {
            constexpr int st_ = M.v % 6, e = M.v / 6 - st_, t = e / 16, r = e % 16;
            if constexpr (e >= 0 && e < 64) {
              if constexpr (st_ == 0) tt[e & 7] = acc[t][r] * c1;
              if constexpr (st_ == 1) tt[e & 7] = __builtin_amdgcn_exp2f(tt[e & 7]);
              if constexpr (st_ == 2) tt[e & 7] = __builtin_fmaf(tt[e & 7], sigma, sigma);
              if constexpr (st_ == 3) tt[e & 7] = __builtin_amdgcn_rcpf(tt[e & 7]);
              if constexpr (st_ == 4) tt[e & 7] = acc[t][r] * tt[e & 7];
              if constexpr (st_ < 5) asm volatile("" : "+v"(tt[e & 7]));
              if constexpr (st_ == 5 && (e & 1)) {
                u32 pkd = pk2(tt[(e - 1) & 7], tt[e & 7]); pin1(pkd); hp[(e >> 1) & 3] = pkd;
                if constexpr ((e & 7) == 7) gst16_sc1<((e >> 3) & 3) * 1024>(hb + (e >> 5) * 4096, l16, hp);
              }
            }
          };
          constexpr int UB = 32 * 6, UC = 48 * 6;
          gemm<1, 0, 32, 4, 32>(acc + 2, xf, [&](auto KB, auto) { return trfrag(wa[2] + KB.v * 2048, 1024); }, nop, [&](auto I) { if constexpr (I.v >= 1) sfor<(I.v - 1) * UB / 31, I.v * UB / 31>(uop); });
          lnfix(acc[2], Sp + 512, nmean);
          STAMP(27);
          gemm<1, 0, 32, 4, 32>(acc + 3, xf, [&](auto KB, auto) { return trfrag(wa[3] + KB.v * 2048, 1024); },
                                [&](auto I) { if constexpr (I.v == 18) { STAMP(34); DRAIN(); RAWBAR();   } },
                                [&](auto I) { constexpr int i = I.v, u0 = i < 6 ? 5 * i : 30 + (i - 6) * (UC - UB - 30) / 26, u1 = i + 1 < 6 ? 5 * (i + 1) : 30 + (i - 5) * (UC - UB - 30) / 26; sfor<UB + u0, UB + u1>(uop); });
          lnfix(acc[3], Sp + 768, nmean);
          STAMP(8 + 12 * layer);
          sfor<UC, 70 * 6>(uop);
          STAMP(35);
          RAWBAR();
          STAMP(55);
        } else { RAWBAR(); RAWBAR(); RAWBAR(); RAWBAR(); }
      }
      FRESH();
      {
        const size_t ch = (size_t)(4 * P + 2 * th + cc);
        const char* xb = ws + WS_H + (size_t)layer * (7u << 20) + (ch * 112 + 56 * kh) * 1024;
        const int t0 = kh * 8192 + (TRB0 ^ (64 * kh)), t1 = t0 ^ 64;
        LAS char* wa[6] = {opaque(lds + t0), opaque(lds + t1), opaque(lds + 65536 + t0), opaque(lds + 65536 + t1), opaque(lds + 131072 + t0), opaque(lds + 131072 + t1)};
        asm volatile("" : "+s"(xb));
        RES2ACC()
        DRAIN();
        STAMP(9 + 12 * layer);
        RAWBAR();
        STAMP(10 + 12 * layer);
#define LDH(i) LDF((i) % 32, (i) < 28 ? 8 * ((i) / 4) + (i) % 4 : 8 * (((i) - 28) / 4) + 4 + (i) % 4)
        constexpr int XB = 44;
        sfor<0, N0>([&](auto I) { LDH(I.v); });
        STAMP(56);
        gemm<2, 0, 56, 4, 32>(acc, xf, [&](auto KB, auto T) { constexpr int so = b2slot(KB.v >> 2); return trfrag(wa[(so >> 16) * 2 + T.v] + (so & 65535) + (KB.v & 3) * 2048, 1024); }, [&](auto I) {
          constexpr int s = I.v, kb = s / 2;
          if constexpr (s == XB) STAMP(57);
          if constexpr (s == 68) STAMP(59);
          if constexpr (s == 100) STAMP(61);
          if constexpr (s == 32 || s == XB || s == 68 || s == 100) RAWBAR();
          if constexpr (s == XB) STAMP(58);
          if constexpr (s == 68) STAMP(60);
          constexpr int KX = XB / 2, NL = KX + N0 - 28;
          constexpr int iss = kb <= KX ? (N0 + kb < 28 ? N0 + kb : 28) : (N0 + kb < 56 ? N0 + kb : 56);
          if constexpr (s % 2 == 0) waitv<iss - kb - 1>(xf[kb % 32]);
          if constexpr (s == XB) sfor<28, 28 + NL>([&](auto J) { LDH(J.v); });
          if constexpr (s % 2 == 1 && (kb < KX ? N0 + kb < 28 : N0 + kb < 56)) LDH(N0 + kb);
        }, nop);
        STAMP(11 + 12 * layer);
        TILE_REDUCE(98304)
        if (layer == 0) {
          char* xo = ws + WS_X2 + (ch * 32 + 4 * cg + 2 * kh) * 1024;
          char* so = ws + WS_ST2 + (ch * 16 + 2 * cg + kh) * 256;
          TILE_PUBLISH(xo, so)
          STAMP(12);
          HANDOFF(4);
        } else {
          char* ob = out + (size_t)(128 * P + 64 * th + 32 * cc) * 1024 + 128 * cg + 64 * kh;
          sfor<0, 2>([&](auto S) {
            auto r0 = __builtin_amdgcn_permlane32_swap(resid[S.v].x, resid[S.v].z, false, false);
            auto r1 = __builtin_amdgcn_permlane32_swap(resid[S.v].y, resid[S.v].w, false, false);
            gst16<32 * S.v>(ob, tk * 1024u + 16u * hh, u32x4{r0[0], r1[0], r0[1], r1[1]});
          });
        }
      }
    }
  }
}

extern "C" void kernel_launch(void* const* d_in, const int* in_sizes, int n_in, void* d_out, int out_size, void* d_ws, size_t ws_size, hipStream_t stream) {
  mega<<<256, 512, 0, stream>>>((const char*)d_in[0], (const char*)d_in[1], (const char*)d_in[2], (const char*)d_in[3], (const char*)d_in[5], (const char*)d_in[6], (char*)d_out, (char*)d_ws);
}
```

```cpp
#define PROBE 0
#include <hip/hip_runtime.h>
#include <hip/hip_bf16.h>

typedef unsigned u32;
typedef unsigned short u16;
typedef u32 u32x4 __attribute__((ext_vector_type(4)));
typedef u32 u32x2 __attribute__((ext_vector_type(2)));
typedef float f32x16 __attribute__((ext_vector_type(16)));
typedef float f32x4 __attribute__((ext_vector_type(4)));
typedef float f32x2 __attribute__((ext_vector_type(2)));
typedef short s16x4 __attribute__((ext_vector_type(4)));
typedef __bf16 bf16x2 __attribute__((ext_vector_type(2)));
typedef __bf16 bf16x8 __attribute__((ext_vector_type(8)));
#define LAS __attribute__((address_space(3)))
#define DI __device__ __forceinline__

constexpr size_t WS_CTL = 0;
constexpr size_t WS_ST1 = 512u << 10;
constexpr size_t WS_ST2 = 1024u << 10;
constexpr size_t WS_KVP = 1280u << 10;
constexpr u32 KVP_UNIT = 9216;
constexpr size_t WS_O = 4u << 20;
constexpr size_t WS_X1 = 8u << 20;
constexpr size_t WS_X2 = 12u << 20;
constexpr size_t WS_H = 14u << 20;
constexpr size_t WS_STAMP = 40u << 20;
constexpr u32 C_IN1 = 1024, C_REQ = 1152;

constexpr u32 LD_STG = 96 * 1024;
constexpr u32 LD_WA3 = 64 * 1024;
constexpr u32 LD_SC = 128 * 1024;
constexpr u32 LD_HX2 = 144 * 1024;
constexpr u32 LD_RS = 153 * 1024;
constexpr u32 LD_STAMP = 154 * 1024;
constexpr u32 LD_S = 158 * 1024;
#define TOUCH 1
#define N0V 12
constexpr u32 SPIN_CAP = 1u << 17;

#define BAR() do { asm volatile("s_waitcnt lgkmcnt(0)" ::: "memory"); __builtin_amdgcn_s_barrier(); asm volatile("" ::: "memory"); } while (0)
#define RAWBAR() do { asm volatile("" ::: "memory"); __builtin_amdgcn_s_barrier(); asm volatile("" ::: "memory"); } while (0)
#define FRESH() do { asm volatile("" : "+s"(ws), "+s"(P), "+s"(j), "+s"(w), "+s"(x), "+s"(wqkv), "+s"(wo), "+s"(w1), "+s"(w2)); asm volatile("" : "+v"(lane)); th = j >> 3; cg = j & 7; cc = w & 1; kh = (w >> 1) & 1; \
  hh = lane >> 5; tk = lane & 31; g = lane >> 4; t16 = lane & 15; q = (lane >> 2) & 3; p = lane & 3; l16 = lane * 16u; } while (0)
#define TRB0 ((4 * hh + q) * 128 + 64 * (q >> 1) + 32 * (g & 1) + 8 * p)
#define TRB1 (TRB0 ^ 64)
#define TRA ((4 * hh + q) * 64 + 32 * (g & 1) + 8 * p)
#define HEL_OWN ((u32)(P * 16 + j) * 2048u + 4u * tk)
#define HEL_PEER (tk < 16 ? (u32)(P * 16 + tk) * 2048u + 4u * j : (u32)((tk - 16) * 16 + j) * 2048u + 64u + 4u * P)
#define PCNT(l, n) ((u32)P * 32768u + (l) * 2048u + (n) * 128u)
#define KCNT(l) ((u32)((1 + (l)) * 16 + j) * 2048u + 896u)
#define DRAIN() asm volatile("s_waitcnt vmcnt(0)" ::: "memory")

#define STAMP(i) do { if ((i) == 0) probe0 = __builtin_amdgcn_s_memrealtime(); if ((i) == PROBE) probe1 = __builtin_amdgcn_s_memrealtime(); } while (0)

template <int N> DI void waitv(u32x4& a) { asm volatile("s_waitcnt vmcnt(%1)" : "+v"(a) : "i"(N)); }
template <int N> DI void waitv4(u32x4& a, u32x4& b, u32x4& c, u32x4& d) { asm volatile("s_waitcnt vmcnt(%4)" : "+v"(a), "+v"(b), "+v"(c), "+v"(d) : "i"(N)); }
DI void waitl8(u32x4* f) { asm volatile("" : "+v"(f[0]), "+v"(f[1]), "+v"(f[2]), "+v"(f[3]), "+v"(f[4]), "+v"(f[5]), "+v"(f[6]), "+v"(f[7])::"memory"); }
DI void pin(u32x4& a) { asm volatile("" : "+v"(a)); }
DI void pin2(u32x2& a) { asm volatile("" : "+v"(a)); }
DI void pin1(u32& a) { asm volatile("" : "+v"(a)); }

template <int OFF> DI void gld16_sc1(u32x4& d, const char* sb, u32 vo) { asm volatile("global_load_dwordx4 %0, %1, %2 offset:%3 sc1" : "=v"(d) : "v"(vo), "s"(sb), "i"(OFF) : "memory"); }
template <int OFF> DI void gld16(u32x4& d, const char* sb, u32 vo) { asm volatile("global_load_dwordx4 %0, %1, %2 offset:%3" : "=v"(d) : "v"(vo), "s"(sb), "i"(OFF) : "memory"); }
template <int OFF> DI void gld8(u32x2& d, const char* sb, u32 vo) { asm volatile("global_load_dwordx2 %0, %1, %2 offset:%3" : "=v"(d) : "v"(vo), "s"(sb), "i"(OFF) : "memory"); }
DI void gld4_sc1(u32& d, const char* sb, u32 vo) { asm volatile("global_load_dword %0, %1, %2 sc1" : "=v"(d) : "v"(vo), "s"(sb) : "memory"); }
template <int OFF> DI void gst16_sc1(char* sb, u32 vo, u32x4 d) { asm volatile("global_store_dwordx4 %0, %1, %2 offset:%3 sc1\n\ts_nop 1" ::"v"(vo), "v"(d), "s"(sb), "i"(OFF) : "memory"); }
DI void gst8_sc1(char* sb, u32 vo, u32x2 d) { asm volatile("global_store_dwordx2 %0, %1, %2 sc1" ::"v"(vo), "v"(d), "s"(sb) : "memory"); }
template <int OFF> DI void gst8(char* sb, u32 vo, u32x2 d) { asm volatile("global_store_dwordx2 %0, %1, %2 offset:%3" ::"v"(vo), "v"(d), "s"(sb), "i"(OFF) : "memory"); }
DI void gadd1(char* sb, u32 vo) { asm volatile("global_atomic_add %0, %1, %2 sc1" ::"v"(vo), "v"(1u), "s"(sb) : "memory"); }
template <int OFF> DI void gst16(char* sb, u32 vo, u32x4 d) { asm volatile("global_store_dwordx4 %0, %1, %2 offset:%3\n\ts_nop 1" ::"v"(vo), "v"(d), "s"(sb), "i"(OFF) : "memory"); }
DI void gst4_sc1(char* sb, u32 vo, u32 d) { asm volatile("global_store_dword %0, %1, %2 sc1" ::"v"(vo), "v"(d), "s"(sb) : "memory"); }
DI void glds(u32 ldsaddr, const char* sb, u32 vo) { asm volatile("s_mov_b32 m0, %0\n\ts_nop 0\n\tglobal_load_lds_dwordx4 %1, %2" ::"s"(ldsaddr), "v"(vo), "s"(sb) : "memory"); }

DI bf16x8 trfrag(LAS char* p, int second) {
  s16x4 a = __builtin_amdgcn_ds_read_tr16_b64_v4i16((LAS s16x4*)p);
  s16x4 b = __builtin_amdgcn_ds_read_tr16_b64_v4i16((LAS s16x4*)(p + second));
  return __builtin_bit_cast(bf16x8, __builtin_shufflevector(a, b, 0, 1, 2, 3, 4, 5, 6, 7));
}
DI bf16x8 asb(u32x4 v) { return __builtin_bit_cast(bf16x8, v); }
DI f32x4 mfma16(bf16x8 a, bf16x8 b, f32x4 c) { return __builtin_amdgcn_mfma_f32_16x16x32_bf16(a, b, c, 0, 0, 0); }
DI f32x16 mfma(bf16x8 a, bf16x8 b, f32x16 c) { return __builtin_amdgcn_mfma_f32_32x32x16_bf16(a, b, c, 0, 0, 0); }
DI u32 pk2(float a, float b) { f32x2 v = {a, b}; return __builtin_bit_cast(u32, __builtin_convertvector(v, bf16x2)); }
DI u32 pk2v(f32x2 v) { return __builtin_bit_cast(u32, __builtin_convertvector(v, bf16x2)); }
DI u32x2 pk4(f32x4 v) { u32x2 r; r.x = pk2(v[0], v[1]); r.y = pk2(v[2], v[3]); return r; }
DI float bflo(u32 u) { return __uint_as_float(u << 16); }
DI float bfhi(u32 u) { return __uint_as_float(u & 0xffff0000u); }
DI float dot2(u32 a, u32 b, float c) { return __builtin_amdgcn_fdot2_f32_bf16(__builtin_bit_cast(bf16x2, a), __builtin_bit_cast(bf16x2, b), c, false); }
DI void swap32(float& a, float& b) { auto r = __builtin_amdgcn_permlane32_swap(__float_as_uint(a), __float_as_uint(b), false, false); a = __uint_as_float(r[0]); b = __uint_as_float(r[1]); }
DI void swap16(float& a, float& b) { auto r = __builtin_amdgcn_permlane16_swap(__float_as_uint(a), __float_as_uint(b), false, false); a = __uint_as_float(r[0]); b = __uint_as_float(r[1]); }
DI float mx2(float a, float b) { return __builtin_amdgcn_fmed3f(a, b, __builtin_inff()); }
DI float mx3(float a, float b, float c) { return __builtin_fmaxf(__builtin_fmaxf(a, b), c); }
DI void red2max(float& a, float& b) { swap32(a, b); float u = mx2(a, b); a = u; b = u; swap32(a, b); }
DI void red2sum(float& a, float& b) { swap32(a, b); float u = a + b; a = u; b = u; swap32(a, b); }
template <int CTRL> DI float dppadd(float v) { return v + __uint_as_float(__builtin_amdgcn_update_dpp(0u, __float_as_uint(v), CTRL, 0xf, 0xf, true)); }
DI float rowsum16(float v) { v = dppadd<0xB1>(v); v = dppadd<0x4E>(v); v = dppadd<0x141>(v); v = dppadd<0x140>(v); return v; }
DI f32x2 pr(const f32x16& a, int i) { return f32x2{a[2 * i], a[2 * i + 1]}; }

DI void softmax2(const f32x16& p, const float L2E  , u32x4 (&f)[2][2]) {
  float mx[2], mn[2];
#pragma unroll
  for (int hd = 0; hd < 2; ++hd) {
    const int b = 8 * hd;
    mx[hd] = mx2(mx3(mx3(mx3(p[b], p[b + 1], p[b + 2]), p[b + 3], p[b + 4]), p[b + 5], p[b + 6]), p[b + 7]);
    mn[hd] = mx2(mx3(mx3(mx3(-p[b], -p[b + 1], -p[b + 2]), -p[b + 3], -p[b + 4]), -p[b + 5], -p[b + 6]), -p[b + 7]);
    red2max(mx[hd], mn[hd]);
  }
  f32x2 e[2][2][4]; float s[2][2];
#pragma unroll
  for (int hd = 0; hd < 2; ++hd) {
    const float cp = -mx[hd] * L2E, cn = -mn[hd] * L2E;
#pragma unroll
    for (int i = 0; i < 4; ++i) {
      const f32x2 v = pr(p, 4 * hd + i), a = v * L2E + cp, b = v * -L2E + cn;
      e[hd][0][i] = f32x2{__builtin_amdgcn_exp2f(a.x), __builtin_amdgcn_exp2f(a.y)};
      e[hd][1][i] = f32x2{__builtin_amdgcn_exp2f(b.x), __builtin_amdgcn_exp2f(b.y)};
    }
#pragma unroll
    for (int sg = 0; sg < 2; ++sg) { const f32x2 t = (e[hd][sg][0] + e[hd][sg][1]) + (e[hd][sg][2] + e[hd][sg][3]); s[hd][sg] = t.x + t.y; }
    red2sum(s[hd][0], s[hd][1]);
#pragma unroll
    for (int sg = 0; sg < 2; ++sg) {
      const float r = __builtin_amdgcn_rcpf(s[hd][sg]);
#pragma unroll
      for (int i = 0; i < 4; ++i) f[hd][sg][i] = pk2v(e[hd][sg][i] * r);
    }
  }
}
DI void pack16(const f32x16& a, u32x4 (&y)[2]) {
#pragma unroll
  for (int i = 0; i < 8; ++i) y[i >> 2][i & 3] = pk2v(pr(a, i));
}
DI void lnfix(f32x16& a, LAS char* Sp, u32 negmean) { a = mfma(asb(u32x4{*(LAS u32*)Sp, 0u, 0u, 0u}), asb(u32x4{negmean, 0u, 0u, 0u}), a); }
DI u32 split2(float v) { const u32 hi = pk2(v, v) & 0xffffu; return hi | (pk2(v - bflo(hi), 0.f) << 16); }

template <int V> struct IC { static constexpr int v = V; };
template <int B, int E, class F> DI void sfor(F&& f) { if constexpr (B < E) { f(IC<B>{}); sfor<B + 1, E>(f); } }
#define SB() __builtin_amdgcn_sched_barrier(0)
template <int NT, int K0, int K1, int D, int XM, class LD, class PRE, class POST> DI void gemm(f32x16* acc, u32x4* xf, LD&& ld, PRE&& pre, POST&& post) {
  bf16x8 wf[D + 1];
  constexpr int S0 = K0 * NT, S1 = K1 * NT;
  sfor<S0, S0 + D>([&](auto I) { constexpr int s = I.v; wf[s % (D + 1)] = ld(IC<s / NT>{}, IC<s % NT>{}); });
  SB();
  sfor<S0, S1>([&](auto I) {
    constexpr int s = I.v;
    pre(I);
    if constexpr (s + D < S1) wf[(s + D) % (D + 1)] = ld(IC<(s + D) / NT>{}, IC<(s + D) % NT>{});
    SB();
    post(I);
    acc[s % NT] = mfma(wf[s % (D + 1)], asb(xf[(s / NT) % XM]), acc[s % NT]);
    asm volatile("" : "+v"(acc[s % NT]));
    SB();
  });
}
constexpr int b2slot(int t) { return t < 4 ? t * 16384 : t == 4 || t == 13 ? 131072 : t < 9 ? 65536 + (t - 5) * 16384 : (t - 9) * 16384; }
DI LAS char* opaque(LAS char* p) { asm volatile("" : "+v"(p)); return p; }

DI void cnt_wait(const char* ctl, u32 off, u32 val) {
  for (u32 sp = 0; sp < SPIN_CAP; ++sp) {
    u32 v; gld4_sc1(v, ctl, off); asm volatile("s_waitcnt vmcnt(0)" : "+v"(v)::"memory");
    if (__builtin_amdgcn_readfirstlane(v) >= val) break;
  }
}
#define HANDOFF(n) do { RAWBAR(); if (w == 0) { if (lane == 0) gadd1(ws, PCNT(layer, n)); cnt_wait(ws, PCNT(layer, n), 16u); } RAWBAR(); } while (0)

__global__ __launch_bounds__(512, 2) void mega(const char* x, const char* wqkv, const char* wphi, const char* wo, const char* w1, const char* w2, char* out, char* ws) {
  __shared__ __attribute__((aligned(16))) char smem[163840];
  LAS char* lds = (LAS char*)smem;
  const u32 lb = (u32)(size_t)lds;
  int lane = threadIdx.x & 63; int w = __builtin_amdgcn_readfirstlane(threadIdx.x >> 6);
  int hh = lane >> 5, tk = lane & 31, g = lane >> 4, t16 = lane & 15, q = (lane >> 2) & 3, p = lane & 3;
  const int bid = blockIdx.x, loc = bid >> 3;
  int P = (bid & 7) * 2 + (loc >> 4), j = loc & 15;
  int th = j >> 3, cg = j & 7, cc = w & 1, kh = (w >> 1) & 1;
  u32 l16 = lane * 16u;
  const u32x4 ones4 = {0x3f803f80u, 0x3f803f80u, 0x3f803f80u, 0x3f803f80u};
  const f32x16 z16 = {0.f, 0.f, 0.f, 0.f, 0.f, 0.f, 0.f, 0.f, 0.f, 0.f, 0.f, 0.f, 0.f, 0.f, 0.f, 0.f};
  unsigned long long probe0 = 0, probe1 = 0;
  STAMP(0);
  if (w >= 4) {
    const u32 swz = 16u * ((lane & 7) ^ ((g & 1) << 2));
    const u32 vo1024 = (lane >> 3) * 1024u + swz, vo3584 = (lane >> 3) * 3584u + swz, voA1 = (lane >> 2) * 3072u + 16u * (lane & 3);
    u32x4 wa1[16];
    u32x4 wb2[36];
    LAS char* wl = lds + l16;
#define HW (w & 3)
#define GLDS_Q(n) glds(lb + 65536 + (n) * 1024, wqkv + 64 * j + (size_t)((n) * 16) * 3072, voA1)
#define GLDS_B1(n) glds(lb + (n) * 1024, w1 + (size_t)(((n) & 63) * 8) * 3584 + j * 256 + ((n) >> 6) * 128, vo3584)
#define B2SRC(n) (w2 + cg * 128 + (size_t)(896 * (((n) >> 3) & 1) + (((n) >> 4) < 7 ? 128 * ((n) >> 4) : 128 * ((n) >> 4) - 832) + 8 * ((n) & 7)) * 1024)
#define GLDS_B2(n) glds(lb + ((n) < 64 ? (n) : (n) + 64) * 1024, B2SRC(n), vo1024)
#define GLDS_A3(n) glds(lb + LD_WA3 + (n) * 1024, wo + (size_t)(n) * 8192 + cg * 128, vo1024)
    auto putA1 = [&](auto R, auto M) { *(LAS u32x4*)(wl + R.v * 32768 + (4 * M.v + HW) * 1024) = wa1[R.v * 8 + M.v]; };
    auto putB2 = [&](auto T) { sfor<0, 4>([&](auto I) { *(LAS u32x4*)(wl + b2slot(T.v) + (4 * HW + I.v) * 1024) = wb2[(T.v - 5) * 4 + I.v]; }); };
    auto ldB2 = [&](auto T) { sfor<0, 4>([&](auto I) { gld16<0>(wb2[(T.v - 5) * 4 + I.v], B2SRC(T.v * 16 + 4 * HW + I.v), vo1024); }); };
    auto ldB1b = [&](u32x4* r) { const char* b = w1 + (size_t)(HW * 128) * 3584 + (j < 14 ? j : 13) * 256 + 128; sfor<0, 16>([&](auto I) { gld16<0>(r[I.v], b + (size_t)(I.v * 8) * 3584, vo3584); }); };
    auto putB1b = [&](u32x4* r) { sfor<0, 16>([&](auto I) { pin(r[I.v]); *(LAS u32x4*)(wl + 65536 + (HW * 16 + I.v) * 1024) = r[I.v]; }); };
    auto colsum = [&](LAS char* sp, int kstride, int second, auto K0, auto K1, f32x16& a) {
      bf16x8 wf[3];
      wf[K0.v % 3] = trfrag(sp + K0.v * kstride, second); wf[(K0.v + 1) % 3] = trfrag(sp + (K0.v + 1) * kstride, second);
      sfor<K0.v, K1.v>([&](auto K) {
        if constexpr (K.v + 2 < K1.v) wf[(K.v + 2) % 3] = trfrag(sp + (K.v + 2) * kstride, second);
        a = mfma(asb(ones4), wf[K.v % 3], a); asm volatile("" : "+v"(a)); SB();
      });
    };
    auto putS = [&](int tile, float a) { const u32 sp = split2(a); *(LAS u32*)(lds + LD_S + tile * 256 + lane * 4) = hh ? (sp >> 16) * 0x10001u : (sp & 0xffffu) * 0x10001u; };
    {
      u32 a0 = 0, sent = 0;
      if (HW == 3) {
        gld4_sc1(a0, ws, HEL_OWN + C_IN1); gst16_sc1<0>(ws + (size_t)(P * 16 + j) * 2048, l16, u32x4{0u, 0u, 0u, 0u});
        asm volatile("s_waitcnt vmcnt(0)" : "+v"(a0)::"memory");
        if (lane < 32) gst4_sc1(ws, HEL_PEER + C_REQ, a0);
      }
      const char* wb = wqkv + 64 * j + (size_t)(16 * HW) * 3072;
      RAWBAR();
      sfor<0, 8>([&](auto M) { gld16<1024>(wa1[M.v], wb + (size_t)(64 * M.v) * 3072, voA1); gld16<2048>(wa1[8 + M.v], wb + (size_t)(64 * M.v) * 3072, voA1); });
      sfor<0, 4>([&](auto C) {
        constexpr int c = C.v;
        waitv4<12 - 4 * c>(wa1[2 * c], wa1[2 * c + 1], wa1[8 + 2 * c], wa1[8 + 2 * c + 1]);
        putA1(IC<0>{}, IC<2 * c>{}); putA1(IC<0>{}, IC<2 * c + 1>{}); putA1(IC<1>{}, IC<2 * c>{}); putA1(IC<1>{}, IC<2 * c + 1>{});
      });
      {
        asm volatile("s_waitcnt lgkmcnt(0)" ::: "memory");
        f32x16 a0_ = z16, a1_ = z16;
        colsum(lds + HW * 1024 + TRA, 4096, 512, IC<0>{}, IC<8>{}, a0_); colsum(lds + 32768 + HW * 1024 + TRA, 4096, 512, IC<0>{}, IC<8>{}, a1_);
        *(LAS float*)(lds + LD_RS + HW * 256 + lane * 4) = hh ? a1_[0] : a0_[0];
      }
      STAMP(33);
      BAR();
      if (HW == 3) {
        gld4_sc1(sent, ws, HEL_OWN + C_REQ); asm volatile("s_waitcnt vmcnt(0)" : "+v"(sent)::"memory");
        if (lane < 32) gst4_sc1(ws, HEL_PEER + C_IN1, sent + 1u);
      }
      u32 tch[4];
      {
        const u32 l4k = lane * 4096u;
        const int ly = HW & 1;
        auto touch = [&](u32& d, const char* b, u32 vo) { asm volatile("global_load_dword %0, %1, %2" : "=v"(d) : "v"(vo), "s"(b) : "memory"); };
        if (HW < 2) {
          touch(tch[0], ws + WS_H + (size_t)ly * (7u << 20) + (size_t)P * 458752, l4k);
          touch(tch[1], ws + WS_H + (size_t)ly * (7u << 20) + (size_t)P * 458752 + 196608, l4k);
          touch(tch[2], ws + WS_O + (size_t)ly * (2u << 20) + (size_t)P * 131072, (lane < 32 ? 0u : (4u << 20) - 131072u) + l4k);
          touch(tch[3], ws + WS_KVP + (size_t)(ly * 16 + j) * 4 * KVP_UNIT, (lane & 7) * 4096u);
        } else {
          touch(tch[0], ly ? out + (size_t)P * 131072 : ws + WS_X2 + (size_t)P * 131072, tk * 4096u);
          touch(tch[1], ws + WS_ST1 + (size_t)P * 16384, (lane & 3) * 4096u + (lane >> 2 < 3 ? (lane >> 2) : 0) * (256u << 10));
          touch(tch[2], w2 + (size_t)ly * 917504, lane * 14336u);
          touch(tch[3], ly ? wo : w1, ly ? lane * 8192u : lane * 28672u);
        }
      }
      _Pragma("unroll 4") for (int i = 0; i < 8; ++i) GLDS_Q(HW * 8 + i);
      ldB1b(wb2 + 20);
      if (HW < 2) { LAS float* pp = (LAS float*)(lds + LD_RS + HW * 128 + tk * 4); putS(HW, (pp[0] + pp[64]) + (pp[128] + pp[192])); }
      STAMP(39);
      BAR();
      if (HW == 3) {
        for (u32 sp_ = 0; sp_ < SPIN_CAP; ++sp_) {
          u32 v, r; gld4_sc1(v, ws, HEL_OWN + C_IN1); gld4_sc1(r, ws, HEL_OWN + C_REQ);
          asm volatile("s_waitcnt vmcnt(0)" : "+v"(v), "+v"(r)::"memory");
          if (r != sent) { sent = r; if (lane < 32) gst4_sc1(ws, HEL_PEER + C_IN1, r + 1u); }
          if (__all(v != a0)) break;
        }
        u32 r; gld4_sc1(r, ws, HEL_OWN + C_REQ); asm volatile("s_waitcnt vmcnt(0)" : "+v"(r)::"memory");
        if (r != sent && lane < 32) gst4_sc1(ws, HEL_PEER + C_IN1, r + 1u);
      }
      DRAIN();
      asm volatile("" ::"v"(tch[0]), "v"(tch[1]), "v"(tch[2]), "v"(tch[3]));
      { f32x16 aS = z16; colsum(lds + 65536 + HW * 8192 + TRA, 1024, 512, IC<0>{}, IC<8>{}, aS); *(LAS float*)(lds + LD_RS + HW * 256 + lane * 4) = aS[0]; }
      BAR();
      if (HW == 2) { LAS float* pp = (LAS float*)(lds + LD_RS + tk * 4); putS(2, (pp[0] + pp[64]) + (pp[128] + pp[192])); }
      BAR();
    }
    sfor<0, 2>([&](auto L) {
      constexpr int layer = L.v;
      FRESH();
      if constexpr (layer == 1) {
        RAWBAR();
        _Pragma("unroll 4") for (int i = 0; i < 8; ++i) GLDS_Q(HW * 8 + i);
        ldB1b(wa1);
        asm volatile("s_waitcnt vmcnt(16)" ::: "memory");
        STAMP(42);
        RAWBAR();
        RAWBAR();
      }
      _Pragma("unroll 4") for (int i = 0; i < 8; ++i) GLDS_A3(32 + HW * 8 + i);
      if (j < 14) { _Pragma("unroll 4") for (int i = 0; i < 8; ++i) GLDS_B1(HW * 16 + i); }
      STAMP(36);
      RAWBAR();
      _Pragma("unroll 4") for (int i = 0; i < 8; ++i) GLDS_A3(HW * 8 + i);
      RAWBAR();
      RAWBAR();
      DRAIN();
      RAWBAR(); RAWBAR();
      if (j < 14) { _Pragma("unroll 4") for (int i = 8; i < 16; ++i) GLDS_B1(HW * 16 + i); }
      RAWBAR();
      putB1b(layer == 0 ? wb2 + 20 : wa1);
      DRAIN();
      RAWBAR(); BAR();
      FRESH();
      if (layer == 0 && j < 14) { f32x16 aS = z16; colsum(lds + (HW >> 1) * 65536 + ((HW & 1) ? TRB1 : TRB0), 2048, 1024, IC<0>{}, IC<32>{}, aS); putS(3 + HW, aS[0]); }
      BAR();
      if constexpr (layer == 0) { ldB2(IC<5>{}); ldB2(IC<6>{}); ldB2(IC<7>{}); ldB2(IC<8>{}); ldB2(IC<9>{}); }
      STAMP(25);
      RAWBAR();
      _Pragma("unroll 4") for (int i = 0; i < 20; ++i) GLDS_B2(HW * 20 + i);
      STAMP(34);
      RAWBAR();
      if (HW == 0) { if (lane == 0) gadd1(ws, PCNT(layer, 2)); cnt_wait(ws, PCNT(layer, 2), 16u); }
      STAMP(35);
      RAWBAR();
      FRESH();
      DRAIN();
      sfor<0, 16>([&](auto I) { pin(wb2[I.v]); });
      putB2(IC<5>{}); putB2(IC<6>{});
      STAMP(38);
      RAWBAR();
      if (HW == 0 && lane == 0) gadd1(ws, PCNT(layer, 3));
      putB2(IC<7>{}); putB2(IC<8>{});
      BAR();
      if constexpr (layer == 0) { ldB2(IC<10>{}); ldB2(IC<11>{}); ldB2(IC<12>{}); ldB2(IC<13>{}); }
      if (HW == 0) cnt_wait(ws, PCNT(layer, 3), 16u);
      else if (layer == 1) { putB2(IC<9>{}); putB2(IC<10>{}); }
      STAMP(57);
      RAWBAR();
      DRAIN();
      sfor<16, 36>([&](auto I) { pin(wb2[I.v]); });
      if (layer == 0 || HW == 0) { putB2(IC<9>{}); putB2(IC<10>{}); }
      putB2(IC<11>{}); putB2(IC<12>{});
      STAMP(59);
      BAR();
      putB2(IC<13>{});
      BAR();
      if constexpr (layer == 0) sfor<0, 8>([&](auto M) { putA1(IC<0>{}, M); });
      RAWBAR();
      if constexpr (layer == 0) {
        sfor<0, 8>([&](auto M) { putA1(IC<1>{}, M); });
        RAWBAR(); BAR();
      }
    });
  } else {
    u32x4 resid[2];
    u32x4 wph[2];
    u32x4 xf[32];
    f32x16 acc[4];
    float rstd; u32 nmean;
    auto nop = [&](auto) {};
#define LDF(i, n) gld16_sc1<((n) & 3) * 1024>(xf[i], xb + ((n) >> 2) * 4096, l16)
#define FINSTATS(sum, ssq) { red2sum(sum, ssq); const float mean = sum * (1.f / 512.f); rstd = __builtin_amdgcn_rsqf(mx2(ssq * (1.f / 512.f) - mean * mean, 0.f) + 1e-5f); nmean = split2(-mean); }
#define STATS() { _Pragma("unroll") for (int i = 0; i < 4; ++i) pin(st[i]); \
      const f32x4 v = (__builtin_bit_cast(f32x4, st[0]) + __builtin_bit_cast(f32x4, st[1])) + (__builtin_bit_cast(f32x4, st[2]) + __builtin_bit_cast(f32x4, st[3])); \
      float a = v[0], b = v[1], c = v[2], d = v[3]; swap16(a, c); swap16(b, d); float sum = a + c, ssq = b + d; FINSTATS(sum, ssq) }
#define LDSTATS(SB_) { const char* sb = (SB_) + (size_t)(4 * P + w) * 4096; \
      gld16_sc1<0>(st[0], sb, l16); gld16_sc1<1024>(st[1], sb, l16); gld16_sc1<2048>(st[2], sb, l16); gld16_sc1<3072>(st[3], sb, l16); }
    constexpr int N0 = N0V;
#define STREAM32(NT, SB_) { constexpr int kb_ = s / NT; if constexpr (s % NT == 0) waitv<(N0 + kb_ < 36 ? N0 + kb_ : 36) - kb_ - 1>(xf[kb_]); \
      if constexpr (s % NT == 1 && N0 + kb_ < 32) LDF(N0 + kb_, N0 + kb_); \
      if constexpr (s % NT == 1 && N0 + kb_ >= 32 && N0 + kb_ < 36) gld16_sc1<(N0 + kb_ - 32) * 1024>(st[(N0 + kb_) & 3], (SB_) + (size_t)(4 * P + w) * 4096, l16); }
#define RES2ACC() { _Pragma("unroll") for (int i = 0; i < 8; ++i) { const u32 r = resid[i >> 2][i & 3]; acc[0][2 * i] = bflo(r); acc[0][2 * i + 1] = bfhi(r); } acc[1] = z16; asm volatile("" : "+v"(acc[0])); }
    for (int layer = 0; layer < 2; ++layer) {
      FRESH();
      acc[0] = z16; acc[1] = z16; acc[2] = z16;
      LAS char* wa1p = opaque(lds + TRA);
      auto ldkv = [&](auto KB, auto T) { return trfrag(wa1p + T.v * 32768 + KB.v * 1024, 512); };
      if (layer == 0) {
        const char* xb = x + (size_t)(128 * P + 32 * w) * 1024;
        u32 xvo[4];
#pragma unroll
        for (int i = 0; i < 4; ++i) xvo[i] = g * 1024u + ((t16 ^ (4 * i + g)) * 16u);
        const u32 stg = lb + 65536 + w * 16384;
        auto ldx = [&](auto R) { sfor<0, 8>([&](auto I) { glds(stg + (R.v & 1) * 8192 + I.v * 1024, xb + R.v * 256 + I.v * 4096, xvo[I.v & 3]); }); };
        ldx(IC<0>{}); ldx(IC<1>{});
        u32 wp[8];
        STAMP(32);
        const u32 str = 65536 + w * 16384 + tk * 256 + t16 * 16 + 8 * hh;
        float sm[4] = {0.f, 0.f, 0.f, 0.f}, sq[4] = {0.f, 0.f, 0.f, 0.f};
        sfor<0, 4>([&](auto R) {
          constexpr int b = 8 * R.v;
          asm volatile("s_waitcnt vmcnt(%0)" ::"i"(8) : "memory");
#pragma unroll
          for (int i = 0; i < 8; ++i) {
            u32x2 lo = *(LAS u32x2*)(lds + ((str ^ (32 * i)) + (R.v & 1) * 8192)), hi = *(LAS u32x2*)(lds + ((str ^ (32 * i + 16)) + (R.v & 1) * 8192));
            xf[b + i] = u32x4{lo.x, lo.y, hi.x, hi.y};
          }
          if ((cg >> 1) == R.v && (w >> 1) == th) {
            LAS char* rx = lds + LD_SC + cc * 4096 + l16;
            if (cg & 1) { _Pragma("unroll") for (int i = 0; i < 4; ++i) *(LAS u32x4*)(rx + i * 1024) = xf[b + 4 + i]; }
            else { _Pragma("unroll") for (int i = 0; i < 4; ++i) *(LAS u32x4*)(rx + i * 1024) = xf[b + i]; }
          }
          if constexpr (R.v < 2) { waitl8(xf + b); ldx(IC<R.v + 2>{}); }
          if constexpr (R.v == 0) RAWBAR();
          if constexpr (R.v == 2) {
            const u16* wsrc = (const u16*)wphi + (2 * j + (tk >> 4)) * 256 + (4 * hh) * 16 + t16;
#pragma unroll
            for (int i = 0; i < 8; ++i) asm volatile("global_load_ushort %0, %1, off offset:%2" : "=v"(wp[i]) : "v"(wsrc), "i"((i >> 2) * 256 + (i & 3) * 32) : "memory");
          }
          if constexpr (R.v >= 1) {
            sfor<(R.v == 1 ? 0 : b), b + 8>([&](auto I) {
#pragma unroll
              for (int e = 0; e < 4; ++e) { sm[e] = dot2(xf[I.v][e], 0x3f803f80u, sm[e]); sq[e] = dot2(xf[I.v][e], xf[I.v][e], sq[e]); }
            });
            asm volatile("" : "+v"(sm[0]), "+v"(sm[1]), "+v"(sm[2]), "+v"(sm[3]), "+v"(sq[0]), "+v"(sq[1]), "+v"(sq[2]), "+v"(sq[3]));
          }
          STAMP(29 + (R.v == 3 ? 2 : R.v));
        });
        float sum = (sm[0] + sm[1]) + (sm[2] + sm[3]), ssq = (sq[0] + sq[1]) + (sq[2] + sq[3]);
        FINSTATS(sum, ssq)
        STAMP(33);
        BAR();
        STAMP(54);
        resid[0] = *(LAS u32x4*)(lds + LD_SC + cc * 4096 + kh * 2048 + l16); resid[1] = *(LAS u32x4*)(lds + LD_SC + cc * 4096 + kh * 2048 + 1024 + l16);
        gemm<2, 0, 32, 4, 32>(acc, xf, ldkv, nop, nop);
        STAMP(39);
        DRAIN();
#pragma unroll
        for (int i = 0; i < 8; ++i) pin1(wp[i]);
        const u32x4 wv = {wp[0] | (wp[1] << 16), wp[2] | (wp[3] << 16), wp[4] | (wp[5] << 16), wp[6] | (wp[7] << 16)};
        const u32x4 zz = {0u, 0u, 0u, 0u};
        wph[0] = tk < 16 ? wv : zz; wph[1] = tk < 16 ? zz : wv;
        pin(resid[0]); pin(resid[1]);
        RAWBAR();
      } else {
        STAMP(40);
        u32x4 st[4];
        const char* xb = ws + WS_X2 + (size_t)(4 * P + w) * 32768;
        sfor<0, N0>([&](auto I) { LDF(I.v, I.v); });
        STAMP(41);
        gemm<2, 0, 32, 4, 32>(acc, xf, ldkv, [&](auto I) {
          constexpr int s = I.v;
          if constexpr (s == 2 * (36 - N0)) { STAMP(44); RAWBAR(); STAMP(45); }
          STREAM32(2, ws + WS_ST2)
        }, nop);
        DRAIN();
        STATS()
      }
      STAMP(1 + 12 * layer);
      FRESH();
      {
        u32x4 yk[2], yv[2], f[2][2];
        lnfix(acc[0], lds + LD_S + lane * 4, nmean); lnfix(acc[1], lds + LD_S + 256 + lane * 4, nmean);
        pack16(acc[0], yk);
        f32x16 pp = mfma(asb(wph[0]), asb(yk[0]), z16); pp = mfma(asb(wph[1]), asb(yk[1]), pp);
        pack16(acc[1] * rstd, yv);
        LAS char* kt = lds + LD_SC + (32 * w + tk) * 200 + 8 * hh;
#pragma unroll
        for (int hd = 0; hd < 2; ++hd) { *(LAS u32x2*)(kt + hd * 96 + 64) = u32x2{yv[hd].x, yv[hd].y}; *(LAS u32x2*)(kt + hd * 96 + 80) = u32x2{yv[hd].z, yv[hd].w}; }
        softmax2(pp, rstd * 1.4426950408889634f, f);
#pragma unroll
        for (int hd = 0; hd < 2; ++hd) {
          *(LAS u32x2*)(kt + hd * 96) = u32x2{f[hd][0].x, f[hd][0].y}; *(LAS u32x2*)(kt + hd * 96 + 16) = u32x2{f[hd][0].z, f[hd][0].w};
          *(LAS u32x2*)(kt + hd * 96 + 32) = u32x2{f[hd][1].x, f[hd][1].y}; *(LAS u32x2*)(kt + hd * 96 + 48) = u32x2{f[hd][1].z, f[hd][1].w};
        }
      }
      {
        LAS char* ka = lds + LD_SC + (4 * g + q) * 200 + 8 * p + (w >> 1) * 96;
        char* kb_ = ws + WS_KVP + (size_t)((layer * 16 + j) * 4 + w) * KVP_UNIT;
        asm volatile("" : "+v"(ka)); asm volatile("" : "+s"(kb_));
        STAMP(42);
        BAR();
        STAMP(43);
        f32x4 akv = {0.f, 0.f, 0.f, 0.f}, az = {0.f, 0.f, 0.f, 0.f};
        bf16x8 A[4], B[4];
#pragma unroll
        for (int ks = 0; ks < 4; ++ks) { A[ks] = trfrag(ka + ks * 6400 + (w & 1) * 32, 3200); B[ks] = trfrag(ka + ks * 6400 + 64, 3200); }
        SB();
#pragma unroll
        for (int ks = 0; ks < 4; ++ks) { akv = mfma16(A[ks], B[ks], akv); az = mfma16(A[ks], asb(ones4), az); }
        {
          const u32x2 kp = pk4(akv);
          auto r0 = __builtin_amdgcn_permlane32_swap(kp.x, kp.x, false, false), r1 = __builtin_amdgcn_permlane32_swap(kp.y, kp.y, false, false);
          if (lane < 32) gst16_sc1<0>(kb_ + P * 512, l16, u32x4{r0[0], r1[0], r0[1], r1[1]});
        }
        asm volatile("s_nop 7" : "+v"(az));
        if (t16 == 0) gst16_sc1<0>(kb_ + 8192 + P * 64, g * 16u, __builtin_bit_cast(u32x4, az));
        DRAIN();
      }
      STAMP(37);
      RAWBAR();
      if (w == 0 && lane == 0) gadd1(ws, KCNT(layer));
      STAMP(2 + 12 * layer);
      u32x4 phq[2][2];
      {
        LAS char* wq = opaque(lds + 65536 + TRA);
        gemm<1, 0, 32, 4, 32>(acc + 2, xf, [&](auto KB, auto) { return trfrag(wq + KB.v * 1024, 512); }, nop, nop);
        STAMP(36);
        RAWBAR();
        STAMP(46);
        u32x4 yq[2];
        lnfix(acc[2], lds + LD_S + 512 + lane * 4, nmean);
        pack16(acc[2], yq);
        f32x16 pp = mfma(asb(wph[0]), asb(yq[0]), z16); pp = mfma(asb(wph[1]), asb(yq[1]), pp);
        softmax2(pp, rstd * 1.4426950408889634f, phq);
      }
      STAMP(47);
      FRESH();
      if (w == 0) cnt_wait(ws, KCNT(layer), 16u);
      RAWBAR();
      STAMP(3 + 12 * layer);
      {
        const char* xb = ws + WS_KVP + (size_t)((layer * 16 + j) * 4 + w) * KVP_UNIT;
        u32x4 zz;
        gld16_sc1<0>(zz, xb + 8192, t16 * 64u + g * 16u);
        sfor<0, 8>([&](auto I) { LDF(I.v, I.v); });
        RES2ACC()
        LAS char* gi = lds + LD_SC + 16384 + w * 680 + 8 * g;
        LAS char* gr = lds + LD_SC + 16384 + (tk < 16 ? tk : 16) * 40 + 8 * hh;
        char* ob = ws + WS_O + (size_t)layer * (2u << 20) + ((size_t)(4 * P + w) * 32 + 2 * j) * 1024;
        asm volatile("" : "+v"(gi), "+v"(gr)); asm volatile("" : "+s"(ob));
        waitv<8>(zz);
        f32x4 sz = __builtin_bit_cast(f32x4, zz);
#pragma unroll
        for (int e = 0; e < 4; ++e) sz[e] = rowsum16(sz[e]);
        if (t16 == 0) *(LAS u32x2*)(gi + 640) = pk4(sz);
        float sa[8] = {0.f, 0.f, 0.f, 0.f, 0.f, 0.f, 0.f, 0.f};
        u32 c_lo = 0x00003f80u, c_hi = 0x3f800000u; asm volatile("" : "+s"(c_lo), "+s"(c_hi));
        sfor<0, 8>([&](auto I) {
          waitv<7 - I.v>(xf[I.v]);
#pragma unroll
          for (int e = 0; e < 4; ++e) { sa[2 * e] = dot2(xf[I.v][e], c_lo, sa[2 * e]); sa[2 * e + 1] = dot2(xf[I.v][e], c_hi, sa[2 * e + 1]); }
        });
        f32x4 s0;
#pragma unroll
        for (int e = 0; e < 4; ++e) { swap32(sa[e], sa[4 + e]); s0[e] = sa[e] + sa[4 + e]; }
        *(LAS u32x2*)(gi + t16 * 40) = pk4(s0);
        STAMP(48);
        BAR();
        STAMP(49);
        f32x16 o[2];
        sfor<0, 2>([&](auto HD) {
          constexpr int hd = HD.v;
          u32x2 a = *(LAS u32x2*)(gr + hd * 1360), b = *(LAS u32x2*)(gr + hd * 1360 + 16), c = *(LAS u32x2*)(gr + hd * 1360 + 680), d = *(LAS u32x2*)(gr + hd * 1360 + 696);
          o[hd] = mfma(asb(u32x4{a.x, a.y, b.x, b.y}), asb(phq[hd][0]), z16);
          o[hd] = mfma(asb(u32x4{c.x, c.y, d.x, d.y}), asb(phq[hd][1]), o[hd]);
        });
        sfor<0, 2>([&](auto HD) {
          constexpr int hd = HD.v;
          const float r = __builtin_amdgcn_rcpf(o[hd][8] + 1e-6f);
          gst16_sc1<hd * 1024>(ob, l16, u32x4{pk2v(pr(o[hd], 0) * r), pk2v(pr(o[hd], 1) * r), pk2v(pr(o[hd], 2) * r), pk2v(pr(o[hd], 3) * r)});
        });
        STAMP(50);
        DRAIN();
        STAMP(4 + 12 * layer);
      }
#define TILE_REDUCE(EXOFF) { LAS char* ex = lds + (EXOFF) + l16; \
        _Pragma("unroll") for (int i = 0; i < 4; ++i) *(LAS f32x4*)(ex + w * 4096 + i * 1024) = f32x4{acc[1][4 * i], acc[1][4 * i + 1], acc[1][4 * i + 2], acc[1][4 * i + 3]}; \
        BAR(); \
        _Pragma("unroll") for (int i = 0; i < 4; ++i) { const f32x4 v = *(LAS f32x4*)(ex + (w ^ 2) * 4096 + i * 1024) + f32x4{acc[0][4 * i], acc[0][4 * i + 1], acc[0][4 * i + 2], acc[0][4 * i + 3]}; \
          resid[i >> 1][2 * (i & 1)] = pk2(v[0], v[1]); resid[i >> 1][2 * (i & 1) + 1] = pk2(v[2], v[3]); } }
#define TILE_PUBLISH(xo, so) { gst16_sc1<0>(xo, l16, resid[0]); gst16_sc1<1024>(xo, l16, resid[1]); \
        float sum = 0.f, ssq = 0.f, sum2 = 0.f, ssq2 = 0.f; \
        _Pragma("unroll") for (int e = 0; e < 4; ++e) { sum = dot2(resid[0][e], 0x3f803f80u, sum); ssq = dot2(resid[0][e], resid[0][e], ssq); sum2 = dot2(resid[1][e], 0x3f803f80u, sum2); ssq2 = dot2(resid[1][e], resid[1][e], ssq2); } \
        sum += sum2; ssq += ssq2; red2sum(sum, ssq); sum2 = sum; ssq2 = ssq; swap16(sum, sum2); swap16(ssq, ssq2); \
        if (lane < 16) gst16_sc1<0>(so, l16, u32x4{__float_as_uint(sum), __float_as_uint(ssq), __float_as_uint(sum2), __float_as_uint(ssq2)}); \
        DRAIN(); }
      FRESH();
      {
        const size_t ch = (size_t)(4 * P + 2 * th + cc);
        const char* xb = ws + WS_O + (size_t)layer * (2u << 20) + (ch * 32 + 16 * kh) * 1024;
        char* xo = ws + WS_X1 + (size_t)layer * (2u << 20) + (ch * 32 + 4 * cg + 2 * kh) * 1024;
        char* so = ws + WS_ST1 + (size_t)layer * (256u << 10) + (ch * 16 + 2 * cg + kh) * 256;
        LAS char* wa[2] = {opaque(lds + LD_WA3 + kh * 32768 + (TRB0 ^ (64 * kh))), opaque(lds + LD_WA3 + kh * 32768 + (TRB1 ^ (64 * kh)))};
        asm volatile("" : "+s"(xb), "+s"(xo), "+s"(so));
        HANDOFF(0);
        STAMP(5 + 12 * layer);
        sfor<0, N0>([&](auto I) { LDF(I.v, I.v); });
        STAMP(51);
        gemm<2, 0, 16, 4, 32>(acc, xf, [&](auto KB, auto T) { return trfrag(wa[T.v] + KB.v * 2048, 1024); }, [&](auto I) {
          constexpr int s = I.v;
          if constexpr (s % 2 == 0) waitv<(N0 + s / 2 < 16 ? N0 + s / 2 : 16) - s / 2 - 1>(xf[s / 2]);
          if constexpr (s % 2 == 1 && N0 + s / 2 < 16) LDF(N0 + s / 2, N0 + s / 2);
        }, nop);
        STAMP(52);
        TILE_REDUCE(LD_SC)
        STAMP(53);
        TILE_PUBLISH(xo, so)
        STAMP(6 + 12 * layer);
      }
      FRESH();
      {
        const char* xb = ws + WS_X1 + (size_t)layer * (2u << 20) + (size_t)(4 * P + w) * 32768;
        LAS char* wa[4] = {opaque(lds + TRB0), opaque(lds + TRB1), opaque(lds + 65536 + TRB0), opaque(lds + 65536 + TRB1)};
        asm volatile("" : "+s"(xb));
        HANDOFF(1);
        STAMP(7 + 12 * layer);
        if (j < 14) {
          u32x4 st[4];
          sfor<0, N0>([&](auto I) { LDF(I.v, I.v); });
          STAMP(28);
          acc[0] = z16; acc[1] = z16; acc[2] = z16; acc[3] = z16;
          gemm<2, 0, 32, 4, 32>(acc, xf, [&](auto KB, auto T) { return trfrag(wa[T.v] + KB.v * 2048, 1024); }, [&](auto I) {
            constexpr int s = I.v;
            if constexpr (s == 2 * (36 - N0)) RAWBAR();
            STREAM32(2, ws + WS_ST1 + (size_t)layer * (256u << 10))
            if constexpr (s == 60) DRAIN();
          }, [&](auto I) { if constexpr (I.v == 60) { STATS() asm volatile("" : "+v"(rstd), "+v"(nmean)); } });
          LAS char* Sp = lds + LD_S + 768 + lane * 4;
          lnfix(acc[0], Sp, nmean); lnfix(acc[1], Sp + 256, nmean);
          STAMP(25);
          RAWBAR();
          STAMP(26);
          char* hb = ws + WS_H + (size_t)layer * (7u << 20) + ((size_t)(4 * P + w) * 112 + 8 * j) * 1024;
          const float c1 = rstd * -1.4426950408889634f, sigma = __builtin_amdgcn_rcpf(rstd);
          float tt[8]; u32x4 hp;
          auto uop = [&](auto M) {
            constexpr int st_ = M.v % 6, e = M.v / 6 - st_, t = e / 16, r = e % 16;
            if constexpr (e >= 0 && e < 64) {
              if constexpr (st_ == 0) tt[e & 7] = acc[t][r] * c1;
              if constexpr (st_ == 1) tt[e & 7] = __builtin_amdgcn_exp2f(tt[e & 7]);
              if constexpr (st_ == 2) tt[e & 7] = __builtin_fmaf(tt[e & 7], sigma, sigma);
              if constexpr (st_ == 3) tt[e & 7] = __builtin_amdgcn_rcpf(tt[e & 7]);
              if constexpr (st_ == 4) tt[e & 7] = acc[t][r] * tt[e & 7];
              if constexpr (st_ < 5) asm volatile("" : "+v"(tt[e & 7]));
              if constexpr (st_ == 5 && (e & 1)) {
                u32 pkd = pk2(tt[(e - 1) & 7], tt[e & 7]); pin1(pkd); hp[(e >> 1) & 3] = pkd;
                if constexpr ((e & 7) == 7) gst16_sc1<((e >> 3) & 3) * 1024>(hb + (e >> 5) * 4096, l16, hp);
              }
            }
          };
          constexpr int UB = 32 * 6, UC = 48 * 6;
          gemm<1, 0, 32, 4, 32>(acc + 2, xf, [&](auto KB, auto) { return trfrag(wa[2] + KB.v * 2048, 1024); }, nop, [&](auto I) { if constexpr (I.v >= 1) sfor<(I.v - 1) * UB / 31, I.v * UB / 31>(uop); });
          lnfix(acc[2], Sp + 512, nmean);
          STAMP(27);
          gemm<1, 0, 32, 4, 32>(acc + 3, xf, [&](auto KB, auto) { return trfrag(wa[3] + KB.v * 2048, 1024); },
                                [&](auto I) { if constexpr (I.v == 18) { STAMP(34); DRAIN(); RAWBAR();   } },
                                [&](auto I) { constexpr int i = I.v, u0 = i < 6 ? 5 * i : 30 + (i - 6) * (UC - UB - 30) / 26, u1 = i + 1 < 6 ? 5 * (i + 1) : 30 + (i - 5) * (UC - UB - 30) / 26; sfor<UB + u0, UB + u1>(uop); });
          lnfix(acc[3], Sp + 768, nmean);
          STAMP(8 + 12 * layer);
          sfor<UC, 70 * 6>(uop);
          STAMP(35);
          RAWBAR();
          STAMP(55);
        } else { RAWBAR(); RAWBAR(); RAWBAR(); RAWBAR(); }
      }
      FRESH();
      {
        const size_t ch = (size_t)(4 * P + 2 * th + cc);
        const char* xb = ws + WS_H + (size_t)layer * (7u << 20) + (ch * 112 + 56 * kh) * 1024;
        const int t0 = kh * 8192 + (TRB0 ^ (64 * kh)), t1 = t0 ^ 64;
        LAS char* wa[6] = {opaque(lds + t0), opaque(lds + t1), opaque(lds + 65536 + t0), opaque(lds + 65536 + t1), opaque(lds + 131072 + t0), opaque(lds + 131072 + t1)};
        asm volatile("" : "+s"(xb));
        RES2ACC()
        DRAIN();
        STAMP(9 + 12 * layer);
        RAWBAR();
        STAMP(10 + 12 * layer);
#define LDH(i) LDF((i) % 32, (i) < 28 ? 8 * ((i) / 4) + (i) % 4 : 8 * (((i) - 28) / 4) + 4 + (i) % 4)
        constexpr int XB = 44;
        sfor<0, N0>([&](auto I) { LDH(I.v); });
        STAMP(56);
        gemm<2, 0, 56, 4, 32>(acc, xf, [&](auto KB, auto T) { constexpr int so = b2slot(KB.v >> 2); return trfrag(wa[(so >> 16) * 2 + T.v] + (so & 65535) + (KB.v & 3) * 2048, 1024); }, [&](auto I) {
          constexpr int s = I.v, kb = s / 2;
          if constexpr (s == XB) STAMP(57);
          if constexpr (s == 68) STAMP(59);
          if constexpr (s == 100) STAMP(61);
          if constexpr (s == 32 || s == XB || s == 68 || s == 100) RAWBAR();
          if constexpr (s == XB) STAMP(58);
          if constexpr (s == 68) STAMP(60);
          constexpr int KX = XB / 2, NL = KX + N0 - 28;
          constexpr int iss = kb <= KX ? (N0 + kb < 28 ? N0 + kb : 28) : (N0 + kb < 56 ? N0 + kb : 56);
          if constexpr (s % 2 == 0) waitv<iss - kb - 1>(xf[kb % 32]);
          if constexpr (s == XB) sfor<28, 28 + NL>([&](auto J) { LDH(J.v); });
          if constexpr (s % 2 == 1 && (kb < KX ? N0 + kb < 28 : N0 + kb < 56)) LDH(N0 + kb);
        }, nop);
        STAMP(11 + 12 * layer);
        TILE_REDUCE(98304)
        if (layer == 0) {
          char* xo = ws + WS_X2 + (ch * 32 + 4 * cg + 2 * kh) * 1024;
          char* so = ws + WS_ST2 + (ch * 16 + 2 * cg + kh) * 256;
          TILE_PUBLISH(xo, so)
          STAMP(12);
          HANDOFF(4);
        } else {
          char* ob = out + (size_t)(128 * P + 64 * th + 32 * cc) * 1024 + 128 * cg + 64 * kh;
          sfor<0, 2>([&](auto S) {
            auto r0 = __builtin_amdgcn_permlane32_swap(resid[S.v].x, resid[S.v].z, false, false);
            auto r1 = __builtin_amdgcn_permlane32_swap(resid[S.v].y, resid[S.v].w, false, false);
            gst16<32 * S.v>(ob, tk * 1024u + 16u * hh, u32x4{r0[0], r1[0], r0[1], r1[1]});
          });
        }
      }
    }
  }
  STAMP(24);
  if (blockIdx.x == 40 && threadIdx.x < 64 && PROBE > 0) { const unsigned long long e = __builtin_amdgcn_s_memrealtime(); while (__builtin_amdgcn_s_memrealtime() - e < probe1 - probe0) {} }
}

extern "C" void kernel_launch(void* const* d_in, const int* in_sizes, int n_in, void* d_out, int out_size, void* d_ws, size_t ws_size, hipStream_t stream) {
  mega<<<256, 512, 0, stream>>>((const char*)d_in[0], (const char*)d_in[1], (const char*)d_in[2], (const char*)d_in[3], (const char*)d_in[5], (const char*)d_in[6], (char*)d_out, (char*)d_ws);
}
```

```cpp
#include <hip/hip_runtime.h>
#include <hip/hip_bf16.h>

typedef unsigned u32;
typedef unsigned short u16;
typedef u32 u32x4 __attribute__((ext_vector_type(4)));
typedef u32 u32x2 __attribute__((ext_vector_type(2)));
typedef float f32x16 __attribute__((ext_vector_type(16)));
typedef float f32x4 __attribute__((ext_vector_type(4)));
typedef float f32x2 __attribute__((ext_vector_type(2)));
typedef short s16x4 __attribute__((ext_vector_type(4)));
typedef __bf16 bf16x2 __attribute__((ext_vector_type(2)));
typedef __bf16 bf16x8 __attribute__((ext_vector_type(8)));
#define LAS __attribute__((address_space(3)))
#define DI __device__ __forceinline__

constexpr size_t WS_CTL = 0;
constexpr size_t WS_ST1 = 512u << 10;
constexpr size_t WS_ST2 = 1024u << 10;
constexpr size_t WS_KVP = 1280u << 10;
constexpr u32 KVP_UNIT = 9216;
constexpr size_t WS_O = 4u << 20;
constexpr size_t WS_X1 = 8u << 20;
constexpr size_t WS_X2 = 12u << 20;
constexpr size_t WS_H = 14u << 20;
constexpr size_t WS_STAMP = 40u << 20;
constexpr u32 C_IN1 = 1024, C_REQ = 1152;

constexpr u32 LD_STG = 96 * 1024;
constexpr u32 LD_WA3 = 64 * 1024;
constexpr u32 LD_SC = 128 * 1024;
constexpr u32 LD_HX2 = 144 * 1024;
constexpr u32 LD_RS = 153 * 1024;
constexpr u32 LD_STAMP = 154 * 1024;
constexpr u32 LD_S = 158 * 1024;
#define TOUCH 1
#define HSLEEP 20
#define N0V 12
constexpr u32 SPIN_CAP = 1u << 17;

#define BAR() do { asm volatile("s_waitcnt lgkmcnt(0)" ::: "memory"); __builtin_amdgcn_s_barrier(); asm volatile("" ::: "memory"); } while (0)
#define RAWBAR() do { asm volatile("" ::: "memory"); __builtin_amdgcn_s_barrier(); asm volatile("" ::: "memory"); } while (0)
#define FRESH() do { asm volatile("" : "+s"(ws), "+s"(P), "+s"(j), "+s"(w), "+s"(x), "+s"(wqkv), "+s"(wo), "+s"(w1), "+s"(w2)); asm volatile("" : "+v"(lane)); th = j >> 3; cg = j & 7; cc = w & 1; kh = (w >> 1) & 1; \
  hh = lane >> 5; tk = lane & 31; g = lane >> 4; t16 = lane & 15; q = (lane >> 2) & 3; p = lane & 3; l16 = lane * 16u; } while (0)
#define TRB0 ((4 * hh + q) * 128 + 64 * (q >> 1) + 32 * (g & 1) + 8 * p)
#define TRB1 (TRB0 ^ 64)
#define TRA ((4 * hh + q) * 64 + 32 * (g & 1) + 8 * p)
#define HEL_OWN ((u32)(P * 16 + j) * 2048u + 4u * tk)
#define HEL_PEER (tk < 16 ? (u32)(P * 16 + tk) * 2048u + 4u * j : (u32)((tk - 16) * 16 + j) * 2048u + 64u + 4u * P)
#define PCNT(l, n) ((u32)P * 32768u + (l) * 2048u + (n) * 128u)
#define KCNT(l) ((u32)((1 + (l)) * 16 + j) * 2048u + 896u)
#define DRAIN() asm volatile("s_waitcnt vmcnt(0)" ::: "memory")

#define STAMP(i) do {} while (0)

template <int N> DI void waitv(u32x4& a) { asm volatile("s_waitcnt vmcnt(%1)" : "+v"(a) : "i"(N)); }
template <int N> DI void waitv4(u32x4& a, u32x4& b, u32x4& c, u32x4& d) { asm volatile("s_waitcnt vmcnt(%4)" : "+v"(a), "+v"(b), "+v"(c), "+v"(d) : "i"(N)); }
DI void waitl8(u32x4* f) { asm volatile("" : "+v"(f[0]), "+v"(f[1]), "+v"(f[2]), "+v"(f[3]), "+v"(f[4]), "+v"(f[5]), "+v"(f[6]), "+v"(f[7])::"memory"); }
DI void pin(u32x4& a) { asm volatile("" : "+v"(a)); }
DI void pin2(u32x2& a) { asm volatile("" : "+v"(a)); }
DI void pin1(u32& a) { asm volatile("" : "+v"(a)); }

template <int OFF> DI void gld16_sc1(u32x4& d, const char* sb, u32 vo) { asm volatile("global_load_dwordx4 %0, %1, %2 offset:%3 sc1" : "=v"(d) : "v"(vo), "s"(sb), "i"(OFF) : "memory"); }
template <int OFF> DI void gld16(u32x4& d, const char* sb, u32 vo) { asm volatile("global_load_dwordx4 %0, %1, %2 offset:%3" : "=v"(d) : "v"(vo), "s"(sb), "i"(OFF) : "memory"); }
template <int OFF> DI void gld8(u32x2& d, const char* sb, u32 vo) { asm volatile("global_load_dwordx2 %0, %1, %2 offset:%3" : "=v"(d) : "v"(vo), "s"(sb), "i"(OFF) : "memory"); }
DI void gld4_sc1(u32& d, const char* sb, u32 vo) { asm volatile("global_load_dword %0, %1, %2 sc1" : "=v"(d) : "v"(vo), "s"(sb) : "memory"); }
template <int OFF> DI void gst16_sc1(char* sb, u32 vo, u32x4 d) { asm volatile("global_store_dwordx4 %0, %1, %2 offset:%3 sc1\n\ts_nop 1" ::"v"(vo), "v"(d), "s"(sb), "i"(OFF) : "memory"); }
DI void gst8_sc1(char* sb, u32 vo, u32x2 d) { asm volatile("global_store_dwordx2 %0, %1, %2 sc1" ::"v"(vo), "v"(d), "s"(sb) : "memory"); }
template <int OFF> DI void gst8(char* sb, u32 vo, u32x2 d) { asm volatile("global_store_dwordx2 %0, %1, %2 offset:%3" ::"v"(vo), "v"(d), "s"(sb), "i"(OFF) : "memory"); }
DI void gadd1(char* sb, u32 vo) { asm volatile("global_atomic_add %0, %1, %2 sc1" ::"v"(vo), "v"(1u), "s"(sb) : "memory"); }
template <int OFF> DI void gst16(char* sb, u32 vo, u32x4 d) { asm volatile("global_store_dwordx4 %0, %1, %2 offset:%3\n\ts_nop 1" ::"v"(vo), "v"(d), "s"(sb), "i"(OFF) : "memory"); }
DI void gst4_sc1(char* sb, u32 vo, u32 d) { asm volatile("global_store_dword %0, %1, %2 sc1" ::"v"(vo), "v"(d), "s"(sb) : "memory"); }
DI void glds(u32 ldsaddr, const char* sb, u32 vo) { asm volatile("s_mov_b32 m0, %0\n\ts_nop 0\n\tglobal_load_lds_dwordx4 %1, %2" ::"s"(ldsaddr), "v"(vo), "s"(sb) : "memory"); }

DI bf16x8 trfrag(LAS char* p, int second) {
  s16x4 a = __builtin_amdgcn_ds_read_tr16_b64_v4i16((LAS s16x4*)p);
  s16x4 b = __builtin_amdgcn_ds_read_tr16_b64_v4i16((LAS s16x4*)(p + second));
  return __builtin_bit_cast(bf16x8, __builtin_shufflevector(a, b, 0, 1, 2, 3, 4, 5, 6, 7));
}
DI bf16x8 asb(u32x4 v) { return __builtin_bit_cast(bf16x8, v); }
DI f32x4 mfma16(bf16x8 a, bf16x8 b, f32x4 c) { return __builtin_amdgcn_mfma_f32_16x16x32_bf16(a, b, c, 0, 0, 0); }
DI f32x16 mfma(bf16x8 a, bf16x8 b, f32x16 c) { return __builtin_amdgcn_mfma_f32_32x32x16_bf16(a, b, c, 0, 0, 0); }
DI u32 pk2(float a, float b) { f32x2 v = {a, b}; return __builtin_bit_cast(u32, __builtin_convertvector(v, bf16x2)); }
DI u32 pk2v(f32x2 v) { return __builtin_bit_cast(u32, __builtin_convertvector(v, bf16x2)); }
DI u32x2 pk4(f32x4 v) { u32x2 r; r.x = pk2(v[0], v[1]); r.y = pk2(v[2], v[3]); return r; }
DI float bflo(u32 u) { return __uint_as_float(u << 16); }
DI float bfhi(u32 u) { return __uint_as_float(u & 0xffff0000u); }
DI float dot2(u32 a, u32 b, float c) { return __builtin_amdgcn_fdot2_f32_bf16(__builtin_bit_cast(bf16x2, a), __builtin_bit_cast(bf16x2, b), c, false); }
DI void swap32(float& a, float& b) { auto r = __builtin_amdgcn_permlane32_swap(__float_as_uint(a), __float_as_uint(b), false, false); a = __uint_as_float(r[0]); b = __uint_as_float(r[1]); }
DI void swap16(float& a, float& b) { auto r = __builtin_amdgcn_permlane16_swap(__float_as_uint(a), __float_as_uint(b), false, false); a = __uint_as_float(r[0]); b = __uint_as_float(r[1]); }
DI float mx2(float a, float b) { return __builtin_amdgcn_fmed3f(a, b, __builtin_inff()); }
DI float mx3(float a, float b, float c) { return __builtin_fmaxf(__builtin_fmaxf(a, b), c); }
DI void red2max(float& a, float& b) { swap32(a, b); float u = mx2(a, b); a = u; b = u; swap32(a, b); }
DI void red2sum(float& a, float& b) { swap32(a, b); float u = a + b; a = u; b = u; swap32(a, b); }
template <int CTRL> DI float dppadd(float v) { return v + __uint_as_float(__builtin_amdgcn_update_dpp(0u, __float_as_uint(v), CTRL, 0xf, 0xf, true)); }
DI float rowsum16(float v) { v = dppadd<0xB1>(v); v = dppadd<0x4E>(v); v = dppadd<0x141>(v); v = dppadd<0x140>(v); return v; }
DI f32x2 pr(const f32x16& a, int i) { return f32x2{a[2 * i], a[2 * i + 1]}; }

DI void softmax2(const f32x16& p, const float L2E  , u32x4 (&f)[2][2]) {
  float mx[2], mn[2];
#pragma unroll
  for (int hd = 0; hd < 2; ++hd) {
    const int b = 8 * hd;
    mx[hd] = mx2(mx3(mx3(mx3(p[b], p[b + 1], p[b + 2]), p[b + 3], p[b + 4]), p[b + 5], p[b + 6]), p[b + 7]);
    mn[hd] = mx2(mx3(mx3(mx3(-p[b], -p[b + 1], -p[b + 2]), -p[b + 3], -p[b + 4]), -p[b + 5], -p[b + 6]), -p[b + 7]);
    red2max(mx[hd], mn[hd]);
  }
  f32x2 e[2][2][4]; float s[2][2];
#pragma unroll
  for (int hd = 0; hd < 2; ++hd) {
    const float cp = -mx[hd] * L2E, cn = -mn[hd] * L2E;
#pragma unroll
    for (int i = 0; i < 4; ++i) {
      const f32x2 v = pr(p, 4 * hd + i), a = v * L2E + cp, b = v * -L2E + cn;
      e[hd][0][i] = f32x2{__builtin_amdgcn_exp2f(a.x), __builtin_amdgcn_exp2f(a.y)};
      e[hd][1][i] = f32x2{__builtin_amdgcn_exp2f(b.x), __builtin_amdgcn_exp2f(b.y)};
    }
#pragma unroll
    for (int sg = 0; sg < 2; ++sg) { const f32x2 t = (e[hd][sg][0] + e[hd][sg][1]) + (e[hd][sg][2] + e[hd][sg][3]); s[hd][sg] = t.x + t.y; }
    red2sum(s[hd][0], s[hd][1]);
#pragma unroll
    for (int sg = 0; sg < 2; ++sg) {
      const float r = __builtin_amdgcn_rcpf(s[hd][sg]);
#pragma unroll
      for (int i = 0; i < 4; ++i) f[hd][sg][i] = pk2v(e[hd][sg][i] * r);
    }
  }
}
DI void pack16(const f32x16& a, u32x4 (&y)[2]) {
#pragma unroll
  for (int i = 0; i < 8; ++i) y[i >> 2][i & 3] = pk2v(pr(a, i));
}
DI void lnfix(f32x16& a, LAS char* Sp, u32 negmean) { a = mfma(asb(u32x4{*(LAS u32*)Sp, 0u, 0u, 0u}), asb(u32x4{negmean, 0u, 0u, 0u}), a); }
DI u32 split2(float v) { const u32 hi = pk2(v, v) & 0xffffu; return hi | (pk2(v - bflo(hi), 0.f) << 16); }

template <int V> struct IC { static constexpr int v = V; };
template <int B, int E, class F> DI void sfor(F&& f) { if constexpr (B < E) { f(IC<B>{}); sfor<B + 1, E>(f); } }
#define SB() __builtin_amdgcn_sched_barrier(0)
template <int NT, int K0, int K1, int D, int XM, class LD, class PRE, class POST> DI void gemm(f32x16* acc, u32x4* xf, LD&& ld, PRE&& pre, POST&& post) {
  bf16x8 wf[D + 1];
  constexpr int S0 = K0 * NT, S1 = K1 * NT;
  sfor<S0, S0 + D>([&](auto I) { constexpr int s = I.v; wf[s % (D + 1)] = ld(IC<s / NT>{}, IC<s % NT>{}); });
  SB();
  sfor<S0, S1>([&](auto I) {
    constexpr int s = I.v;
    pre(I);
    if constexpr (s + D < S1) wf[(s + D) % (D + 1)] = ld(IC<(s + D) / NT>{}, IC<(s + D) % NT>{});
    SB();
    post(I);
    acc[s % NT] = mfma(wf[s % (D + 1)], asb(xf[(s / NT) % XM]), acc[s % NT]);
    asm volatile("" : "+v"(acc[s % NT]));
    SB();
  });
}
constexpr int b2slot(int t) { return t < 4 ? t * 16384 : t == 4 || t == 13 ? 131072 : t < 9 ? 65536 + (t - 5) * 16384 : (t - 9) * 16384; }
DI LAS char* opaque(LAS char* p) { asm volatile("" : "+v"(p)); return p; }

DI void cnt_wait(const char* ctl, u32 off, u32 val) {
  for (u32 sp = 0; sp < SPIN_CAP; ++sp) {
    u32 v; gld4_sc1(v, ctl, off); asm volatile("s_waitcnt vmcnt(0)" : "+v"(v)::"memory");
    if (__builtin_amdgcn_readfirstlane(v) >= val) break;
  }
}
#define HANDOFF(n) do { RAWBAR(); if (w == 0) { if (lane == 0) gadd1(ws, PCNT(layer, n)); cnt_wait(ws, PCNT(layer, n), 16u); } RAWBAR(); } while (0)

__global__ __launch_bounds__(512, 2) void mega(const char* x, const char* wqkv, const char* wphi, const char* wo, const char* w1, const char* w2, char* out, char* ws) {
  __shared__ __attribute__((aligned(16))) char smem[163840];
  LAS char* lds = (LAS char*)smem;
  const u32 lb = (u32)(size_t)lds;
  int lane = threadIdx.x & 63; int w = __builtin_amdgcn_readfirstlane(threadIdx.x >> 6);
  int hh = lane >> 5, tk = lane & 31, g = lane >> 4, t16 = lane & 15, q = (lane >> 2) & 3, p = lane & 3;
  const int bid = blockIdx.x, loc = bid >> 3;
  int P = (bid & 7) * 2 + (loc >> 4), j = loc & 15;
  int th = j >> 3, cg = j & 7, cc = w & 1, kh = (w >> 1) & 1;
  u32 l16 = lane * 16u;
  const u32x4 ones4 = {0x3f803f80u, 0x3f803f80u, 0x3f803f80u, 0x3f803f80u};
  const f32x16 z16 = {0.f, 0.f, 0.f, 0.f, 0.f, 0.f, 0.f, 0.f, 0.f, 0.f, 0.f, 0.f, 0.f, 0.f, 0.f, 0.f};
  STAMP(0);
  if (w >= 4) {
    const u32 swz = 16u * ((lane & 7) ^ ((g & 1) << 2));
    const u32 vo1024 = (lane >> 3) * 1024u + swz, vo3584 = (lane >> 3) * 3584u + swz, voA1 = (lane >> 2) * 3072u + 16u * (lane & 3);
    u32x4 wa1[16];
    u32x4 wb2[36];
    LAS char* wl = lds + l16;
#define HW (w & 3)
#define GLDS_Q(n) glds(lb + 65536 + (n) * 1024, wqkv + 64 * j + (size_t)((n) * 16) * 3072, voA1)
#define GLDS_B1(n) glds(lb + (n) * 1024, w1 + (size_t)(((n) & 63) * 8) * 3584 + j * 256 + ((n) >> 6) * 128, vo3584)
#define B2SRC(n) (w2 + cg * 128 + (size_t)(896 * (((n) >> 3) & 1) + (((n) >> 4) < 7 ? 128 * ((n) >> 4) : 128 * ((n) >> 4) - 832) + 8 * ((n) & 7)) * 1024)
#define GLDS_B2(n) glds(lb + ((n) < 64 ? (n) : (n) + 64) * 1024, B2SRC(n), vo1024)
#define GLDS_A3(n) glds(lb + LD_WA3 + (n) * 1024, wo + (size_t)(n) * 8192 + cg * 128, vo1024)
    auto putA1 = [&](auto R, auto M) { *(LAS u32x4*)(wl + R.v * 32768 + (4 * M.v + HW) * 1024) = wa1[R.v * 8 + M.v]; };
    auto putB2 = [&](auto T) { sfor<0, 4>([&](auto I) { *(LAS u32x4*)(wl + b2slot(T.v) + (4 * HW + I.v) * 1024) = wb2[(T.v - 5) * 4 + I.v]; }); };
    auto ldB2 = [&](auto T) { sfor<0, 4>([&](auto I) { gld16<0>(wb2[(T.v - 5) * 4 + I.v], B2SRC(T.v * 16 + 4 * HW + I.v), vo1024); }); };
    auto ldB1b = [&](u32x4* r) { const char* b = w1 + (size_t)(HW * 128) * 3584 + (j < 14 ? j : 13) * 256 + 128; sfor<0, 16>([&](auto I) { gld16<0>(r[I.v], b + (size_t)(I.v * 8) * 3584, vo3584); }); };
    auto putB1b = [&](u32x4* r) { sfor<0, 16>([&](auto I) { pin(r[I.v]); *(LAS u32x4*)(wl + 65536 + (HW * 16 + I.v) * 1024) = r[I.v]; }); };
    auto colsum = [&](LAS char* sp, int kstride, int second, auto K0, auto K1, f32x16& a) {
      bf16x8 wf[3];
      wf[K0.v % 3] = trfrag(sp + K0.v * kstride, second); wf[(K0.v + 1) % 3] = trfrag(sp + (K0.v + 1) * kstride, second);
      sfor<K0.v, K1.v>([&](auto K) {
        if constexpr (K.v + 2 < K1.v) wf[(K.v + 2) % 3] = trfrag(sp + (K.v + 2) * kstride, second);
        a = mfma(asb(ones4), wf[K.v % 3], a); asm volatile("" : "+v"(a)); SB();
      });
    };
    auto putS = [&](int tile, float a) { const u32 sp = split2(a); *(LAS u32*)(lds + LD_S + tile * 256 + lane * 4) = hh ? (sp >> 16) * 0x10001u : (sp & 0xffffu) * 0x10001u; };
    {
      u32 a0 = 0, sent = 0;
      if (HW == 3) {
        gld4_sc1(a0, ws, HEL_OWN + C_IN1); gst16_sc1<0>(ws + (size_t)(P * 16 + j) * 2048, l16, u32x4{0u, 0u, 0u, 0u});
      }
      const char* wb = wqkv + 64 * j + (size_t)(16 * HW) * 3072;
      __builtin_amdgcn_s_sleep(HSLEEP);
      sfor<0, 8>([&](auto M) { gld16<1024>(wa1[M.v], wb + (size_t)(64 * M.v) * 3072, voA1); gld16<2048>(wa1[8 + M.v], wb + (size_t)(64 * M.v) * 3072, voA1); });
      {
        const int id = (P * 16 + j) * 4 + HW;
        gld16<0>(wb2[0], w1 + (size_t)id * 1792, lane < 56 ? l16 : 0u); gld16<896>(wb2[1], w1 + (size_t)id * 1792, lane < 56 ? l16 : 0u);
        gld16<0>(wb2[2], w2 + (size_t)id * 1792, lane < 56 ? l16 : 0u); gld16<896>(wb2[3], w2 + (size_t)id * 1792, lane < 56 ? l16 : 0u);
        gld16<0>(wb2[4], wo + (size_t)id * 512, tk * 16u);
        const u32 ly = HW & 1, pg = lane & 7;
        const u32 to = HW < 2 ? (u32)WS_H + ly * (7u << 20) + P * 458752u + (pg < 7 ? pg : 6) * 65536u
                              : (pg < 4 ? (u32)WS_O + (pg >> 1) * (4u << 20) + ly * (2u << 20) : pg < 6 ? (u32)WS_X2 : (u32)WS_ST1 + ly * (1u << 20)) + (pg < 6 ? P * 131072u + (pg & 1) * 65536u : (pg & 1) * (2u << 20));
        gld16<0>(wb2[5], ws, to);
      }
      sfor<0, 4>([&](auto C) {
        constexpr int c = C.v;
        waitv4<18 - 4 * c>(wa1[2 * c], wa1[2 * c + 1], wa1[8 + 2 * c], wa1[8 + 2 * c + 1]);
        if (c == 0 && HW == 3) { pin1(a0); if (lane < 32) gst4_sc1(ws, HEL_PEER + C_REQ, a0); }
        putA1(IC<0>{}, IC<2 * c>{}); putA1(IC<0>{}, IC<2 * c + 1>{}); putA1(IC<1>{}, IC<2 * c>{}); putA1(IC<1>{}, IC<2 * c + 1>{});
      });
      {
        asm volatile("s_waitcnt lgkmcnt(0)" ::: "memory");
        f32x16 a0_ = z16, a1_ = z16;
        colsum(lds + HW * 1024 + TRA, 4096, 512, IC<0>{}, IC<8>{}, a0_); colsum(lds + 32768 + HW * 1024 + TRA, 4096, 512, IC<0>{}, IC<8>{}, a1_);
        *(LAS float*)(lds + LD_RS + HW * 256 + lane * 4) = hh ? a1_[0] : a0_[0];
      }
      STAMP(33);
      BAR();
      if (HW < 2) { LAS float* pp = (LAS float*)(lds + LD_RS + HW * 128 + tk * 4); putS(HW, (pp[0] + pp[64]) + (pp[128] + pp[192])); }
      BAR();
      _Pragma("unroll 4") for (int i = 0; i < 8; ++i) GLDS_Q(HW * 8 + i);
      if (HW == 3) {
        gld4_sc1(sent, ws, HEL_OWN + C_REQ); asm volatile("s_waitcnt vmcnt(0)" : "+v"(sent)::"memory");
        if (lane < 32) gst4_sc1(ws, HEL_PEER + C_IN1, sent + 1u);
        for (u32 sp_ = 0; sp_ < SPIN_CAP; ++sp_) {
          u32 v, r; gld4_sc1(v, ws, HEL_OWN + C_IN1); gld4_sc1(r, ws, HEL_OWN + C_REQ);
          asm volatile("s_waitcnt vmcnt(0)" : "+v"(v), "+v"(r)::"memory");
          if (r != sent) { sent = r; if (lane < 32) gst4_sc1(ws, HEL_PEER + C_IN1, r + 1u); }
          if (__all(v != a0)) break;
        }
      } else {
        ldB1b(wb2 + 20);
        asm volatile("s_waitcnt vmcnt(16)" ::: "memory");
      }
      sfor<0, 6>([&](auto I) { pin(wb2[I.v]); });
      STAMP(39);
      { f32x16 aS = z16; colsum(lds + 65536 + HW * 8192 + TRA, 1024, 512, IC<0>{}, IC<8>{}, aS); *(LAS float*)(lds + LD_RS + HW * 256 + lane * 4) = aS[0]; }
      BAR();
      if (HW == 2) { LAS float* pp = (LAS float*)(lds + LD_RS + tk * 4); putS(2, (pp[0] + pp[64]) + (pp[128] + pp[192])); }
      BAR();
      if (HW == 3) {
        u32 r; gld4_sc1(r, ws, HEL_OWN + C_REQ); asm volatile("s_waitcnt vmcnt(0)" : "+v"(r)::"memory");
        if (r != sent && lane < 32) gst4_sc1(ws, HEL_PEER + C_IN1, r + 1u);
        ldB1b(wb2 + 20);
      }
    }
    sfor<0, 2>([&](auto L) {
      constexpr int layer = L.v;
      FRESH();
      if constexpr (layer == 1) {
        RAWBAR();
        _Pragma("unroll 4") for (int i = 0; i < 8; ++i) GLDS_Q(HW * 8 + i);
        ldB1b(wa1);
        asm volatile("s_waitcnt vmcnt(16)" ::: "memory");
        STAMP(42);
        RAWBAR();
        RAWBAR();
      }
      _Pragma("unroll 4") for (int i = 0; i < 8; ++i) GLDS_A3(32 + HW * 8 + i);
      if (j < 14) { _Pragma("unroll 4") for (int i = 0; i < 8; ++i) GLDS_B1(HW * 16 + i); }
      STAMP(36);
      RAWBAR();
      _Pragma("unroll 4") for (int i = 0; i < 8; ++i) GLDS_A3(HW * 8 + i);
      RAWBAR();
      RAWBAR();
      DRAIN();
      RAWBAR(); RAWBAR();
      if (j < 14) { _Pragma("unroll 4") for (int i = 8; i < 16; ++i) GLDS_B1(HW * 16 + i); }
      RAWBAR();
      putB1b(layer == 0 ? wb2 + 20 : wa1);
      DRAIN();
      RAWBAR(); BAR();
      FRESH();
      if (layer == 0 && j < 14) { f32x16 aS = z16; colsum(lds + (HW >> 1) * 65536 + ((HW & 1) ? TRB1 : TRB0), 2048, 1024, IC<0>{}, IC<32>{}, aS); putS(3 + HW, aS[0]); }
      BAR();
      if constexpr (layer == 0) { ldB2(IC<5>{}); ldB2(IC<6>{}); ldB2(IC<7>{}); ldB2(IC<8>{}); ldB2(IC<9>{}); }
      STAMP(25);
      RAWBAR();
      _Pragma("unroll 4") for (int i = 0; i < 20; ++i) GLDS_B2(HW * 20 + i);
      STAMP(34);
      RAWBAR();
      if (HW == 0) { if (lane == 0) gadd1(ws, PCNT(layer, 2)); cnt_wait(ws, PCNT(layer, 2), 16u); }
      STAMP(35);
      RAWBAR();
      FRESH();
      DRAIN();
      sfor<0, 16>([&](auto I) { pin(wb2[I.v]); });
      putB2(IC<5>{}); putB2(IC<6>{});
      STAMP(38);
      RAWBAR();
      if (HW == 0 && lane == 0) gadd1(ws, PCNT(layer, 3));
      putB2(IC<7>{}); putB2(IC<8>{});
      BAR();
      if constexpr (layer == 0) { ldB2(IC<10>{}); ldB2(IC<11>{}); ldB2(IC<12>{}); ldB2(IC<13>{}); }
      if (HW == 0) cnt_wait(ws, PCNT(layer, 3), 16u);
      else if (layer == 1) { putB2(IC<9>{}); putB2(IC<10>{}); }
      STAMP(57);
      RAWBAR();
      DRAIN();
      sfor<16, 36>([&](auto I) { pin(wb2[I.v]); });
      if (layer == 0 || HW == 0) { putB2(IC<9>{}); putB2(IC<10>{}); }
      putB2(IC<11>{}); putB2(IC<12>{});
      STAMP(59);
      BAR();
      putB2(IC<13>{});
      BAR();
      if constexpr (layer == 0) sfor<0, 8>([&](auto M) { putA1(IC<0>{}, M); });
      RAWBAR();
      if constexpr (layer == 0) {
        sfor<0, 8>([&](auto M) { putA1(IC<1>{}, M); });
        RAWBAR(); BAR();
      }
    });
  } else {
    u32x4 resid[2];
    u32x4 wph[2];
    u32x4 xf[32];
    f32x16 acc[4];
    float rstd; u32 nmean;
    auto nop = [&](auto) {};
#define LDF(i, n) gld16_sc1<((n) & 3) * 1024>(xf[i], xb + ((n) >> 2) * 4096, l16)
#define FINSTATS(sum, ssq) { red2sum(sum, ssq); const float mean = sum * (1.f / 512.f); rstd = __builtin_amdgcn_rsqf(mx2(ssq * (1.f / 512.f) - mean * mean, 0.f) + 1e-5f); nmean = split2(-mean); }
#define STATS() { _Pragma("unroll") for (int i = 0; i < 4; ++i) pin(st[i]); \
      const f32x4 v = (__builtin_bit_cast(f32x4, st[0]) + __builtin_bit_cast(f32x4, st[1])) + (__builtin_bit_cast(f32x4, st[2]) + __builtin_bit_cast(f32x4, st[3])); \
      float a = v[0], b = v[1], c = v[2], d = v[3]; swap16(a, c); swap16(b, d); float sum = a + c, ssq = b + d; FINSTATS(sum, ssq) }
#define LDSTATS(SB_) { const char* sb = (SB_) + (size_t)(4 * P + w) * 4096; \
      gld16_sc1<0>(st[0], sb, l16); gld16_sc1<1024>(st[1], sb, l16); gld16_sc1<2048>(st[2], sb, l16); gld16_sc1<3072>(st[3], sb, l16); }
    constexpr int N0 = N0V;
#define STREAM32(NT, SB_) { constexpr int kb_ = s / NT; if constexpr (s % NT == 0) waitv<(N0 + kb_ < 36 ? N0 + kb_ : 36) - kb_ - 1>(xf[kb_]); \
      if constexpr (s % NT == 1 && N0 + kb_ < 32) LDF(N0 + kb_, N0 + kb_); \
      if constexpr (s % NT == 1 && N0 + kb_ >= 32 && N0 + kb_ < 36) gld16_sc1<(N0 + kb_ - 32) * 1024>(st[(N0 + kb_) & 3], (SB_) + (size_t)(4 * P + w) * 4096, l16); }
#define RES2ACC() { _Pragma("unroll") for (int i = 0; i < 8; ++i) { const u32 r = resid[i >> 2][i & 3]; acc[0][2 * i] = bflo(r); acc[0][2 * i + 1] = bfhi(r); } acc[1] = z16; asm volatile("" : "+v"(acc[0])); }
    for (int layer = 0; layer < 2; ++layer) {
      FRESH();
      acc[0] = z16; acc[1] = z16; acc[2] = z16;
      LAS char* wa1p = opaque(lds + TRA);
      auto ldkv = [&](auto KB, auto T) { return trfrag(wa1p + T.v * 32768 + KB.v * 1024, 512); };
      if (layer == 0) {
        const char* xb = x + (size_t)(128 * P + 32 * w) * 1024;
        u32 xvo[4];
#pragma unroll
        for (int i = 0; i < 4; ++i) xvo[i] = g * 1024u + ((t16 ^ (4 * i + g)) * 16u);
        const u32 stg = lb + 65536 + w * 16384;
        auto ldx = [&](auto R) { sfor<0, 8>([&](auto I) { glds(stg + (R.v & 1) * 8192 + I.v * 1024, xb + R.v * 256 + I.v * 4096, xvo[I.v & 3]); }); };
        ldx(IC<0>{}); ldx(IC<1>{});
        sfor<16, 32>([&](auto I) { gld16<(I.v >> 3) * 256>(xf[I.v], xb + (I.v & 7) * 4096, xvo[I.v & 3]); });
        u32 wp[8];
        {
          const u16* wsrc = (const u16*)wphi + (2 * j + (tk >> 4)) * 256 + (4 * hh) * 16 + t16;
#pragma unroll
          for (int i = 0; i < 8; ++i) asm volatile("global_load_ushort %0, %1, off offset:%2" : "=v"(wp[i]) : "v"(wsrc), "i"((i >> 2) * 256 + (i & 3) * 32) : "memory");
        }
        STAMP(32);
        const u32 str = 65536 + w * 16384 + tk * 256 + t16 * 16 + 8 * hh;
        float sm[4] = {0.f, 0.f, 0.f, 0.f}, sq[4] = {0.f, 0.f, 0.f, 0.f};
        sfor<0, 4>([&](auto R) {
          constexpr int b = 8 * R.v;
          asm volatile("s_waitcnt vmcnt(%0)" ::"i"(32 - b) : "memory");
          if constexpr (R.v >= 2) {
            waitl8(xf + b);
#pragma unroll
            for (int i = 0; i < 8; ++i) *(LAS u32x4*)(lds + 65536 + w * 16384 + (R.v & 1) * 8192 + i * 1024 + l16) = xf[b + i];
          }
#pragma unroll
          for (int i = 0; i < 8; ++i) {
            u32x2 lo = *(LAS u32x2*)(lds + ((str ^ (32 * i)) + (R.v & 1) * 8192)), hi = *(LAS u32x2*)(lds + ((str ^ (32 * i + 16)) + (R.v & 1) * 8192));
            xf[b + i] = u32x4{lo.x, lo.y, hi.x, hi.y};
          }
          if ((cg >> 1) == R.v && (w >> 1) == th) {
            LAS char* rx = lds + LD_SC + cc * 4096 + l16;
            if (cg & 1) { _Pragma("unroll") for (int i = 0; i < 4; ++i) *(LAS u32x4*)(rx + i * 1024) = xf[b + 4 + i]; }
            else { _Pragma("unroll") for (int i = 0; i < 4; ++i) *(LAS u32x4*)(rx + i * 1024) = xf[b + i]; }
          }
          STAMP(29 + (R.v == 3 ? 2 : R.v));
        });
        sfor<0, 32>([&](auto I) {
#pragma unroll
          for (int e = 0; e < 4; ++e) { sm[e] = dot2(xf[I.v][e], 0x3f803f80u, sm[e]); sq[e] = dot2(xf[I.v][e], xf[I.v][e], sq[e]); }
        });
        float sum = (sm[0] + sm[1]) + (sm[2] + sm[3]), ssq = (sq[0] + sq[1]) + (sq[2] + sq[3]);
        FINSTATS(sum, ssq)
        STAMP(33);
        BAR();
        STAMP(54);
        resid[0] = *(LAS u32x4*)(lds + LD_SC + cc * 4096 + kh * 2048 + l16); resid[1] = *(LAS u32x4*)(lds + LD_SC + cc * 4096 + kh * 2048 + 1024 + l16);
        pin(resid[0]); pin(resid[1]);
        gemm<2, 0, 32, 4, 32>(acc, xf, ldkv, [&](auto I) { if constexpr (I.v == 8) RAWBAR();   }, nop);
        STAMP(39);
        DRAIN();
#pragma unroll
        for (int i = 0; i < 8; ++i) pin1(wp[i]);
        const u32x4 wv = {wp[0] | (wp[1] << 16), wp[2] | (wp[3] << 16), wp[4] | (wp[5] << 16), wp[6] | (wp[7] << 16)};
        const u32x4 zz = {0u, 0u, 0u, 0u};
        wph[0] = tk < 16 ? wv : zz; wph[1] = tk < 16 ? zz : wv;
        pin(resid[0]); pin(resid[1]);
      } else {
        STAMP(40);
        u32x4 st[4];
        const char* xb = ws + WS_X2 + (size_t)(4 * P + w) * 32768;
        sfor<0, N0>([&](auto I) { LDF(I.v, I.v); });
        STAMP(41);
        gemm<2, 0, 32, 4, 32>(acc, xf, ldkv, [&](auto I) {
          constexpr int s = I.v;
          if constexpr (s == 2 * (36 - N0)) { STAMP(44); RAWBAR(); STAMP(45); }
          STREAM32(2, ws + WS_ST2)
        }, nop);
        DRAIN();
        STATS()
      }
      STAMP(1 + 12 * layer);
      FRESH();
      {
        u32x4 yk[2], yv[2], f[2][2];
        lnfix(acc[0], lds + LD_S + lane * 4, nmean); lnfix(acc[1], lds + LD_S + 256 + lane * 4, nmean);
        pack16(acc[0], yk);
        f32x16 pp = mfma(asb(wph[0]), asb(yk[0]), z16); pp = mfma(asb(wph[1]), asb(yk[1]), pp);
        pack16(acc[1] * rstd, yv);
        LAS char* kt = lds + LD_SC + (32 * w + tk) * 200 + 8 * hh;
#pragma unroll
        for (int hd = 0; hd < 2; ++hd) { *(LAS u32x2*)(kt + hd * 96 + 64) = u32x2{yv[hd].x, yv[hd].y}; *(LAS u32x2*)(kt + hd * 96 + 80) = u32x2{yv[hd].z, yv[hd].w}; }
        softmax2(pp, rstd * 1.4426950408889634f, f);
#pragma unroll
        for (int hd = 0; hd < 2; ++hd) {
          *(LAS u32x2*)(kt + hd * 96) = u32x2{f[hd][0].x, f[hd][0].y}; *(LAS u32x2*)(kt + hd * 96 + 16) = u32x2{f[hd][0].z, f[hd][0].w};
          *(LAS u32x2*)(kt + hd * 96 + 32) = u32x2{f[hd][1].x, f[hd][1].y}; *(LAS u32x2*)(kt + hd * 96 + 48) = u32x2{f[hd][1].z, f[hd][1].w};
        }
      }
      {
        LAS char* ka = lds + LD_SC + (4 * g + q) * 200 + 8 * p + (w >> 1) * 96;
        char* kb_ = ws + WS_KVP + (size_t)((layer * 16 + j) * 4 + w) * KVP_UNIT;
        asm volatile("" : "+v"(ka)); asm volatile("" : "+s"(kb_));
        STAMP(42);
        BAR();
        STAMP(43);
        f32x4 akv = {0.f, 0.f, 0.f, 0.f}, az = {0.f, 0.f, 0.f, 0.f};
        bf16x8 A[4], B[4];
#pragma unroll
        for (int ks = 0; ks < 4; ++ks) { A[ks] = trfrag(ka + ks * 6400 + (w & 1) * 32, 3200); B[ks] = trfrag(ka + ks * 6400 + 64, 3200); }
        SB();
#pragma unroll
        for (int ks = 0; ks < 4; ++ks) { akv = mfma16(A[ks], B[ks], akv); az = mfma16(A[ks], asb(ones4), az); }
        {
          const u32x2 kp = pk4(akv);
          auto r0 = __builtin_amdgcn_permlane32_swap(kp.x, kp.x, false, false), r1 = __builtin_amdgcn_permlane32_swap(kp.y, kp.y, false, false);
          if (lane < 32) gst16_sc1<0>(kb_ + P * 512, l16, u32x4{r0[0], r1[0], r0[1], r1[1]});
        }
        asm volatile("s_nop 7" : "+v"(az));
        if (t16 == 0) gst16_sc1<0>(kb_ + 8192 + P * 64, g * 16u, __builtin_bit_cast(u32x4, az));
        DRAIN();
      }
      STAMP(37);
      RAWBAR();
      if (w == 0 && lane == 0) gadd1(ws, KCNT(layer));
      STAMP(2 + 12 * layer);
      u32x4 phq[2][2];
      {
        LAS char* wq = opaque(lds + 65536 + TRA);
        gemm<1, 0, 32, 4, 32>(acc + 2, xf, [&](auto KB, auto) { return trfrag(wq + KB.v * 1024, 512); }, nop, nop);
        STAMP(36);
        RAWBAR();
        STAMP(46);
        u32x4 yq[2];
        lnfix(acc[2], lds + LD_S + 512 + lane * 4, nmean);
        pack16(acc[2], yq);
        f32x16 pp = mfma(asb(wph[0]), asb(yq[0]), z16); pp = mfma(asb(wph[1]), asb(yq[1]), pp);
        softmax2(pp, rstd * 1.4426950408889634f, phq);
      }
      STAMP(47);
      FRESH();
      if (w == 0) cnt_wait(ws, KCNT(layer), 16u);
      RAWBAR();
      STAMP(3 + 12 * layer);
      {
        const char* xb = ws + WS_KVP + (size_t)((layer * 16 + j) * 4 + w) * KVP_UNIT;
        u32x4 zz;
        gld16_sc1<0>(zz, xb + 8192, t16 * 64u + g * 16u);
        sfor<0, 8>([&](auto I) { LDF(I.v, I.v); });
        RES2ACC()
        LAS char* gi = lds + LD_SC + 16384 + w * 680 + 8 * g;
        LAS char* gr = lds + LD_SC + 16384 + (tk < 16 ? tk : 16) * 40 + 8 * hh;
        char* ob = ws + WS_O + (size_t)layer * (2u << 20) + ((size_t)(4 * P + w) * 32 + 2 * j) * 1024;
        asm volatile("" : "+v"(gi), "+v"(gr)); asm volatile("" : "+s"(ob));
        waitv<8>(zz);
        f32x4 sz = __builtin_bit_cast(f32x4, zz);
#pragma unroll
        for (int e = 0; e < 4; ++e) sz[e] = rowsum16(sz[e]);
        if (t16 == 0) *(LAS u32x2*)(gi + 640) = pk4(sz);
        float sa[8] = {0.f, 0.f, 0.f, 0.f, 0.f, 0.f, 0.f, 0.f};
        u32 c_lo = 0x00003f80u, c_hi = 0x3f800000u; asm volatile("" : "+s"(c_lo), "+s"(c_hi));
        sfor<0, 8>([&](auto I) {
          waitv<7 - I.v>(xf[I.v]);
#pragma unroll
          for (int e = 0; e < 4; ++e) { sa[2 * e] = dot2(xf[I.v][e], c_lo, sa[2 * e]); sa[2 * e + 1] = dot2(xf[I.v][e], c_hi, sa[2 * e + 1]); }
        });
        f32x4 s0;
#pragma unroll
        for (int e = 0; e < 4; ++e) { swap32(sa[e], sa[4 + e]); s0[e] = sa[e] + sa[4 + e]; }
        *(LAS u32x2*)(gi + t16 * 40) = pk4(s0);
        STAMP(48);
        BAR();
        STAMP(49);
        f32x16 o[2];
        sfor<0, 2>([&](auto HD) {
          constexpr int hd = HD.v;
          u32x2 a = *(LAS u32x2*)(gr + hd * 1360), b = *(LAS u32x2*)(gr + hd * 1360 + 16), c = *(LAS u32x2*)(gr + hd * 1360 + 680), d = *(LAS u32x2*)(gr + hd * 1360 + 696);
          o[hd] = mfma(asb(u32x4{a.x, a.y, b.x, b.y}), asb(phq[hd][0]), z16);
          o[hd] = mfma(asb(u32x4{c.x, c.y, d.x, d.y}), asb(phq[hd][1]), o[hd]);
        });
        sfor<0, 2>([&](auto HD) {
          constexpr int hd = HD.v;
          const float r = __builtin_amdgcn_rcpf(o[hd][8] + 1e-6f);
          gst16_sc1<hd * 1024>(ob, l16, u32x4{pk2v(pr(o[hd], 0) * r), pk2v(pr(o[hd], 1) * r), pk2v(pr(o[hd], 2) * r), pk2v(pr(o[hd], 3) * r)});
        });
        STAMP(50);
        DRAIN();
        STAMP(4 + 12 * layer);
      }
#define TILE_REDUCE(EXOFF) { LAS char* ex = lds + (EXOFF) + l16; \
        _Pragma("unroll") for (int i = 0; i < 4; ++i) *(LAS f32x4*)(ex + w * 4096 + i * 1024) = f32x4{acc[1][4 * i], acc[1][4 * i + 1], acc[1][4 * i + 2], acc[1][4 * i + 3]}; \
        BAR(); \
        _Pragma("unroll") for (int i = 0; i < 4; ++i) { const f32x4 v = *(LAS f32x4*)(ex + (w ^ 2) * 4096 + i * 1024) + f32x4{acc[0][4 * i], acc[0][4 * i + 1], acc[0][4 * i + 2], acc[0][4 * i + 3]}; \
          resid[i >> 1][2 * (i & 1)] = pk2(v[0], v[1]); resid[i >> 1][2 * (i & 1) + 1] = pk2(v[2], v[3]); } }
#define TILE_PUBLISH(xo, so) { gst16_sc1<0>(xo, l16, resid[0]); gst16_sc1<1024>(xo, l16, resid[1]); \
        float sum = 0.f, ssq = 0.f, sum2 = 0.f, ssq2 = 0.f; \
        _Pragma("unroll") for (int e = 0; e < 4; ++e) { sum = dot2(resid[0][e], 0x3f803f80u, sum); ssq = dot2(resid[0][e], resid[0][e], ssq); sum2 = dot2(resid[1][e], 0x3f803f80u, sum2); ssq2 = dot2(resid[1][e], resid[1][e], ssq2); } \
        sum += sum2; ssq += ssq2; red2sum(sum, ssq); sum2 = sum; ssq2 = ssq; swap16(sum, sum2); swap16(ssq, ssq2); \
        if (lane < 16) gst16_sc1<0>(so, l16, u32x4{__float_as_uint(sum), __float_as_uint(ssq), __float_as_uint(sum2), __float_as_uint(ssq2)}); \
        DRAIN(); }
      FRESH();
      {
        const size_t ch = (size_t)(4 * P + 2 * th + cc);
        const char* xb = ws + WS_O + (size_t)layer * (2u << 20) + (ch * 32 + 16 * kh) * 1024;
        char* xo = ws + WS_X1 + (size_t)layer * (2u << 20) + (ch * 32 + 4 * cg + 2 * kh) * 1024;
        char* so = ws + WS_ST1 + (size_t)layer * (256u << 10) + (ch * 16 + 2 * cg + kh) * 256;
        LAS char* wa[2] = {opaque(lds + LD_WA3 + kh * 32768 + (TRB0 ^ (64 * kh))), opaque(lds + LD_WA3 + kh * 32768 + (TRB1 ^ (64 * kh)))};
        asm volatile("" : "+s"(xb), "+s"(xo), "+s"(so));
        HANDOFF(0);
        STAMP(5 + 12 * layer);
        sfor<0, N0>([&](auto I) { LDF(I.v, I.v); });
        STAMP(51);
        gemm<2, 0, 16, 4, 32>(acc, xf, [&](auto KB, auto T) { return trfrag(wa[T.v] + KB.v * 2048, 1024); }, [&](auto I) {
          constexpr int s = I.v;
          if constexpr (s % 2 == 0) waitv<(N0 + s / 2 < 16 ? N0 + s / 2 : 16) - s / 2 - 1>(xf[s / 2]);
          if constexpr (s % 2 == 1 && N0 + s / 2 < 16) LDF(N0 + s / 2, N0 + s / 2);
        }, nop);
        STAMP(52);
        TILE_REDUCE(LD_SC)
        STAMP(53);
        TILE_PUBLISH(xo, so)
        STAMP(6 + 12 * layer);
      }
      FRESH();
      {
        const char* xb = ws + WS_X1 + (size_t)layer * (2u << 20) + (size_t)(4 * P + w) * 32768;
        LAS char* wa[4] = {opaque(lds + TRB0), opaque(lds + TRB1), opaque(lds + 65536 + TRB0), opaque(lds + 65536 + TRB1)};
        asm volatile("" : "+s"(xb));
        HANDOFF(1);
        STAMP(7 + 12 * layer);
        if (j < 14) {
          u32x4 st[4];
          sfor<0, N0>([&](auto I) { LDF(I.v, I.v); });
          STAMP(28);
          acc[0] = z16; acc[1] = z16; acc[2] = z16; acc[3] = z16;
          gemm<2, 0, 32, 4, 32>(acc, xf, [&](auto KB, auto T) { return trfrag(wa[T.v] + KB.v * 2048, 1024); }, [&](auto I) {
            constexpr int s = I.v;
            if constexpr (s == 2 * (36 - N0)) RAWBAR();
            STREAM32(2, ws + WS_ST1 + (size_t)layer * (256u << 10))
            if constexpr (s == 60) DRAIN();
          }, [&](auto I) { if constexpr (I.v == 60) { STATS() asm volatile("" : "+v"(rstd), "+v"(nmean)); } });
          LAS char* Sp = lds + LD_S + 768 + lane * 4;
          lnfix(acc[0], Sp, nmean); lnfix(acc[1], Sp + 256, nmean);
          STAMP(25);
          RAWBAR();
          STAMP(26);
          char* hb = ws + WS_H + (size_t)layer * (7u << 20) + ((size_t)(4 * P + w) * 112 + 8 * j) * 1024;
          const float c1 = rstd * -1.4426950408889634f, sigma = __builtin_amdgcn_rcpf(rstd);
          float tt[8]; u32x4 hp;
          auto uop = [&](auto M) {
            constexpr int st_ = M.v % 6, e = M.v / 6 - st_, t = e / 16, r = e % 16;
            if constexpr (e >= 0 && e < 64) {
              if constexpr (st_ == 0) tt[e & 7] = acc[t][r] * c1;
              if constexpr (st_ == 1) tt[e & 7] = __builtin_amdgcn_exp2f(tt[e & 7]);
              if constexpr (st_ == 2) tt[e & 7] = __builtin_fmaf(tt[e & 7], sigma, sigma);
              if constexpr (st_ == 3) tt[e & 7] = __builtin_amdgcn_rcpf(tt[e & 7]);
              if constexpr (st_ == 4) tt[e & 7] = acc[t][r] * tt[e & 7];
              if constexpr (st_ < 5) asm volatile("" : "+v"(tt[e & 7]));
              if constexpr (st_ == 5 && (e & 1)) {
                u32 pkd = pk2(tt[(e - 1) & 7], tt[e & 7]); pin1(pkd); hp[(e >> 1) & 3] = pkd;
                if constexpr ((e & 7) == 7) gst16_sc1<((e >> 3) & 3) * 1024>(hb + (e >> 5) * 4096, l16, hp);
              }
            }
          };
          constexpr int UB = 32 * 6, UC = 48 * 6;
          gemm<1, 0, 32, 4, 32>(acc + 2, xf, [&](auto KB, auto) { return trfrag(wa[2] + KB.v * 2048, 1024); }, nop, [&](auto I) { if constexpr (I.v >= 1) sfor<(I.v - 1) * UB / 31, I.v * UB / 31>(uop); });
          lnfix(acc[2], Sp + 512, nmean);
          STAMP(27);
          gemm<1, 0, 32, 4, 32>(acc + 3, xf, [&](auto KB, auto) { return trfrag(wa[3] + KB.v * 2048, 1024); },
                                [&](auto I) { if constexpr (I.v == 18) { STAMP(34); DRAIN(); RAWBAR();   } },
                                [&](auto I) { constexpr int i = I.v, u0 = i < 6 ? 5 * i : 30 + (i - 6) * (UC - UB - 30) / 26, u1 = i + 1 < 6 ? 5 * (i + 1) : 30 + (i - 5) * (UC - UB - 30) / 26; sfor<UB + u0, UB + u1>(uop); });
          lnfix(acc[3], Sp + 768, nmean);
          STAMP(8 + 12 * layer);
          sfor<UC, 70 * 6>(uop);
          STAMP(35);
          RAWBAR();
          STAMP(55);
        } else { RAWBAR(); RAWBAR(); RAWBAR(); RAWBAR(); }
      }
      FRESH();
      {
        const size_t ch = (size_t)(4 * P + 2 * th + cc);
        const char* xb = ws + WS_H + (size_t)layer * (7u << 20) + (ch * 112 + 56 * kh) * 1024;
        const int t0 = kh * 8192 + (TRB0 ^ (64 * kh)), t1 = t0 ^ 64;
        LAS char* wa[6] = {opaque(lds + t0), opaque(lds + t1), opaque(lds + 65536 + t0), opaque(lds + 65536 + t1), opaque(lds + 131072 + t0), opaque(lds + 131072 + t1)};
        asm volatile("" : "+s"(xb));
        RES2ACC()
        DRAIN();
        STAMP(9 + 12 * layer);
        RAWBAR();
        STAMP(10 + 12 * layer);
#define LDH(i) LDF((i) % 32, (i) < 28 ? 8 * ((i) / 4) + (i) % 4 : 8 * (((i) - 28) / 4) + 4 + (i) % 4)
        constexpr int XB = 44;
        sfor<0, N0>([&](auto I) { LDH(I.v); });
        STAMP(56);
        gemm<2, 0, 56, 4, 32>(acc, xf, [&](auto KB, auto T) { constexpr int so = b2slot(KB.v >> 2); return trfrag(wa[(so >> 16) * 2 + T.v] + (so & 65535) + (KB.v & 3) * 2048, 1024); }, [&](auto I) {
          constexpr int s = I.v, kb = s / 2;
          if constexpr (s == XB) STAMP(57);
          if constexpr (s == 68) STAMP(59);
          if constexpr (s == 100) STAMP(61);
          if constexpr (s == 32 || s == XB || s == 68 || s == 100) RAWBAR();
          if constexpr (s == XB) STAMP(58);
          if constexpr (s == 68) STAMP(60);
          constexpr int KX = XB / 2, NL = KX + N0 - 28;
          constexpr int iss = kb <= KX ? (N0 + kb < 28 ? N0 + kb : 28) : (N0 + kb < 56 ? N0 + kb : 56);
          if constexpr (s % 2 == 0) waitv<iss - kb - 1>(xf[kb % 32]);
          if constexpr (s == XB) sfor<28, 28 + NL>([&](auto J) { LDH(J.v); });
          if constexpr (s % 2 == 1 && (kb < KX ? N0 + kb < 28 : N0 + kb < 56)) LDH(N0 + kb);
        }, nop);
        STAMP(11 + 12 * layer);
        TILE_REDUCE(98304)
        if (layer == 0) {
          char* xo = ws + WS_X2 + (ch * 32 + 4 * cg + 2 * kh) * 1024;
          char* so = ws + WS_ST2 + (ch * 16 + 2 * cg + kh) * 256;
          TILE_PUBLISH(xo, so)
          STAMP(12);
          HANDOFF(4);
        } else {
          char* ob = out + (size_t)(128 * P + 64 * th + 32 * cc) * 1024 + 128 * cg + 64 * kh;
          sfor<0, 2>([&](auto S) {
            auto r0 = __builtin_amdgcn_permlane32_swap(resid[S.v].x, resid[S.v].z, false, false);
            auto r1 = __builtin_amdgcn_permlane32_swap(resid[S.v].y, resid[S.v].w, false, false);
            gst16<32 * S.v>(ob, tk * 1024u + 16u * hh, u32x4{r0[0], r1[0], r0[1], r1[1]});
          });
        }
      }
    }
  }
}

extern "C" void kernel_launch(void* const* d_in, const int* in_sizes, int n_in, void* d_out, int out_size, void* d_ws, size_t ws_size, hipStream_t stream) {
  mega<<<256, 512, 0, stream>>>((const char*)d_in[0], (const char*)d_in[1], (const char*)d_in[2], (const char*)d_in[3], (const char*)d_in[5], (const char*)d_in[6], (char*)d_out, (char*)d_ws);
}
```

```cpp
#include <hip/hip_runtime.h>
#include <hip/hip_bf16.h>

typedef unsigned u32;
typedef unsigned short u16;
typedef u32 u32x4 __attribute__((ext_vector_type(4)));
typedef u32 u32x2 __attribute__((ext_vector_type(2)));
typedef float f32x16 __attribute__((ext_vector_type(16)));
typedef float f32x4 __attribute__((ext_vector_type(4)));
typedef float f32x2 __attribute__((ext_vector_type(2)));
typedef short s16x4 __attribute__((ext_vector_type(4)));
typedef __bf16 bf16x2 __attribute__((ext_vector_type(2)));
typedef __bf16 bf16x8 __attribute__((ext_vector_type(8)));
#define LAS __attribute__((address_space(3)))
#define DI __device__ __forceinline__

constexpr size_t WS_CTL = 0;
constexpr size_t WS_ST1 = 512u << 10;
constexpr size_t WS_ST2 = 1024u << 10;
constexpr size_t WS_KVP = 1280u << 10;
constexpr u32 KVP_UNIT = 9216;
constexpr size_t WS_O = 4u << 20;
constexpr size_t WS_X1 = 8u << 20;
constexpr size_t WS_X2 = 12u << 20;
constexpr size_t WS_H = 14u << 20;
constexpr size_t WS_STAMP = 40u << 20;
constexpr u32 C_HEL = 1024;

constexpr u32 LD_WA3 = 64 * 1024;
constexpr u32 LD_SC = 128 * 1024;
constexpr u32 LD_S = 153 * 1024;
constexpr u32 LD_STAMP = 157 * 1024;
constexpr u32 LD_ID = 159 * 1024 + 512;
#define TOUCH 1
#define HSLEEP 14
#define A3SLEEP 12
#define PFX 1
#define PFW 0
#define NEARLY 0
#define PFC 44
#define BSSTEP 56
#define HPRIO 3
#define PSLEEP 0
#define REP 16
#define KSTEP 12
#define NW4 2
#define NW6 2
#define A3PRE 4
#define N0V 12
constexpr u32 SPIN_CAP = 1u << 17;

#define BAR() do { asm volatile("s_waitcnt lgkmcnt(0)" ::: "memory"); __builtin_amdgcn_s_barrier(); asm volatile("" ::: "memory"); } while (0)
#define RAWBAR() do { asm volatile("" ::: "memory"); __builtin_amdgcn_s_barrier(); asm volatile("" ::: "memory"); } while (0)
#define FRESH() do { asm volatile("" : "+s"(ws), "+s"(P), "+s"(j), "+s"(w), "+s"(x), "+s"(wqkv), "+s"(wo), "+s"(w1), "+s"(w2)); asm volatile("" : "+v"(lane)); th = j >> 3; cg = j & 7; cc = w & 1; kh = (w >> 1) & 1; \
  hh = lane >> 5; tk = lane & 31; g = lane >> 4; t16 = lane & 15; q = (lane >> 2) & 3; p = lane & 3; l16 = lane * 16u; } while (0)
#define TRB0 ((4 * hh + q) * 128 + 64 * (q >> 1) + 32 * (g & 1) + 8 * p)
#define TRB1 (TRB0 ^ 64)
#define TRA ((4 * hh + q) * 64 + 32 * (g & 1) + 8 * p)
#define HEL_OWN ((u32)(P * 16 + j) * 2048u + C_HEL + 8u * tk)
#define HEL_PEER (C_HEL + (tk < 16 ? (u32)(P * 16 + tk) * 2048u + 8u * j : (u32)((tk - 16) * 16 + j) * 2048u + 128u + 8u * P))
#define PSIG(n) do { if (lane < REP) gadd1(ws, (u32)(P * 16 + lane * (16 / REP)) * 2048u + (n) * 128u); } while (0)
#define PWAIT(n) cnt_wait(ws, (u32)(P * 16 + (j & -(16 / REP))) * 2048u + (n) * 128u, 16u * (layer + 1))
#define KOFF(PP) ((u32)((PP) * 16 + j) * 2048u + 1536u + w * 128u)
#define DRAIN() asm volatile("s_waitcnt vmcnt(0)" ::: "memory")

#define STAMP(i) do {} while (0)

template <int N> DI void waitv(u32x4& a) { asm volatile("s_waitcnt vmcnt(%1)" : "+v"(a) : "i"(N)); }
template <int N> DI void waitv4(u32x4& a, u32x4& b, u32x4& c, u32x4& d) { asm volatile("s_waitcnt vmcnt(%4)" : "+v"(a), "+v"(b), "+v"(c), "+v"(d) : "i"(N)); }
DI void waitl8(u32x4* f) { asm volatile("" : "+v"(f[0]), "+v"(f[1]), "+v"(f[2]), "+v"(f[3]), "+v"(f[4]), "+v"(f[5]), "+v"(f[6]), "+v"(f[7])::"memory"); }
DI void pin(u32x4& a) { asm volatile("" : "+v"(a)); }
DI void pin2(u32x2& a) { asm volatile("" : "+v"(a)); }
DI void pin1(u32& a) { asm volatile("" : "+v"(a)); }

template <int OFF> DI void gld16_sc1(u32x4& d, const char* sb, u32 vo) { asm volatile("global_load_dwordx4 %0, %1, %2 offset:%3 sc1" : "=v"(d) : "v"(vo), "s"(sb), "i"(OFF) : "memory"); }
template <int OFF> DI void gld16(u32x4& d, const char* sb, u32 vo) { asm volatile("global_load_dwordx4 %0, %1, %2 offset:%3" : "=v"(d) : "v"(vo), "s"(sb), "i"(OFF) : "memory"); }
template <int OFF> DI void gld8(u32x2& d, const char* sb, u32 vo) { asm volatile("global_load_dwordx2 %0, %1, %2 offset:%3" : "=v"(d) : "v"(vo), "s"(sb), "i"(OFF) : "memory"); }
DI void gld8_sc1(u32x2& d, const char* sb, u32 vo) { asm volatile("global_load_dwordx2 %0, %1, %2 sc1" : "=v"(d) : "v"(vo), "s"(sb) : "memory"); }
template <int OFF> DI void gld4o_sc1(u32& d, const char* sb, u32 vo) { asm volatile("global_load_dword %0, %1, %2 offset:%3 sc1" : "=v"(d) : "v"(vo), "s"(sb), "i"(OFF) : "memory"); }
DI void gld4_sc1(u32& d, const char* sb, u32 vo) { asm volatile("global_load_dword %0, %1, %2 sc1" : "=v"(d) : "v"(vo), "s"(sb) : "memory"); }
template <int OFF> DI void gst16_sc1(char* sb, u32 vo, u32x4 d) { asm volatile("global_store_dwordx4 %0, %1, %2 offset:%3 sc1\n\ts_nop 1" ::"v"(vo), "v"(d), "s"(sb), "i"(OFF) : "memory"); }
DI void gst8_sc1(char* sb, u32 vo, u32x2 d) { asm volatile("global_store_dwordx2 %0, %1, %2 sc1" ::"v"(vo), "v"(d), "s"(sb) : "memory"); }
template <int OFF> DI void gst8(char* sb, u32 vo, u32x2 d) { asm volatile("global_store_dwordx2 %0, %1, %2 offset:%3" ::"v"(vo), "v"(d), "s"(sb), "i"(OFF) : "memory"); }
DI void gadd1(char* sb, u32 vo) { asm volatile("global_atomic_add %0, %1, %2 sc1" ::"v"(vo), "v"(1u), "s"(sb) : "memory"); }
#define OUTMOD ""
template <int OFF> DI void gst16(char* sb, u32 vo, u32x4 d) { asm volatile("global_store_dwordx4 %0, %1, %2 offset:%3 " OUTMOD "\n\ts_nop 1" ::"v"(vo), "v"(d), "s"(sb), "i"(OFF) : "memory"); }
DI void gst4_sc1(char* sb, u32 vo, u32 d) { asm volatile("global_store_dword %0, %1, %2 sc1" ::"v"(vo), "v"(d), "s"(sb) : "memory"); }
DI void glds(u32 ldsaddr, const char* sb, u32 vo) { asm volatile("s_mov_b32 m0, %0\n\ts_nop 0\n\tglobal_load_lds_dwordx4 %1, %2" ::"s"(ldsaddr), "v"(vo), "s"(sb) : "memory"); }
DI void touch3(u32 ldsaddr, const char* a) { a += ((3u - ((u32)(size_t)a >> 7)) & 7u) << 7; asm volatile("s_mov_b32 m0, %0\n\ts_nop 0\n\tglobal_load_lds_dword %1, off" ::"s"(ldsaddr), "v"(a) : "memory"); }

DI bf16x8 trfrag(LAS char* p, int second) {
  s16x4 a = __builtin_amdgcn_ds_read_tr16_b64_v4i16((LAS s16x4*)p);
  s16x4 b = __builtin_amdgcn_ds_read_tr16_b64_v4i16((LAS s16x4*)(p + second));
  return __builtin_bit_cast(bf16x8, __builtin_shufflevector(a, b, 0, 1, 2, 3, 4, 5, 6, 7));
}
DI bf16x8 asb(u32x4 v) { return __builtin_bit_cast(bf16x8, v); }
DI f32x4 mfma16(bf16x8 a, bf16x8 b, f32x4 c) { return __builtin_amdgcn_mfma_f32_16x16x32_bf16(a, b, c, 0, 0, 0); }
DI f32x16 mfma(bf16x8 a, bf16x8 b, f32x16 c) { return __builtin_amdgcn_mfma_f32_32x32x16_bf16(a, b, c, 0, 0, 0); }
DI u32 pk2(float a, float b) { f32x2 v = {a, b}; return __builtin_bit_cast(u32, __builtin_convertvector(v, bf16x2)); }
DI u32 pk2v(f32x2 v) { return __builtin_bit_cast(u32, __builtin_convertvector(v, bf16x2)); }
DI u32x2 pk4(f32x4 v) { u32x2 r; r.x = pk2(v[0], v[1]); r.y = pk2(v[2], v[3]); return r; }
DI float bflo(u32 u) { return __uint_as_float(u << 16); }
DI float bfhi(u32 u) { return __uint_as_float(u & 0xffff0000u); }
DI float dot2(u32 a, u32 b, float c) { return __builtin_amdgcn_fdot2_f32_bf16(__builtin_bit_cast(bf16x2, a), __builtin_bit_cast(bf16x2, b), c, false); }
DI void swap32(float& a, float& b) { auto r = __builtin_amdgcn_permlane32_swap(__float_as_uint(a), __float_as_uint(b), false, false); a = __uint_as_float(r[0]); b = __uint_as_float(r[1]); }
DI void swap16(float& a, float& b) { auto r = __builtin_amdgcn_permlane16_swap(__float_as_uint(a), __float_as_uint(b), false, false); a = __uint_as_float(r[0]); b = __uint_as_float(r[1]); }
DI float mx2(float a, float b) { return __builtin_amdgcn_fmed3f(a, b, __builtin_inff()); }
DI float mx3(float a, float b, float c) { return __builtin_fmaxf(__builtin_fmaxf(a, b), c); }
DI void red2max(float& a, float& b) { swap32(a, b); float u = mx2(a, b); a = u; b = u; swap32(a, b); }
DI void red2sum(float& a, float& b) { swap32(a, b); float u = a + b; a = u; b = u; swap32(a, b); }
template <int CTRL> DI float dppadd(float v) { return v + __uint_as_float(__builtin_amdgcn_update_dpp(0u, __float_as_uint(v), CTRL, 0xf, 0xf, true)); }
DI float rowsum16(float v) { v = dppadd<0xB1>(v); v = dppadd<0x4E>(v); v = dppadd<0x141>(v); v = dppadd<0x140>(v); return v; }
DI f32x2 pr(const f32x16& a, int i) { return f32x2{a[2 * i], a[2 * i + 1]}; }

template <class MID> DI void softmax2(const f32x16& p, const float L2E  , u32x4 (&f)[2][2], MID&& mid) {
  float mx[2], mn[2];
#pragma unroll
  for (int hd = 0; hd < 2; ++hd) {
    const int b = 8 * hd;
    mx[hd] = mx2(mx3(mx3(mx3(p[b], p[b + 1], p[b + 2]), p[b + 3], p[b + 4]), p[b + 5], p[b + 6]), p[b + 7]);
    mn[hd] = mx2(mx3(mx3(mx3(-p[b], -p[b + 1], -p[b + 2]), -p[b + 3], -p[b + 4]), -p[b + 5], -p[b + 6]), -p[b + 7]);
    red2max(mx[hd], mn[hd]);
  }
  mid(mx, mn);
  f32x2 e[2][2][4]; float s[2][2];
#pragma unroll
  for (int hd = 0; hd < 2; ++hd) {
    const float cp = -mx[hd] * L2E, cn = -mn[hd] * L2E;
#pragma unroll
    for (int i = 0; i < 4; ++i) {
      const f32x2 v = pr(p, 4 * hd + i), a = v * L2E + cp, b = v * -L2E + cn;
      e[hd][0][i] = f32x2{__builtin_amdgcn_exp2f(a.x), __builtin_amdgcn_exp2f(a.y)};
      e[hd][1][i] = f32x2{__builtin_amdgcn_exp2f(b.x), __builtin_amdgcn_exp2f(b.y)};
    }
#pragma unroll
    for (int sg = 0; sg < 2; ++sg) { const f32x2 t = (e[hd][sg][0] + e[hd][sg][1]) + (e[hd][sg][2] + e[hd][sg][3]); s[hd][sg] = t.x + t.y; }
    red2sum(s[hd][0], s[hd][1]);
#pragma unroll
    for (int sg = 0; sg < 2; ++sg) {
      const float r = __builtin_amdgcn_rcpf(s[hd][sg]);
#pragma unroll
      for (int i = 0; i < 4; ++i) f[hd][sg][i] = pk2v(e[hd][sg][i] * r);
    }
  }
}
DI void pack16(const f32x16& a, u32x4 (&y)[2]) {
#pragma unroll
  for (int i = 0; i < 8; ++i) y[i >> 2][i & 3] = pk2v(pr(a, i));
}
DI void lnfix(f32x16& a, LAS char* Sp, u32 negmean) { a = mfma(asb(u32x4{*(LAS u32*)Sp, 0u, 0u, 0u}), asb(u32x4{negmean, 0u, 0u, 0u}), a); }
DI void lnfix4(f32x16& a, LAS char* Sp, u32 negmean) { a = mfma(asb(*(LAS u32x4*)Sp), asb(u32x4{negmean, negmean, negmean, negmean}), a); }
DI u32 split2(float v) { const u32 hi = pk2(v, v) & 0xffffu; return hi | (pk2(v - bflo(hi), 0.f) << 16); }

template <int V> struct IC { static constexpr int v = V; };
template <int B, int E, class F> DI void sfor(F&& f) { if constexpr (B < E) { f(IC<B>{}); sfor<B + 1, E>(f); } }
#define SB() __builtin_amdgcn_sched_barrier(0)
template <int NT, int K0, int K1, int D, int XM, class LD, class PRE, class POST> DI void gemm(f32x16* acc, u32x4* xf, LD&& ld, PRE&& pre, POST&& post) {
  bf16x8 wf[D + 1];
  constexpr int S0 = K0 * NT, S1 = K1 * NT;
  sfor<S0, S0 + D>([&](auto I) { constexpr int s = I.v; wf[s % (D + 1)] = ld(IC<s / NT>{}, IC<s % NT>{}); });
  SB();
  sfor<S0, S1>([&](auto I) {
    constexpr int s = I.v;
    pre(I);
    if constexpr (s + D < S1) wf[(s + D) % (D + 1)] = ld(IC<(s + D) / NT>{}, IC<(s + D) % NT>{});
    SB();
    post(I);
    acc[s % NT] = mfma(wf[s % (D + 1)], asb(xf[(s / NT) % XM]), acc[s % NT]);
    asm volatile("" : "+v"(acc[s % NT]));
    SB();
  });
}
constexpr int b2slot(int t) { return t < 4 ? t * 16384 : t == 4 || t == 13 ? 131072 : t < 9 ? 65536 + (t - 5) * 16384 : (t - 9) * 16384; }
DI LAS char* opaque(LAS char* p) { asm volatile("" : "+v"(p)); return p; }

DI void cnt_wait(const char* ctl, u32 off, u32 val) {
  for (u32 sp = 0; sp < SPIN_CAP; ++sp) {
    u32 v; gld4_sc1(v, ctl, off); asm volatile("s_waitcnt vmcnt(0)" : "+v"(v)::"memory");
    if (__builtin_amdgcn_readfirstlane(v) >= val) break;
    if (PSLEEP) __builtin_amdgcn_s_sleep(PSLEEP);
  }
}
#define HANDOFF(n) do { DRAIN(); STAMP(n == 0 ? 4 + 12 * layer : n == 1 ? 6 + 12 * layer : 12); RAWBAR(); if (w == 0) { PSIG(n); PWAIT(n); } RAWBAR(); } while (0)

__global__ __launch_bounds__(512, 2) void mega(const char* x, const char* wqkv, const char* wphi, const char* wo, const char* w1, const char* w2, char* out, char* ws) {
  __shared__ __attribute__((aligned(16))) char smem[163840];
  LAS char* lds = (LAS char*)smem;
  const u32 lb = (u32)(size_t)lds;
  int lane = threadIdx.x & 63; int w = __builtin_amdgcn_readfirstlane(threadIdx.x >> 6);
  int hh = lane >> 5, tk = lane & 31, g = lane >> 4, t16 = lane & 15, q = (lane >> 2) & 3, p = lane & 3;
  const int bid = blockIdx.x, loc = bid >> 3;
  int P = (bid & 7) * 2 + (loc >> 4), j = loc & 15;
  int th = j >> 3, cg = j & 7, cc = w & 1, kh = (w >> 1) & 1;
  u32 l16 = lane * 16u;
  const u32x4 ones4 = {0x3f803f80u, 0x3f803f80u, 0x3f803f80u, 0x3f803f80u};
  const f32x16 z16 = {0.f, 0.f, 0.f, 0.f, 0.f, 0.f, 0.f, 0.f, 0.f, 0.f, 0.f, 0.f, 0.f, 0.f, 0.f, 0.f};
  const f32x4 z4 = {0.f, 0.f, 0.f, 0.f};
  STAMP(0);
  {
    size_t pc_; asm volatile("s_getpc_b64 %0" : "=s"(pc_)); const char* pc = (const char*)(pc_ & ~(size_t)255);
    const int id = (w ^ 4) * 32 + loc;
    if (id < PFC) glds(lb + LD_SC + 8192 + w * 1024, pc + id * 1024, l16);
  }
  glds(lb + LD_SC + 8192 + w * 1024, x + (size_t)(128 * P + 8 * j + w) * 1024, l16);
  if (w >= 4) {
    const u32 swz = 16u * ((lane & 7) ^ ((g & 1) << 2));
    const u32 vo1024 = (lane >> 3) * 1024u + swz, vo3584 = (lane >> 3) * 3584u + swz, voA1 = (lane >> 2) * 3072u + 16u * (lane & 3);
    u32x4 wa1[16];
    u32x4 wb2[36];
    LAS char* wl = lds + l16;
#define HW (w & 3)
#define GLDS_Q(n) glds(lb + 65536 + (n) * 1024, wqkv + 64 * j + (size_t)((n) * 16) * 3072, voA1)
#define GLDS_B1(n) glds(lb + (n) * 1024, w1 + (size_t)(((n) & 63) * 8) * 3584 + j * 256 + ((n) >> 6) * 128, vo3584)
#define B2SRC(n) (w2 + cg * 128 + (size_t)(896 * (((n) >> 3) & 1) + (((n) >> 4) < 7 ? 128 * ((n) >> 4) : 128 * ((n) >> 4) - 832) + 8 * ((n) & 7)) * 1024)
#define GLDS_B2(n) glds(lb + ((n) < 64 ? (n) : (n) + 64) * 1024, B2SRC(n), vo1024)
#define GLDS_A3(n) glds(lb + LD_WA3 + (n) * 1024, wo + (size_t)(n) * 8192 + cg * 128, vo1024)
    auto putA1 = [&](auto R, auto M) { *(LAS u32x4*)(wl + R.v * 32768 + (4 * M.v + HW) * 1024) = wa1[R.v * 8 + M.v]; };
    auto putB2 = [&](auto T) { sfor<0, 4>([&](auto I) { *(LAS u32x4*)(wl + b2slot(T.v) + (4 * HW + I.v) * 1024) = wb2[(T.v - 5) * 4 + I.v]; }); };
    auto ldB2 = [&](auto T) { sfor<0, 4>([&](auto I) { gld16<0>(wb2[(T.v - 5) * 4 + I.v], B2SRC(T.v * 16 + 4 * HW + I.v), vo1024); }); };
    auto ldB1b = [&](u32x4* r) { const char* b = w1 + (size_t)(HW * 128) * 3584 + (j < 14 ? j : 13) * 256 + 128; sfor<0, 16>([&](auto I) { gld16<0>(r[I.v], b + (size_t)(I.v * 8) * 3584, vo3584); }); };
    auto putB1b = [&](u32x4* r) { sfor<0, 16>([&](auto I) { pin(r[I.v]); *(LAS u32x4*)(wl + 65536 + (HW * 16 + I.v) * 1024) = r[I.v]; }); };
    auto colsum8 = [&](LAS char* sp, int kstride, int second, f32x4& a, f32x4& b) {
      const u32 o = ((t16 >> 2) & 1) == (g & 1) ? 0x3f803f80u : 0u; const u32x4 sel = {o, o, o, o};
      bf16x8 wf[8];
      sfor<0, 8>([&](auto K) { wf[K.v] = trfrag(sp + K.v * kstride, second); });
      sfor<0, 4>([&](auto K) { a = mfma16(asb(sel), wf[2 * K.v], a); b = mfma16(asb(sel), wf[2 * K.v + 1], b); });
      asm volatile("" : "+v"(a), "+v"(b)); SB();
    };
    auto putS = [&](u32 off, float a) { const u32 sp = split2(a); *(LAS u32*)(lds + LD_S + off) = __builtin_amdgcn_perm(sp, sp, hh ? 0x03020302u : 0x01000100u); };
    const int lc = (P & 1) * 16 + j;
#define T3_KV() do { if (lane < 32) touch3(lb + 159 * 1024, wqkv + (size_t)(16 * lc + t16) * 3072 + 1024 + g * 1024); } while (0)
#define T3_A() do { if (lane < 32) touch3(lb + 159 * 1024, g == 0 ? wqkv + (size_t)(16 * lc + t16) * 3072 : wo + (size_t)(16 * lc + t16) * 1024); } while (0)
#define T3_B2() do { const int i_ = 20 * lc + lane; if (lane < 20) touch3(lb + 159 * 1024, w2 + (size_t)(896 * ((i_ >> 6) & 1) + 128 * (i_ >> 7) + (i_ & 63)) * 1024); } while (0)
#define T3_W(wm) touch3(lb + 159 * 1024, (wm) + (size_t)(56 * lc + (lane < 56 ? lane : 55)) * 1024)
    {
      if (HW == 0) T3_KV();
      if (HW == 1) *(LAS u32x2*)(lds + LD_ID + lane * 8) = g == p ? u32x2{q == 0 ? 0x3f80u : q == 1 ? 0x3f800000u : 0u, q == 2 ? 0x3f80u : q == 3 ? 0x3f800000u : 0u} : u32x2{0u, 0u};
      u32x2 h0 = {0u, 0u};
      if (HW == 3) {
        gld8_sc1(h0, ws, HEL_OWN); gst16_sc1<0>(ws + (size_t)(P * 16 + j) * 2048, l16, u32x4{0u, 0u, 0u, 0u}); gst16_sc1<1536>(ws + (size_t)(P * 16 + j) * 2048, tk * 16u, u32x4{0u, 0u, 0u, 0u});
      }
      const char* wb = wqkv + 64 * j + (size_t)(16 * HW) * 3072;
      __builtin_amdgcn_s_sleep(HSLEEP);
      sfor<0, 8>([&](auto M) { gld16<1024>(wa1[M.v], wb + (size_t)(64 * M.v) * 3072, voA1); gld16<2048>(wa1[8 + M.v], wb + (size_t)(64 * M.v) * 3072, voA1); });
      {
        const int id = (P * 16 + j) * 4 + HW;
        gld16<0>(wb2[0], w1 + (size_t)id * 1792, lane < 56 ? l16 : 0u); gld16<896>(wb2[1], w1 + (size_t)id * 1792, lane < 56 ? l16 : 0u);
        gld16<0>(wb2[2], w2 + (size_t)id * 1792, lane < 56 ? l16 : 0u); gld16<896>(wb2[3], w2 + (size_t)id * 1792, lane < 56 ? l16 : 0u);
        gld16<0>(wb2[4], wo + (size_t)id * 512, tk * 16u);
        const u32 ly = HW & 1, pg = lane & 7;
        const u32 to = HW < 2 ? (u32)WS_H + ly * (7u << 20) + P * 458752u + (pg < 7 ? pg : 6) * 65536u
                              : (pg < 4 ? (u32)WS_O + (pg >> 1) * (4u << 20) + ly * (2u << 20) : pg < 6 ? (u32)WS_X2 : (u32)WS_ST1 + ly * (1u << 20)) + (pg < 6 ? P * 131072u + (pg & 1) * 65536u : (pg & 1) * (2u << 20));
        gld16<0>(wb2[5], ws, to);
        if (HW == 0) T3_A();
        if (HW == 1) T3_W(w1);
        if (HW == 2) T3_W(w2);
      }
      f32x4 sk = z4, sv = z4;
      u32x4 sel;
#pragma unroll
      for (int e = 0; e < 4; ++e) sel[e] = ((t16 & 7) >> 1) == e ? ((t16 & 1) ? 0x3f800000u : 0x00003f80u) : 0u;
      sfor<0, 4>([&](auto C) {
        constexpr int c = C.v;
        waitv4<18 - 4 * c>(wa1[2 * c], wa1[2 * c + 1], wa1[8 + 2 * c], wa1[8 + 2 * c + 1]);
        if (c == 0 && HW == 3) { pin2(h0); if (lane < 32) gst8_sc1(ws, HEL_PEER, u32x2{h0.y, h0.x + 2u}); }
        putA1(IC<0>{}, IC<2 * c>{}); putA1(IC<0>{}, IC<2 * c + 1>{}); putA1(IC<1>{}, IC<2 * c>{}); putA1(IC<1>{}, IC<2 * c + 1>{});
        sk = mfma16(asb(sel), asb(wa1[2 * c]), sk); sv = mfma16(asb(sel), asb(wa1[8 + 2 * c]), sv);
        sk = mfma16(asb(sel), asb(wa1[2 * c + 1]), sk); sv = mfma16(asb(sel), asb(wa1[8 + 2 * c + 1]), sv);
        asm volatile("" : "+v"(sk), "+v"(sv));
        if (c == 3) {
          LAS char* tb = lds + LD_SC + 16384 + HW * 2048;
          *(LAS f32x4*)(tb + l16) = sk; *(LAS f32x4*)(tb + 1024 + l16) = sv;
          LAS float* tr = (LAS float*)(tb + (q & 1) * 256 + (tk >> 3) * 16 + p * 4);
          float a = tr[0], b = tr[16], c_ = tr[32], d = tr[48], e = tr[256], f = tr[272], g_ = tr[288], h = tr[304];
          a += b; asm volatile("" : "+v"(a)); c_ += d; asm volatile("" : "+v"(c_)); e += f; asm volatile("" : "+v"(e)); g_ += h; asm volatile("" : "+v"(g_));
          a += c_; asm volatile("" : "+v"(a)); e += g_;
          putS(l16 + 4 * HW, a); putS(1024 + l16 + 4 * HW, e);
        }
        STAMP(29 + (c == 3 ? 4 : c));
        if (c & 1) BAR();
        if (c & 1) __builtin_amdgcn_s_setprio(c == 1 ? 3 : 0);
      });
      u32x2 hv;
      if (HW == 3) gld8_sc1(hv, ws, HEL_OWN);
      _Pragma("unroll 4") for (int i = 0; i < 8; ++i) GLDS_Q(HW * 8 + i);
      ldB1b(wb2 + 20);
      asm volatile("s_waitcnt vmcnt(24)" ::: "memory");
      sfor<0, 6>([&](auto I) { pin(wb2[I.v]); });
      if (HW == 3) {
        pin2(hv);
        u32 sent = h0.x;
        for (u32 sp_ = 0; sp_ < SPIN_CAP; ++sp_) {
          if (hv.x != sent) { sent = hv.x; if (lane < 32) gst8_sc1(ws, HEL_PEER, u32x2{h0.y, hv.x + 1u}); }
          if (__all(hv.y != h0.y)) break;
          gld8_sc1(hv, ws, HEL_OWN); asm volatile("s_waitcnt vmcnt(0)" : "+v"(hv)::"memory");
        }
      }
      STAMP(32);
      sfor<5, 5 + NEARLY>(ldB2);
      STAMP(39);
      asm volatile("s_waitcnt vmcnt(%0)" ::"i"(16 + 4 * NEARLY) : "memory");
      RAWBAR();
      { f32x4 a = z4, b = z4; colsum8(lds + 65536 + HW * 8192 + TRA, 1024, 512, a, b); putS(2048 + l16 + 4 * HW, a[0] + b[0]); }
      BAR();
    }
    sfor<0, 2>([&](auto L) {
      constexpr int layer = L.v;
      FRESH();
      if constexpr (layer == 1) {
        RAWBAR();
        ldB1b(wa1);
        _Pragma("unroll 4") for (int i = 0; i < 8; ++i) GLDS_A3(32 + HW * 8 + i);
        STAMP(42);
        RAWBAR();
        RAWBAR();
      }
      if (j < 14) { _Pragma("unroll 4") for (int i = 0; i < 8; ++i) GLDS_B1(HW * 16 + i); }
      if constexpr (layer == 0) { _Pragma("unroll 4") for (int i = 0; i < 8; ++i) GLDS_A3(32 + HW * 8 + i); }
      STAMP(36);
      RAWBAR();
      _Pragma("unroll 4") for (int i = 0; i < A3PRE; ++i) GLDS_A3(HW * 8 + i);
      STAMP(48);
      RAWBAR();
      _Pragma("unroll 4") for (int i = A3PRE; i < 8; ++i) GLDS_A3(HW * 8 + i);
      RAWBAR();
      if constexpr (layer == 0) { sfor<5 + NEARLY, 5 + NEARLY + NW4>(ldB2); asm volatile("s_waitcnt vmcnt(%0)" ::"i"(4 * NW4) : "memory"); } else DRAIN();
      RAWBAR();
      __builtin_amdgcn_s_sleep(A3SLEEP);
      if (j < 14) { _Pragma("unroll 4") for (int i = 8; i < 16; ++i) GLDS_B1(HW * 16 + i); }
      if (HW == 1) T3_B2();
      if constexpr (layer == 0) sfor<5 + NEARLY + NW4, 10 - NW6>(ldB2);
      RAWBAR();
      putB1b(layer == 0 ? wb2 + 20 : wa1);
      DRAIN();
      BAR();
      FRESH();
      if constexpr (layer == 0) sfor<10 - NW6, 10>(ldB2);
      if (layer == 0 && j < 14) {
        f32x4 aS = z4, aT = z4; LAS char* sp = lds + (HW >> 1) * 65536 + ((HW & 1) ? TRB1 : TRB0);
        sfor<0, 4>([&](auto C) { colsum8(sp + C.v * 16384, 2048, 1024, aS, aT); });
        putS(3072 + HW * 256 + lane * 4, aS[0] + aT[0]);
      }
      BAR();
      RAWBAR();
      if constexpr (layer == 0) { ldB2(IC<10>{}); ldB2(IC<11>{}); ldB2(IC<12>{}); ldB2(IC<13>{}); }
      STAMP(25);
      RAWBAR();
      _Pragma("unroll 4") for (int i = 0; i < 20; ++i) GLDS_B2(HW * 20 + i);
      STAMP(34);
      RAWBAR();
      if (HW == 0) { PSIG(2); PWAIT(2); }
      DRAIN();
      STAMP(35);
      RAWBAR();
      FRESH();
      if (HW == 0) PSIG(5);
      sfor<0, 36>([&](auto I) { pin(wb2[I.v]); });
      putB2(IC<5>{}); putB2(IC<6>{}); putB2(IC<7>{}); putB2(IC<8>{});
      STAMP(38);
      if (HW == 0) PWAIT(5);
      STAMP(57);
      BAR();
      putB2(IC<9>{}); putB2(IC<10>{}); putB2(IC<11>{});
      STAMP(59);
      BAR();
      if constexpr (layer == 0) { if (HW == 1) T3_A(); if (HW == 2) T3_W(w1); }
      putB2(IC<12>{}); putB2(IC<13>{});
      STAMP(61);
      BAR();
      if constexpr (layer == 0) { _Pragma("unroll 4") for (int i = 0; i < 8; ++i) GLDS_Q(HW * 8 + i); }
      if constexpr (layer == 0) sfor<0, 8>([&](auto M) { putA1(IC<0>{}, M); });
      RAWBAR();
      if constexpr (layer == 0) {
        sfor<0, 8>([&](auto M) { putA1(IC<1>{}, M); });
        RAWBAR(); DRAIN(); BAR();
      }
    });
  } else {
    u32x4 resid[2];
    u32x4 wph[2];
    u32x4 xf[32];
    f32x16 acc[4];
    float rstd; u32 nmean;
    auto nop = [&](auto) {};
#define LDF(i, n) gld16_sc1<((n) & 3) * 1024>(xf[i], xb + ((n) >> 2) * 4096, l16)
#define FINSTATS(sum, ssq) { red2sum(sum, ssq); const float mean = sum * (1.f / 512.f); rstd = __builtin_amdgcn_rsqf(mx2(ssq * (1.f / 512.f) - mean * mean, 0.f) + 1e-5f); nmean = split2(-mean); }
#define STATS() { _Pragma("unroll") for (int i = 0; i < 4; ++i) pin(st[i]); \
      const f32x4 v = (__builtin_bit_cast(f32x4, st[0]) + __builtin_bit_cast(f32x4, st[1])) + (__builtin_bit_cast(f32x4, st[2]) + __builtin_bit_cast(f32x4, st[3])); \
      float a = v[0], b = v[1], c = v[2], d = v[3]; swap16(a, c); swap16(b, d); float sum = a + c, ssq = b + d; FINSTATS(sum, ssq) }
#define LDSTATS(SB_) { const char* sb = (SB_) + (size_t)(4 * P + w) * 4096; \
      gld16_sc1<0>(st[0], sb, l16); gld16_sc1<1024>(st[1], sb, l16); gld16_sc1<2048>(st[2], sb, l16); gld16_sc1<3072>(st[3], sb, l16); }
    constexpr int N0 = N0V;
#define STREAM32(NT, SB_) { constexpr int kb_ = s / NT; if constexpr (s % NT == 0) waitv<(N0 + kb_ < 36 ? N0 + kb_ : 36) - kb_ - 1>(xf[kb_]); \
      if constexpr (s % NT == 1 && N0 + kb_ < 32) LDF(N0 + kb_, N0 + kb_); \
      if constexpr (s % NT == 1 && N0 + kb_ >= 32 && N0 + kb_ < 36) gld16_sc1<(N0 + kb_ - 32) * 1024>(st[(N0 + kb_) & 3], (SB_) + (size_t)(4 * P + w) * 4096, l16); }
#define RES2ACC() { _Pragma("unroll") for (int i = 0; i < 8; ++i) { const u32 r = resid[i >> 2][i & 3]; acc[0][2 * i] = bflo(r); acc[0][2 * i + 1] = bfhi(r); } acc[1] = z16; asm volatile("" : "+v"(acc[0])); }
    for (int layer = 0; layer < 2; ++layer) {
      FRESH();
      acc[0] = z16; acc[1] = z16; acc[2] = z16;
      LAS char* wa1p = opaque(lds + TRA);
      auto ldkv = [&](auto KB, auto T) { return trfrag(wa1p + T.v * 32768 + KB.v * 1024, 512); };
      u32x4 sk, sv; LAS char* kt; LAS char* ka; u32 kvo;
      char* kb_ = ws + WS_KVP + (size_t)((layer * 16 + j) * 4 + w) * KVP_UNIT; asm volatile("" : "+s"(kb_));
      auto kvtail = [&](auto I) {
        if constexpr (I.v == 50) { kt = lds + LD_SC + (32 * w + tk) * 200 + 8 * hh; asm volatile("" : "+v"(kt)); }
        if constexpr (I.v == 52) { ka = lds + LD_SC + (4 * g + q) * 200 + 8 * p + (w >> 1) * 96; asm volatile("" : "+v"(ka)); }
        if constexpr (I.v == 54) { kvo = P * 512u + hh * 256u + t16 * 16u; asm volatile("" : "+v"(kvo)); }
        if constexpr (I.v == 56) { sk = *(LAS u32x4*)(lds + LD_S + l16); sv = *(LAS u32x4*)(lds + LD_S + 1024 + l16); }
        if constexpr (I.v == 61) { acc[0] = mfma(asb(sk), asb(u32x4{nmean, nmean, nmean, nmean}), acc[0]); asm volatile("" : "+v"(acc[0])); }
        if constexpr (I.v == 62) { acc[1] = mfma(asb(sv), asb(u32x4{nmean, nmean, nmean, nmean}), acc[1]); asm volatile("" : "+v"(acc[1])); }
      };
      if (layer == 0) {
        const char* xb = x + (size_t)(128 * P + 32 * w) * 1024;
        u32 xvo[4];
#pragma unroll
        for (int i = 0; i < 4; ++i) xvo[i] = g * 1024u + ((t16 ^ (4 * i + g)) * 16u);
        const u32 stg = lb + 65536 + w * 16384;
        auto ldx = [&](auto R) { sfor<0, 8>([&](auto I) { glds(stg + (R.v & 1) * 8192 + I.v * 1024, xb + R.v * 256 + I.v * 4096, xvo[I.v & 3]); }); };
        ldx(IC<0>{}); ldx(IC<1>{});
        sfor<16, 32>([&](auto I) { gld16<(I.v >> 3) * 256>(xf[I.v], xb + (I.v & 7) * 4096, xvo[I.v & 3]); });
        u32 wp[8];
        {
          const u16* wsrc = (const u16*)wphi + (2 * j + (tk >> 4)) * 256 + (4 * hh) * 16 + t16;
#pragma unroll
          for (int i = 0; i < 8; ++i) asm volatile("global_load_ushort %0, %1, off offset:%2" : "=v"(wp[i]) : "v"(wsrc), "i"((i >> 2) * 256 + (i & 3) * 32) : "memory");
        }
        STAMP(32);
        const u32 str = 65536 + w * 16384 + tk * 256 + t16 * 16 + 8 * hh;
        float sm[4] = {0.f, 0.f, 0.f, 0.f}, sq[4] = {0.f, 0.f, 0.f, 0.f};
        sfor<0, 4>([&](auto R) {
          constexpr int b = 8 * R.v;
          asm volatile("s_waitcnt vmcnt(%0)" ::"i"(32 - b) : "memory");
          if constexpr (R.v >= 2) {
            waitl8(xf + b);
#pragma unroll
            for (int i = 0; i < 8; ++i) *(LAS u32x4*)(lds + 65536 + w * 16384 + (R.v & 1) * 8192 + i * 1024 + l16) = xf[b + i];
          }
#pragma unroll
          for (int i = 0; i < 8; ++i) {
            u32x2 lo = *(LAS u32x2*)(lds + ((str ^ (32 * i)) + (R.v & 1) * 8192)), hi = *(LAS u32x2*)(lds + ((str ^ (32 * i + 16)) + (R.v & 1) * 8192));
            xf[b + i] = u32x4{lo.x, lo.y, hi.x, hi.y};
          }
          if ((cg >> 1) == R.v && (w >> 1) == th) {
            LAS char* rx = lds + LD_SC + cc * 4096 + l16;
            if (cg & 1) { _Pragma("unroll") for (int i = 0; i < 4; ++i) *(LAS u32x4*)(rx + i * 1024) = xf[b + 4 + i]; }
            else { _Pragma("unroll") for (int i = 0; i < 4; ++i) *(LAS u32x4*)(rx + i * 1024) = xf[b + i]; }
          }
          STAMP(29 + (R.v == 3 ? 2 : R.v));
        });
        sfor<0, 32>([&](auto I) {
#pragma unroll
          for (int e = 0; e < 4; ++e) { sm[e] = dot2(xf[I.v][e], 0x3f803f80u, sm[e]); sq[e] = dot2(xf[I.v][e], xf[I.v][e], sq[e]); }
        });
        float sum = (sm[0] + sm[1]) + (sm[2] + sm[3]), ssq = (sq[0] + sq[1]) + (sq[2] + sq[3]);
        FINSTATS(sum, ssq)
        DRAIN();
#pragma unroll
        for (int i = 0; i < 8; ++i) pin1(wp[i]);
        const u32x4 wv = {wp[0] | (wp[1] << 16), wp[2] | (wp[3] << 16), wp[4] | (wp[5] << 16), wp[6] | (wp[7] << 16)};
        const u32x4 zz = {0u, 0u, 0u, 0u};
        wph[0] = tk < 16 ? wv : zz; wph[1] = tk < 16 ? zz : wv;
        pin(wph[0]); pin(wph[1]); asm volatile("" : "+v"(rstd), "+v"(nmean));
        STAMP(33);
        BAR();
        STAMP(54);
        resid[0] = *(LAS u32x4*)(lds + LD_SC + cc * 4096 + kh * 2048 + l16); resid[1] = *(LAS u32x4*)(lds + LD_SC + cc * 4096 + kh * 2048 + 1024 + l16);
        gemm<2, 0, 32, 4, 32>(acc, xf, ldkv, [&](auto I) { if constexpr (I.v == 28) { pin(resid[0]); pin(resid[1]); STAMP(44); RAWBAR(); STAMP(45); }   }, kvtail);
      } else {
        u32x4 st[4];
        const char* xb = ws + WS_X2 + (size_t)(4 * P + w) * 32768;
        asm volatile("" : "+s"(xb));
        { constexpr int layer = 0; HANDOFF(4); }
        STAMP(40);
        sfor<0, N0>([&](auto I) { LDF(I.v, I.v); });
        STAMP(41);
        gemm<2, 0, 32, 4, 32>(acc, xf, ldkv, [&](auto I) {
          constexpr int s = I.v;
          if constexpr (s == 2 * (36 - N0)) RAWBAR();
          STREAM32(2, ws + WS_ST2)
          if constexpr (s == 59) DRAIN();
        }, [&](auto I) { if constexpr (I.v == 59) { STATS() asm volatile("" : "+v"(rstd), "+v"(nmean)); } kvtail(I); });
      }
      STAMP(1 + 12 * layer);
      {
        u32x4 yk[2], yv[2], f[2][2];
        pack16(acc[0], yk);
        f32x16 pp = mfma(asb(wph[0]), asb(yk[0]), z16); pp = mfma(asb(wph[1]), asb(yk[1]), pp);
        pack16(acc[1] * rstd, yv);
#pragma unroll
        for (int hd = 0; hd < 2; ++hd) { *(LAS u32x2*)(kt + hd * 96 + 64) = u32x2{yv[hd].x, yv[hd].y}; *(LAS u32x2*)(kt + hd * 96 + 80) = u32x2{yv[hd].z, yv[hd].w}; }
        softmax2(pp, rstd * 1.4426950408889634f, f, [](float*, float*) {});
#pragma unroll
        for (int hd = 0; hd < 2; ++hd) {
          *(LAS u32x2*)(kt + hd * 96) = u32x2{f[hd][0].x, f[hd][0].y}; *(LAS u32x2*)(kt + hd * 96 + 16) = u32x2{f[hd][0].z, f[hd][0].w};
          *(LAS u32x2*)(kt + hd * 96 + 32) = u32x2{f[hd][1].x, f[hd][1].y}; *(LAS u32x2*)(kt + hd * 96 + 48) = u32x2{f[hd][1].z, f[hd][1].w};
        }
      }
      STAMP(42);
      BAR();
      STAMP(43);
      {
        f32x4 akv = {0.f, 0.f, 0.f, 0.f}, az = {0.f, 0.f, 0.f, 0.f};
        bf16x8 A[4], B[4];
#pragma unroll
        for (int ks = 0; ks < 4; ++ks) { A[ks] = trfrag(ka + ks * 6400 + (w & 1) * 32, 3200); B[ks] = trfrag(ka + ks * 6400 + 64, 3200); }
        SB();
#pragma unroll
        for (int ks = 0; ks < 4; ++ks) { az = mfma16(asb(ones4), A[ks], az); akv = mfma16(A[ks], B[ks], akv); }
        const u32x2 kp = pk4(akv);
        auto r0 = __builtin_amdgcn_permlane16_swap(kp.x, kp.x, false, false), r1 = __builtin_amdgcn_permlane16_swap(kp.y, kp.y, false, false);
        gst16_sc1<0>(kb_, kvo, u32x4{r0[0], r1[0], r0[1], r1[1]});
        gst4_sc1(kb_, 8192u + (kvo >> 9) * 64u + ((kvo >> 2) & 60u), __float_as_uint(az[0]));
      }
      STAMP(37);
      FRESH();
      {
        u32x4 phq[2][2], yq[2], sq_, sel[2];
        u32 kc, zz[4], gvo;
        LAS char* gi; LAS char* gr; char* ob;
        LAS char* wq = opaque(lds + 65536 + TRA);
        gemm<1, 0, 32, 4, 32>(acc + 2, xf, [&](auto KB, auto) { return trfrag(wq + KB.v * 1024, 512); }, [&](auto I) {
          if constexpr (I.v == KSTEP) { DRAIN(); if (lane < 16) gadd1(ws, KOFF(lane)); STAMP(2 + 12 * layer); RAWBAR();   }
        }, [&](auto I) {
          if constexpr (I.v == 2) { gi = lds + LD_SC + 16384 + w * 680 + 8 * g; asm volatile("" : "+v"(gi)); }
          if constexpr (I.v == 4) { gr = lds + LD_SC + 16384 + (tk < 16 ? tk : 16) * 40 + 8 * hh; asm volatile("" : "+v"(gr)); }
          if constexpr (I.v == 6) { ob = ws + WS_O + (size_t)layer * (2u << 20) + ((size_t)(4 * P + w) * 32 + 2 * j) * 1024; gvo = g * 512u + t16 * 16u; asm volatile("" : "+s"(ob), "+v"(gvo)); }
          if constexpr (I.v == 24) sq_ = *(LAS u32x4*)(lds + LD_S + 2048 + l16);
          if constexpr (I.v == 30) { acc[2] = mfma(asb(sq_), asb(u32x4{nmean, nmean, nmean, nmean}), acc[2]); asm volatile("" : "+v"(acc[2])); }
        });
        STAMP(36);
        gld4_sc1(kc, ws, KOFF(P));
        sel[0] = *(LAS u32x4*)(lds + LD_ID + t16 * 32); sel[1] = *(LAS u32x4*)(lds + LD_ID + t16 * 32 + 16);
        pack16(acc[2], yq);
        f32x16 pp = mfma(asb(wph[0]), asb(yq[0]), z16); pp = mfma(asb(wph[1]), asb(yq[1]), pp);
        softmax2(pp, rstd * 1.4426950408889634f, phq, [&](float* mx, float* mn) {
          asm volatile("" : "+v"(mx[0]), "+v"(mx[1]), "+v"(mn[0]), "+v"(mn[1])); SB();
          STAMP(55);
          asm volatile("s_waitcnt vmcnt(0)" : "+v"(kc)::"memory");
          if (__builtin_amdgcn_readfirstlane(kc) < 16u * (layer + 1)) cnt_wait(ws, KOFF(P), 16u * (layer + 1));
          STAMP(3 + 12 * layer);
          sfor<0, 4>([&](auto I) { gld4o_sc1<I.v * 256>(zz[I.v], kb_ + 8192, lane * 4u); });
          sfor<0, 8>([&](auto I) { gld16_sc1<((I.v >> 1) & 1) * 2048 + (I.v & 1) * 256>(xf[I.v], kb_ + (I.v >> 2) * 4096, gvo); });
          RAWBAR();
          STAMP(46);
          SB();
        });
        pin(phq[0][0]); pin(phq[0][1]); pin(phq[1][0]); pin(phq[1][1]);
        STAMP(47);
        asm volatile("s_waitcnt vmcnt(8)" : "+v"(zz[0]), "+v"(zz[1]), "+v"(zz[2]), "+v"(zz[3]));
        f32x4 sz = __builtin_amdgcn_mfma_f32_16x16x4f32((__uint_as_float(zz[0]) + __uint_as_float(zz[1])) + (__uint_as_float(zz[2]) + __uint_as_float(zz[3])), 1.f, z4, 0, 0, 0);
        asm volatile("" : "+v"(sz)); SB();
        f32x4 s0 = z4, s1 = z4;
        sfor<0, 4>([&](auto I) {
          waitv<7 - 2 * I.v>(xf[2 * I.v]); s0 = mfma16(asb(sel[0]), asb(xf[2 * I.v]), s0);
          waitv<6 - 2 * I.v>(xf[2 * I.v + 1]); s1 = mfma16(asb(sel[1]), asb(xf[2 * I.v + 1]), s1);
          SB();
        });
        *(LAS u32x2*)(gi + 640) = pk4(sz);
        s0 += s1;
        *(LAS u32x2*)(gi + t16 * 40) = pk4(s0);
        STAMP(48);
        BAR();
        STAMP(49);
        f32x16 o[2]; u32x4 kf[4];
#pragma unroll
        for (int i = 0; i < 4; ++i) { const u32x2 a = *(LAS u32x2*)(gr + i * 680), b = *(LAS u32x2*)(gr + i * 680 + 16); kf[i] = u32x4{a.x, a.y, b.x, b.y}; }
        SB();
        o[0] = mfma(asb(kf[0]), asb(phq[0][0]), z16); o[1] = mfma(asb(kf[2]), asb(phq[1][0]), z16);
        o[0] = mfma(asb(kf[1]), asb(phq[0][1]), o[0]); o[1] = mfma(asb(kf[3]), asb(phq[1][1]), o[1]);
        asm volatile("" : "+v"(o[0]), "+v"(o[1])); SB();
        sfor<0, 2>([&](auto HD) {
          constexpr int hd = HD.v;
          const float r = __builtin_amdgcn_rcpf(o[hd][8] + 1e-6f);
          gst16_sc1<hd * 1024>(ob, l16, u32x4{pk2v(pr(o[hd], 0) * r), pk2v(pr(o[hd], 1) * r), pk2v(pr(o[hd], 2) * r), pk2v(pr(o[hd], 3) * r)});
        });
        STAMP(50);
        RES2ACC()
      }
#define TILE_REDUCE(EXOFF) { LAS char* ex = lds + (EXOFF) + l16; \
        _Pragma("unroll") for (int i = 0; i < 4; ++i) *(LAS f32x4*)(ex + w * 4096 + i * 1024) = f32x4{acc[1][4 * i], acc[1][4 * i + 1], acc[1][4 * i + 2], acc[1][4 * i + 3]}; \
        BAR(); \
        f32x4 rd[4]; _Pragma("unroll") for (int i = 0; i < 4; ++i) rd[i] = *(LAS f32x4*)(ex + (w ^ 2) * 4096 + i * 1024); SB(); \
        _Pragma("unroll") for (int i = 0; i < 4; ++i) { const f32x4 v = rd[i] + f32x4{acc[0][4 * i], acc[0][4 * i + 1], acc[0][4 * i + 2], acc[0][4 * i + 3]}; \
          resid[i >> 1][2 * (i & 1)] = pk2(v[0], v[1]); resid[i >> 1][2 * (i & 1) + 1] = pk2(v[2], v[3]); } }
#define TILE_PUBLISH(xo, so) { gst16_sc1<0>(xo, l16, resid[0]); gst16_sc1<1024>(xo, l16, resid[1]); \
        float sum = 0.f, ssq = 0.f, sum2 = 0.f, ssq2 = 0.f; \
        _Pragma("unroll") for (int e = 0; e < 4; ++e) { sum = dot2(resid[0][e], 0x3f803f80u, sum); ssq = dot2(resid[0][e], resid[0][e], ssq); sum2 = dot2(resid[1][e], 0x3f803f80u, sum2); ssq2 = dot2(resid[1][e], resid[1][e], ssq2); } \
        sum += sum2; ssq += ssq2; red2sum(sum, ssq); sum2 = sum; ssq2 = ssq; swap16(sum, sum2); swap16(ssq, ssq2); \
        if (lane < 16) gst16_sc1<0>(so, l16, u32x4{__float_as_uint(sum), __float_as_uint(ssq), __float_as_uint(sum2), __float_as_uint(ssq2)}); }
      FRESH();
      {
        const size_t ch = (size_t)(4 * P + 2 * th + cc);
        const char* xb = ws + WS_O + (size_t)layer * (2u << 20) + (ch * 32 + 16 * kh) * 1024;
        char* xo = ws + WS_X1 + (size_t)layer * (2u << 20) + (ch * 32 + 4 * cg + 2 * kh) * 1024;
        char* so = ws + WS_ST1 + (size_t)layer * (256u << 10) + (ch * 16 + 2 * cg + kh) * 256;
        LAS char* wa[2] = {opaque(lds + LD_WA3 + kh * 32768 + (TRB0 ^ (64 * kh))), opaque(lds + LD_WA3 + kh * 32768 + (TRB1 ^ (64 * kh)))};
        asm volatile("" : "+s"(xb), "+s"(xo), "+s"(so));
        HANDOFF(0);
        STAMP(5 + 12 * layer);
        sfor<0, N0>([&](auto I) { LDF(I.v, I.v); });
        STAMP(51);
        gemm<2, 0, 16, 4, 32>(acc, xf, [&](auto KB, auto T) { return trfrag(wa[T.v] + KB.v * 2048, 1024); }, [&](auto I) {
          constexpr int s = I.v;
          if constexpr (s % 2 == 0) waitv<(N0 + s / 2 < 16 ? N0 + s / 2 : 16) - s / 2 - 1>(xf[s / 2]);
          if constexpr (s % 2 == 1 && N0 + s / 2 < 16) LDF(N0 + s / 2, N0 + s / 2);
        }, nop);
        STAMP(52);
        TILE_REDUCE(LD_SC)
        STAMP(53);
        TILE_PUBLISH(xo, so)
      }
      FRESH();
      {
        const char* xb = ws + WS_X1 + (size_t)layer * (2u << 20) + (size_t)(4 * P + w) * 32768;
        LAS char* wa[4] = {opaque(lds + TRB0), opaque(lds + TRB1), opaque(lds + 65536 + TRB0), opaque(lds + 65536 + TRB1)};
        asm volatile("" : "+s"(xb));
        HANDOFF(1);
        STAMP(7 + 12 * layer);
        if (j < 14) {
          u32x4 st[4];
          sfor<0, N0>([&](auto I) { LDF(I.v, I.v); });
          STAMP(28);
          acc[0] = z16; acc[1] = z16; acc[2] = z16; acc[3] = z16;
          gemm<2, 0, 32, 4, 32>(acc, xf, [&](auto KB, auto T) { return trfrag(wa[T.v] + KB.v * 2048, 1024); }, [&](auto I) {
            constexpr int s = I.v;
            if constexpr (s == 2 * (36 - N0)) RAWBAR();
            STREAM32(2, ws + WS_ST1 + (size_t)layer * (256u << 10))
            if constexpr (s == 60) DRAIN();
          }, [&](auto I) { if constexpr (I.v == 60) { STATS() asm volatile("" : "+v"(rstd), "+v"(nmean)); } });
          LAS char* Sp = lds + LD_S + 3072 + lane * 4;
          lnfix(acc[0], Sp, nmean); lnfix(acc[1], Sp + 256, nmean);
          STAMP(25);
          RAWBAR();
          STAMP(26);
          char* hb = ws + WS_H + (size_t)layer * (7u << 20) + ((size_t)(4 * P + w) * 112 + 8 * j) * 1024;
          const float c1 = rstd * -1.4426950408889634f, sigma = __builtin_amdgcn_rcpf(rstd);
          float tt[8]; u32x4 hp;
          auto uop = [&](auto M) {
            constexpr int st_ = M.v % 6, e = M.v / 6 - st_, t = e / 16, r = e % 16;
            if constexpr (e >= 0 && e < 64) {
              if constexpr (st_ == 0) tt[e & 7] = acc[t][r] * c1;
              if constexpr (st_ == 1) tt[e & 7] = __builtin_amdgcn_exp2f(tt[e & 7]);
              if constexpr (st_ == 2) tt[e & 7] = __builtin_fmaf(tt[e & 7], sigma, sigma);
              if constexpr (st_ == 3) tt[e & 7] = __builtin_amdgcn_rcpf(tt[e & 7]);
              if constexpr (st_ == 4) tt[e & 7] = acc[t][r] * tt[e & 7];
              if constexpr (st_ < 5) asm volatile("" : "+v"(tt[e & 7]));
              if constexpr (st_ == 5 && (e & 1)) {
                u32 pkd = pk2(tt[(e - 1) & 7], tt[e & 7]); pin1(pkd); hp[(e >> 1) & 3] = pkd;
                if constexpr ((e & 7) == 7) gst16_sc1<((e >> 3) & 3) * 1024>(hb + (e >> 5) * 4096, l16, hp);
              }
            }
          };
          constexpr int UB = 32 * 6, UC = 48 * 6;
          gemm<1, 0, 32, 4, 32>(acc + 2, xf, [&](auto KB, auto) { return trfrag(wa[2] + KB.v * 2048, 1024); }, nop, [&](auto I) { if constexpr (I.v >= 1) sfor<(I.v - 1) * UB / 31, I.v * UB / 31>(uop); });
          lnfix(acc[2], Sp + 512, nmean);
          STAMP(27);
          gemm<1, 0, 32, 4, 32>(acc + 3, xf, [&](auto KB, auto) { return trfrag(wa[3] + KB.v * 2048, 1024); },
                                [&](auto I) { if constexpr (I.v == 18) { STAMP(34); DRAIN(); RAWBAR();   } },
                                [&](auto I) { constexpr int i = I.v, u0 = i < 6 ? 5 * i : 30 + (i - 6) * (UC - UB - 30) / 26, u1 = i + 1 < 6 ? 5 * (i + 1) : 30 + (i - 5) * (UC - UB - 30) / 26; sfor<UB + u0, UB + u1>(uop); });
          lnfix(acc[3], Sp + 768, nmean);
          STAMP(8 + 12 * layer);
          sfor<UC, 70 * 6>(uop);
          STAMP(35);
        } else { RAWBAR(); RAWBAR(); RAWBAR(); }
      }
      FRESH();
      {
        const size_t ch = (size_t)(4 * P + 2 * th + cc);
        const char* xb = ws + WS_H + (size_t)layer * (7u << 20) + (ch * 112 + 56 * kh) * 1024;
        const int t0 = kh * 8192 + (TRB0 ^ (64 * kh)), t1 = t0 ^ 64;
        LAS char* wa[6] = {opaque(lds + t0), opaque(lds + t1), opaque(lds + 65536 + t0), opaque(lds + 65536 + t1), opaque(lds + 131072 + t0), opaque(lds + 131072 + t1)};
        asm volatile("" : "+s"(xb));
#define LDH(i) LDF((i) % 32, (i) < 28 ? 8 * ((i) / 4) + (i) % 4 : 8 * (((i) - 28) / 4) + 4 + (i) % 4)
        RES2ACC()
        DRAIN();
        STAMP(9 + 12 * layer);
        RAWBAR();
        STAMP(10 + 12 * layer);
        sfor<0, N0>([&](auto I) { LDH(I.v); });
        STAMP(56);
        static_assert(2 * (28 - N0) >= 32, "second half of h is loaded behind X_2");
        gemm<2, 0, 56, 4, 32>(acc, xf, [&](auto KB, auto T) { constexpr int so = b2slot(KB.v >> 2); return trfrag(wa[(so >> 16) * 2 + T.v] + (so & 65535) + (KB.v & 3) * 2048, 1024); }, [&](auto I) {
          constexpr int s = I.v, kb = s / 2;
          if constexpr (s == 32) STAMP(57);
          if constexpr (s == 68) STAMP(59);
          if constexpr (s == 92) STAMP(61);
          if constexpr (s == 32 || s == 68 || s == 92) RAWBAR();
          if constexpr (s == 32) STAMP(58);
          if constexpr (s == 68) STAMP(60);
          if constexpr (s % 2 == 0) waitv<(N0 + kb < 56 ? N0 + kb : 56) - kb - 1>(xf[kb % 32]);
          if constexpr (s % 2 == 1 && N0 + kb < 56) LDH(N0 + kb);
        }, nop);
        STAMP(11 + 12 * layer);
        TILE_REDUCE(98304)
        if (layer == 0) {
          char* xo = ws + WS_X2 + (ch * 32 + 4 * cg + 2 * kh) * 1024;
          char* so = ws + WS_ST2 + (ch * 16 + 2 * cg + kh) * 256;
          TILE_PUBLISH(xo, so)
        } else {
          char* ob = out + (size_t)(128 * P + 64 * th + 32 * cc) * 1024 + 128 * cg + 64 * kh;
          sfor<0, 2>([&](auto S) {
            auto r0 = __builtin_amdgcn_permlane32_swap(resid[S.v].x, resid[S.v].z, false, false);
            auto r1 = __builtin_amdgcn_permlane32_swap(resid[S.v].y, resid[S.v].w, false, false);
            gst16<32 * S.v>(ob, tk * 1024u + 16u * hh, u32x4{r0[0], r1[0], r0[1], r1[1]});
          });
        }
      }
    }
  }
}

extern "C" void kernel_launch(void* const* d_in, const int* in_sizes, int n_in, void* d_out, int out_size, void* d_ws, size_t ws_size, hipStream_t stream) {
  mega<<<256, 512, 0, stream>>>((const char*)d_in[0], (const char*)d_in[1], (const char*)d_in[2], (const char*)d_in[3], (const char*)d_in[5], (const char*)d_in[6], (char*)d_out, (char*)d_ws);
}
```

```cpp
#include <hip/hip_runtime.h>
#include <hip/hip_bf16.h>

typedef unsigned u32;
typedef unsigned short u16;
typedef u32 u32x4 __attribute__((ext_vector_type(4)));
typedef u32 u32x2 __attribute__((ext_vector_type(2)));
typedef float f32x16 __attribute__((ext_vector_type(16)));
typedef float f32x4 __attribute__((ext_vector_type(4)));
typedef float f32x2 __attribute__((ext_vector_type(2)));
typedef short s16x4 __attribute__((ext_vector_type(4)));
typedef __bf16 bf16x2 __attribute__((ext_vector_type(2)));
typedef __bf16 bf16x8 __attribute__((ext_vector_type(8)));
#define LAS __attribute__((address_space(3)))
#define DI __device__ __forceinline__

constexpr size_t WS_CTL = 0;
constexpr size_t WS_ST1 = 512u << 10;
constexpr size_t WS_ST2 = 1024u << 10;
constexpr size_t WS_KVP = 1280u << 10;
constexpr u32 KVP_UNIT = 9216;
constexpr size_t WS_O = 4u << 20;
constexpr size_t WS_X1 = 8u << 20;
constexpr size_t WS_X2 = 12u << 20;
constexpr size_t WS_H = 14u << 20;
constexpr size_t WS_STAMP = 40u << 20;
constexpr u32 C_HEL = 1024;

constexpr u32 LD_WA3 = 64 * 1024;
constexpr u32 LD_SC = 128 * 1024;
constexpr u32 LD_S = 153 * 1024;
constexpr u32 LD_STAMP = 157 * 1024;
constexpr u32 LD_ID = 159 * 1024 + 512;
#define TOUCH 1
#define HSLEEP 14
#define A3SLEEP 12
#define PFX 1
#define PFW 0
#define NEARLY 0
#define PFC 44
#define BSSTEP 56
#define HPRIO 3
#define PSLEEP 0
#define REP 16
#define KSTEP 12
#define NW4 2
#define NW6 2
#define A3PRE 4
#define SIG0 14
#define SIG1 0
#define B5B 8
#define NB4 0
#define B5S 2
#define HP0 20
#define SPRIO 0
#define X2S 44
#define CHK 30
#define SIG2 10
#define X2SLEEP 8
#define B5C 12
#define NB 12
#define TPOS 0
#define B1E 16
#define BXR 2
#define WPOS 1
#define TMASK 399
#define PW2SLEEP 17
#define PW5SLEEP 8
#define P8EARLY 0
#define HOSLEEP 13
#define N0V 12
constexpr u32 SPIN_CAP = 1u << 17;

#define BAR() do { asm volatile("s_waitcnt lgkmcnt(0)" ::: "memory"); __builtin_amdgcn_s_barrier(); asm volatile("" ::: "memory"); } while (0)
#define RAWBAR() do { asm volatile("" ::: "memory"); __builtin_amdgcn_s_barrier(); asm volatile("" ::: "memory"); } while (0)
#define FRESH() do { asm volatile("" : "+s"(ws), "+s"(P), "+s"(j), "+s"(w), "+s"(x), "+s"(wqkv), "+s"(wo), "+s"(w1), "+s"(w2)); asm volatile("" : "+v"(lane)); th = j >> 3; cg = j & 7; cc = w & 1; kh = (w >> 1) & 1; \
  hh = lane >> 5; tk = lane & 31; g = lane >> 4; t16 = lane & 15; q = (lane >> 2) & 3; p = lane & 3; l16 = lane * 16u; } while (0)
#define TRB0 ((4 * hh + q) * 128 + 64 * (q >> 1) + 32 * (g & 1) + 8 * p)
#define TRB1 (TRB0 ^ 64)
#define TRA ((4 * hh + q) * 64 + 32 * (g & 1) + 8 * p)
#define HEL_OWN ((u32)(P * 16 + j) * 2048u + C_HEL + 8u * tk)
#define HEL_PEER (C_HEL + (tk < 16 ? (u32)(P * 16 + tk) * 2048u + 8u * j : (u32)((tk - 16) * 16 + j) * 2048u + 128u + 8u * P))
#define PSIG(n) do { if (lane < REP) gadd1(ws, (u32)(P * 16 + lane * (16 / REP)) * 2048u + (n) * 128u); } while (0)
#define PWAIT(n) cnt_wait(ws, (u32)(P * 16 + (j & -(16 / REP))) * 2048u + (n) * 128u, 16u * (layer + 1))
constexpr int b2row(int u) { return 128 * (u % 7) + 32 * (u / 7); }
#define B2SRC(t, c) (w2 + cg * 128 + (size_t)(896 * ((c) >> 1) + (((c) & 1) ? b2row(2 * (t) + 1) : b2row(2 * (t)))) * 1024)
#define VO1024 ((lane >> 3) * 1024u + 16u * ((lane & 7) ^ ((g & 1) << 2)))
#define KOFF(PP) ((u32)((PP) * 16 + j) * 2048u + 1536u + w * 128u)
#define DRAIN() asm volatile("s_waitcnt vmcnt(0)" ::: "memory")

#define STAMP(i) do {} while (0)

template <int N> DI void waitv(u32x4& a) { asm volatile("s_waitcnt vmcnt(%1)" : "+v"(a) : "i"(N)); }
template <int N> DI void waitv4(u32x4& a, u32x4& b, u32x4& c, u32x4& d) { asm volatile("s_waitcnt vmcnt(%4)" : "+v"(a), "+v"(b), "+v"(c), "+v"(d) : "i"(N)); }
DI void waitl8(u32x4* f) { asm volatile("" : "+v"(f[0]), "+v"(f[1]), "+v"(f[2]), "+v"(f[3]), "+v"(f[4]), "+v"(f[5]), "+v"(f[6]), "+v"(f[7])::"memory"); }
DI void pin(u32x4& a) { asm volatile("" : "+v"(a)); }
DI void pin2(u32x2& a) { asm volatile("" : "+v"(a)); }
DI void pin1(u32& a) { asm volatile("" : "+v"(a)); }

template <int OFF> DI void gld16_sc1(u32x4& d, const char* sb, u32 vo) { asm volatile("global_load_dwordx4 %0, %1, %2 offset:%3 sc1" : "=v"(d) : "v"(vo), "s"(sb), "i"(OFF) : "memory"); }
template <int OFF> DI void gld16(u32x4& d, const char* sb, u32 vo) { asm volatile("global_load_dwordx4 %0, %1, %2 offset:%3" : "=v"(d) : "v"(vo), "s"(sb), "i"(OFF) : "memory"); }
template <int OFF> DI void gld8(u32x2& d, const char* sb, u32 vo) { asm volatile("global_load_dwordx2 %0, %1, %2 offset:%3" : "=v"(d) : "v"(vo), "s"(sb), "i"(OFF) : "memory"); }
DI void gld8_sc1(u32x2& d, const char* sb, u32 vo) { asm volatile("global_load_dwordx2 %0, %1, %2 sc1" : "=v"(d) : "v"(vo), "s"(sb) : "memory"); }
template <int OFF> DI void gld4o_sc1(u32& d, const char* sb, u32 vo) { asm volatile("global_load_dword %0, %1, %2 offset:%3 sc1" : "=v"(d) : "v"(vo), "s"(sb), "i"(OFF) : "memory"); }
template <int OFF> DI void gld2(u32& d, const u16* a) { asm volatile("global_load_ushort %0, %1, off offset:%2" : "=v"(d) : "v"(a), "i"(OFF) : "memory"); }
DI void gld4_sc1(u32& d, const char* sb, u32 vo) { asm volatile("global_load_dword %0, %1, %2 sc1" : "=v"(d) : "v"(vo), "s"(sb) : "memory"); }
template <int OFF> DI void gst16_sc1(char* sb, u32 vo, u32x4 d) { asm volatile("global_store_dwordx4 %0, %1, %2 offset:%3 sc1\n\ts_nop 1" ::"v"(vo), "v"(d), "s"(sb), "i"(OFF) : "memory"); }
DI void gst8_sc1(char* sb, u32 vo, u32x2 d) { asm volatile("global_store_dwordx2 %0, %1, %2 sc1" ::"v"(vo), "v"(d), "s"(sb) : "memory"); }
template <int OFF> DI void gst8(char* sb, u32 vo, u32x2 d) { asm volatile("global_store_dwordx2 %0, %1, %2 offset:%3" ::"v"(vo), "v"(d), "s"(sb), "i"(OFF) : "memory"); }
DI void gadd1(char* sb, u32 vo) { asm volatile("global_atomic_add %0, %1, %2 sc1" ::"v"(vo), "v"(1u), "s"(sb) : "memory"); }
#define OUTMOD ""
template <int OFF> DI void gst16(char* sb, u32 vo, u32x4 d) { asm volatile("global_store_dwordx4 %0, %1, %2 offset:%3 " OUTMOD "\n\ts_nop 1" ::"v"(vo), "v"(d), "s"(sb), "i"(OFF) : "memory"); }
DI void gst4_sc1(char* sb, u32 vo, u32 d) { asm volatile("global_store_dword %0, %1, %2 sc1" ::"v"(vo), "v"(d), "s"(sb) : "memory"); }
DI void glds(u32 ldsaddr, const char* sb, u32 vo) { asm volatile("s_mov_b32 m0, %0\n\ts_nop 0\n\tglobal_load_lds_dwordx4 %1, %2" ::"s"(ldsaddr), "v"(vo), "s"(sb) : "memory"); }
DI void touch(u32 ldsaddr, const char* a) { asm volatile("s_mov_b32 m0, %0\n\ts_nop 0\n\tglobal_load_lds_dword %1, off" ::"s"(ldsaddr), "v"(a) : "memory"); }
DI void touch3(u32 ldsaddr, const char* a) { touch(ldsaddr, a + (((3u - ((u32)(size_t)a >> 7)) & 7u) << 7)); }

DI bf16x8 trfrag(LAS char* p, int second) {
  s16x4 a = __builtin_amdgcn_ds_read_tr16_b64_v4i16((LAS s16x4*)p);
  s16x4 b = __builtin_amdgcn_ds_read_tr16_b64_v4i16((LAS s16x4*)(p + second));
  return __builtin_bit_cast(bf16x8, __builtin_shufflevector(a, b, 0, 1, 2, 3, 4, 5, 6, 7));
}
DI bf16x8 asb(u32x4 v) { return __builtin_bit_cast(bf16x8, v); }
DI f32x4 mfma16(bf16x8 a, bf16x8 b, f32x4 c) { return __builtin_amdgcn_mfma_f32_16x16x32_bf16(a, b, c, 0, 0, 0); }
DI f32x16 mfma(bf16x8 a, bf16x8 b, f32x16 c) { return __builtin_amdgcn_mfma_f32_32x32x16_bf16(a, b, c, 0, 0, 0); }
DI u32 pk2(float a, float b) { f32x2 v = {a, b}; return __builtin_bit_cast(u32, __builtin_convertvector(v, bf16x2)); }
DI u32 pk2v(f32x2 v) { return __builtin_bit_cast(u32, __builtin_convertvector(v, bf16x2)); }
DI u32x2 pk4(f32x4 v) { u32x2 r; r.x = pk2(v[0], v[1]); r.y = pk2(v[2], v[3]); return r; }
DI float bflo(u32 u) { return __uint_as_float(u << 16); }
DI float bfhi(u32 u) { return __uint_as_float(u & 0xffff0000u); }
DI float dot2(u32 a, u32 b, float c) { return __builtin_amdgcn_fdot2_f32_bf16(__builtin_bit_cast(bf16x2, a), __builtin_bit_cast(bf16x2, b), c, false); }
DI void swap32(float& a, float& b) { auto r = __builtin_amdgcn_permlane32_swap(__float_as_uint(a), __float_as_uint(b), false, false); a = __uint_as_float(r[0]); b = __uint_as_float(r[1]); }
DI void swap16(float& a, float& b) { auto r = __builtin_amdgcn_permlane16_swap(__float_as_uint(a), __float_as_uint(b), false, false); a = __uint_as_float(r[0]); b = __uint_as_float(r[1]); }
DI float mx2(float a, float b) { return __builtin_amdgcn_fmed3f(a, b, __builtin_inff()); }
DI float mx3(float a, float b, float c) { return __builtin_fmaxf(__builtin_fmaxf(a, b), c); }
DI void red2max(float& a, float& b) { swap32(a, b); float u = mx2(a, b); a = u; b = u; swap32(a, b); }
DI void red2sum(float& a, float& b) { swap32(a, b); float u = a + b; a = u; b = u; swap32(a, b); }
template <int CTRL> DI float dppadd(float v) { return v + __uint_as_float(__builtin_amdgcn_update_dpp(0u, __float_as_uint(v), CTRL, 0xf, 0xf, true)); }
DI float rowsum16(float v) { v = dppadd<0xB1>(v); v = dppadd<0x4E>(v); v = dppadd<0x141>(v); v = dppadd<0x140>(v); return v; }
DI f32x2 pr(const f32x16& a, int i) { return f32x2{a[2 * i], a[2 * i + 1]}; }

template <class MID> DI void softmax2(const f32x16& p, const float L2E  , u32x4 (&f)[2][2], MID&& mid) {
  float mx[2], mn[2];
#pragma unroll
  for (int hd = 0; hd < 2; ++hd) {
    const int b = 8 * hd;
    mx[hd] = mx2(mx3(mx3(mx3(p[b], p[b + 1], p[b + 2]), p[b + 3], p[b + 4]), p[b + 5], p[b + 6]), p[b + 7]);
    mn[hd] = mx2(mx3(mx3(mx3(-p[b], -p[b + 1], -p[b + 2]), -p[b + 3], -p[b + 4]), -p[b + 5], -p[b + 6]), -p[b + 7]);
    red2max(mx[hd], mn[hd]);
  }
  mid(mx, mn);
  f32x2 e[2][2][4]; float s[2][2];
#pragma unroll
  for (int hd = 0; hd < 2; ++hd) {
    const float cp = -mx[hd] * L2E, cn = -mn[hd] * L2E;
#pragma unroll
    for (int i = 0; i < 4; ++i) {
      const f32x2 v = pr(p, 4 * hd + i), a = v * L2E + cp, b = v * -L2E + cn;
      e[hd][0][i] = f32x2{__builtin_amdgcn_exp2f(a.x), __builtin_amdgcn_exp2f(a.y)};
      e[hd][1][i] = f32x2{__builtin_amdgcn_exp2f(b.x), __builtin_amdgcn_exp2f(b.y)};
    }
#pragma unroll
    for (int sg = 0; sg < 2; ++sg) { const f32x2 t = (e[hd][sg][0] + e[hd][sg][1]) + (e[hd][sg][2] + e[hd][sg][3]); s[hd][sg] = t.x + t.y; }
    red2sum(s[hd][0], s[hd][1]);
#pragma unroll
    for (int sg = 0; sg < 2; ++sg) {
      const float r = __builtin_amdgcn_rcpf(s[hd][sg]);
#pragma unroll
      for (int i = 0; i < 4; ++i) f[hd][sg][i] = pk2v(e[hd][sg][i] * r);
    }
  }
}
DI void pack16(const f32x16& a, u32x4 (&y)[2]) {
#pragma unroll
  for (int i = 0; i < 8; ++i) y[i >> 2][i & 3] = pk2v(pr(a, i));
}
DI void lnfix(f32x16& a, LAS char* Sp, u32 negmean) { a = mfma(asb(u32x4{*(LAS u32*)Sp, 0u, 0u, 0u}), asb(u32x4{negmean, 0u, 0u, 0u}), a); }
DI void lnfix4(f32x16& a, LAS char* Sp, u32 negmean) { a = mfma(asb(*(LAS u32x4*)Sp), asb(u32x4{negmean, negmean, negmean, negmean}), a); }
DI u32 split2(float v) { const u32 hi = pk2(v, v) & 0xffffu; return hi | (pk2(v - bflo(hi), 0.f) << 16); }

template <int V> struct IC { static constexpr int v = V; };
template <int B, int E, class F> DI void sfor(F&& f) { if constexpr (B < E) { f(IC<B>{}); sfor<B + 1, E>(f); } }
#define SB() __builtin_amdgcn_sched_barrier(0)
template <int NT, int K0, int K1, int D, int XM, class LD, class PRE, class POST> DI void gemm(f32x16* acc, u32x4* xf, LD&& ld, PRE&& pre, POST&& post) {
  bf16x8 wf[D + 1];
  constexpr int S0 = K0 * NT, S1 = K1 * NT;
  sfor<S0, S0 + D>([&](auto I) { constexpr int s = I.v; wf[s % (D + 1)] = ld(IC<s / NT>{}, IC<s % NT>{}); });
  SB();
  sfor<S0, S1>([&](auto I) {
    constexpr int s = I.v;
    pre(I);
    if constexpr (s + D < S1) wf[(s + D) % (D + 1)] = ld(IC<(s + D) / NT>{}, IC<(s + D) % NT>{});
    SB();
    post(I);
    acc[s % NT] = mfma(wf[s % (D + 1)], asb(xf[(s / NT) % XM]), acc[s % NT]);
    asm volatile("" : "+v"(acc[s % NT]));
    SB();
  });
}
constexpr int b2slot(int t) { return t < 4 ? t * 16384 : t == 4 || t == 13 ? 131072 : t < 9 ? 65536 + (t - 5) * 16384 : (t - 9) * 16384; }
DI LAS char* opaque(LAS char* p) { asm volatile("" : "+v"(p)); return p; }

DI void cnt_wait(const char* ctl, u32 off, u32 val) {
  for (u32 sp = 0; sp < SPIN_CAP; ++sp) {
    u32 v; gld4_sc1(v, ctl, off); asm volatile("s_waitcnt vmcnt(0)" : "+v"(v)::"memory");
    if (__builtin_amdgcn_readfirstlane(v) >= val) break;
    if (PSLEEP) __builtin_amdgcn_s_sleep(PSLEEP);
  }
}
#define HANDOFF(n) do { DRAIN(); STAMP(n == 0 ? 4 + 12 * layer : n == 1 ? 6 + 12 * layer : 12); RAWBAR(); if (w == 0) { PSIG(n); if (HOSLEEP) __builtin_amdgcn_s_sleep(HOSLEEP); PWAIT(n); } RAWBAR(); } while (0)

__global__ __launch_bounds__(512, 2) void mega(const char* x, const char* wqkv, const char* wphi, const char* wo, const char* w1, const char* w2, char* out, char* ws) {
  __shared__ __attribute__((aligned(16))) char smem[163840];
  LAS char* lds = (LAS char*)smem;
  const u32 lb = (u32)(size_t)lds;
  int lane = threadIdx.x & 63; int w = __builtin_amdgcn_readfirstlane(threadIdx.x >> 6);
  int hh = lane >> 5, tk = lane & 31, g = lane >> 4, t16 = lane & 15, q = (lane >> 2) & 3, p = lane & 3;
  const int bid = blockIdx.x, loc = bid >> 3;
  int P = (bid & 7) * 2 + (loc >> 4), j = loc & 15;
  int th = j >> 3, cg = j & 7, cc = w & 1, kh = (w >> 1) & 1;
  u32 l16 = lane * 16u;
  const u32x4 ones4 = {0x3f803f80u, 0x3f803f80u, 0x3f803f80u, 0x3f803f80u};
  const f32x16 z16 = {0.f, 0.f, 0.f, 0.f, 0.f, 0.f, 0.f, 0.f, 0.f, 0.f, 0.f, 0.f, 0.f, 0.f, 0.f, 0.f};
  const f32x4 z4 = {0.f, 0.f, 0.f, 0.f};
  STAMP(0);
  {
    size_t pc_; asm volatile("s_getpc_b64 %0" : "=s"(pc_)); const char* pc = (const char*)(pc_ & ~(size_t)255);
    const int id = (w ^ 4) * 32 + loc;
    if (id < PFC) glds(lb + LD_SC + 8192 + w * 1024, pc + id * 1024, l16);
  }
  glds(lb + LD_SC + 8192 + w * 1024, x + (size_t)(128 * P + 8 * j + w) * 1024, l16);
  if (w >= 4) {
    const u32 swz = 16u * ((lane & 7) ^ ((g & 1) << 2));
    const u32 vo1024 = (lane >> 3) * 1024u + swz, vo3584 = (lane >> 3) * 3584u + swz, voA1 = (lane >> 2) * 3072u + 16u * (lane & 3);
    u32x4 wa1[16];
    u32x4 wb2[36];
    LAS char* wl = lds + l16;
#define HW (w & 3)
#define GLDS_Q(n) glds(lb + 65536 + (n) * 1024, wqkv + 64 * j + (size_t)((n) * 16) * 3072, voA1)
#define GLDS_B1(n) glds(lb + (n) * 1024, w1 + (size_t)(((n) & 63) * 8) * 3584 + j * 256 + ((n) >> 6) * 128, vo3584)
#define GLDS_A3(n) glds(lb + LD_WA3 + (n) * 1024, wo + (size_t)(n) * 8192 + cg * 128, vo1024)
    auto putA1 = [&](auto R, auto M) { *(LAS u32x4*)(wl + R.v * 32768 + (4 * M.v + HW) * 1024) = wa1[R.v * 8 + M.v]; };
    auto putB2 = [&](auto T) { sfor<0, 4>([&](auto I) { *(LAS u32x4*)(wl + b2slot(T.v) + (4 * HW + I.v) * 1024) = wb2[(T.v - 5) * 4 + I.v]; }); };
    auto ldB2 = [&](auto T) { sfor<0, 4>([&](auto I) { gld16<0>(wb2[(T.v - 5) * 4 + I.v], B2SRC(T.v, HW) + I.v * 8192, vo1024); }); };
    auto ldB1b = [&](u32x4* r, auto I0, auto I1) { const char* b = w1 + (size_t)(HW * 128) * 3584 + (j < 14 ? j : 13) * 256 + 128; sfor<I0.v, I1.v>([&](auto I) { gld16<0>(r[I.v], b + (size_t)(I.v * 8) * 3584, vo3584); }); };
    auto putB1b = [&](u32x4* r) { sfor<0, 16>([&](auto I) { pin(r[I.v]); *(LAS u32x4*)(wl + 65536 + (HW * 16 + I.v) * 1024) = r[I.v]; }); };
    auto colsum8 = [&](LAS char* sp, int kstride, int second, f32x4& a, f32x4& b) {
      const u32 o = ((t16 >> 2) & 1) == (g & 1) ? 0x3f803f80u : 0u; const u32x4 sel = {o, o, o, o};
      bf16x8 wf[8];
      sfor<0, 8>([&](auto K) { wf[K.v] = trfrag(sp + K.v * kstride, second); });
      sfor<0, 4>([&](auto K) { a = mfma16(asb(sel), wf[2 * K.v], a); b = mfma16(asb(sel), wf[2 * K.v + 1], b); });
      asm volatile("" : "+v"(a), "+v"(b)); SB();
    };
    auto putS = [&](u32 off, float a) { const u32 sp = split2(a); *(LAS u32*)(lds + LD_S + off) = __builtin_amdgcn_perm(sp, sp, hh ? 0x03020302u : 0x01000100u); };
    const int lc = (P & 1) * 16 + j;
#define T3_KV() do { if (lane < 32) touch3(lb + 159 * 1024, wqkv + (size_t)(16 * lc + t16) * 3072 + 1024 + g * 1024); } while (0)
#define T3_A() do { if (lane < 32) touch3(lb + 159 * 1024, g == 0 ? wqkv + (size_t)(16 * lc + t16) * 3072 : wo + (size_t)(16 * lc + t16) * 1024); } while (0)
#define T3_B2() do { const int i_ = 20 * lc + lane; if (lane < 20) touch3(lb + 159 * 1024, w2 + (size_t)(896 * ((i_ >> 5) & 1) + 128 * (i_ >> 6) - (i_ < 448 ? 0 : 864) + (i_ & 31)) * 1024); } while (0)
#define T3_W(wm) touch3(lb + 159 * 1024, (wm) + (size_t)(56 * lc + (lane < 56 ? lane : 55)) * 1024)
    {
      if (HW == 0) T3_KV();
      if (HW == 1) *(LAS u32x2*)(lds + LD_ID + lane * 8) = g == p ? u32x2{q == 0 ? 0x3f80u : q == 1 ? 0x3f800000u : 0u, q == 2 ? 0x3f80u : q == 3 ? 0x3f800000u : 0u} : u32x2{0u, 0u};
      u32x2 h0 = {0u, 0u};
      if (HW == 3) {
        gld8_sc1(h0, ws, HEL_OWN); gst16_sc1<0>(ws + (size_t)(P * 16 + j) * 2048, l16, u32x4{0u, 0u, 0u, 0u}); gst16_sc1<1536>(ws + (size_t)(P * 16 + j) * 2048, tk * 16u, u32x4{0u, 0u, 0u, 0u});
      }
      const char* wb = wqkv + 64 * j + (size_t)(16 * HW) * 3072;
      __builtin_amdgcn_s_sleep(HSLEEP);
      sfor<0, 8>([&](auto M) { gld16<1024>(wa1[M.v], wb + (size_t)(64 * M.v) * 3072, voA1); gld16<2048>(wa1[8 + M.v], wb + (size_t)(64 * M.v) * 3072, voA1); });
      auto touches = [&] {
        const u32 dump = lb + 159 * 1024;
        const int id = P * 16 + j;
        if ((TMASK & 1) && HW == 3) { touch(dump, lane < 56 ? w2 + (size_t)id * 7168 + lane * 128 : wo + (size_t)id * 2048 + (lane - 56) * 256); }
        if ((TMASK & 8) && HW == 0) { touch(dump, lane < 56 ? w1 + (size_t)id * 7168 + lane * 128 : wo + (size_t)id * 2048 + (lane - 56) * 256 + 128); }
        const u32 ly = HW & 1, pg = lane & 7;
        const u32 to = HW < 2 ? (u32)WS_H + ly * (7u << 20) + P * 458752u + (pg < 7 ? pg : 6) * 65536u
                              : (pg < 4 ? (u32)WS_O + (pg >> 1) * (4u << 20) + ly * (2u << 20) : pg < 6 ? (u32)WS_X2 : (u32)WS_ST1 + ly * (1u << 20)) + (pg < 6 ? P * 131072u + (pg & 1) * 65536u : (pg & 1) * (2u << 20));
        if ((TMASK & 2) && lane < 8) touch(dump, ws + to);
        if ((TMASK & 4) && HW == 1) T3_A();
        if ((TMASK & 16) && HW == 1) T3_W(w1);
        if ((TMASK & 32) && HW == 2) T3_W(w2);
      };
      if (TPOS == 0) touches();
      STAMP(44);
      RAWBAR();
      f32x4 sk = z4, sv = z4;
      u32x4 sel;
#pragma unroll
      for (int e = 0; e < 4; ++e) sel[e] = ((t16 & 7) >> 1) == e ? ((t16 & 1) ? 0x3f800000u : 0x00003f80u) : 0u;
      sfor<0, 4>([&](auto C) {
        constexpr int c = C.v;
        waitv4<(TPOS == 0 && (TMASK & 2) ? 13 : 12) - 4 * c>(wa1[2 * c], wa1[2 * c + 1], wa1[8 + 2 * c], wa1[8 + 2 * c + 1]);
        if (c == 0 && HW == 3) { pin2(h0); if (lane < 32) gst8_sc1(ws, HEL_PEER, u32x2{h0.y, h0.x + 2u}); }
        putA1(IC<0>{}, IC<2 * c>{}); putA1(IC<0>{}, IC<2 * c + 1>{}); putA1(IC<1>{}, IC<2 * c>{}); putA1(IC<1>{}, IC<2 * c + 1>{});
        sk = mfma16(asb(sel), asb(wa1[2 * c]), sk); sv = mfma16(asb(sel), asb(wa1[8 + 2 * c]), sv);
        sk = mfma16(asb(sel), asb(wa1[2 * c + 1]), sk); sv = mfma16(asb(sel), asb(wa1[8 + 2 * c + 1]), sv);
        asm volatile("" : "+v"(sk), "+v"(sv));
        if (c == 3) {
          LAS char* tb = lds + LD_SC + 16384 + HW * 2048;
          *(LAS f32x4*)(tb + l16) = sk; *(LAS f32x4*)(tb + 1024 + l16) = sv;
          LAS float* tr = (LAS float*)(tb + (q & 1) * 256 + (tk >> 3) * 16 + p * 4);
          float a = tr[0], b = tr[16], c_ = tr[32], d = tr[48], e = tr[256], f = tr[272], g_ = tr[288], h = tr[304];
          a += b; asm volatile("" : "+v"(a)); c_ += d; asm volatile("" : "+v"(c_)); e += f; asm volatile("" : "+v"(e)); g_ += h; asm volatile("" : "+v"(g_));
          a += c_; asm volatile("" : "+v"(a)); e += g_;
          putS(l16 + 4 * HW, a); putS(1024 + l16 + 4 * HW, e);
        }
        STAMP(29 + (c == 3 ? 4 : c));
        if (c & 1) BAR();
        if (c & 1) __builtin_amdgcn_s_setprio(c == 1 ? 3 : 0);
      });
      u32x2 hv;
      if (HW == 3) gld8_sc1(hv, ws, HEL_OWN);
      if (TPOS == 1) touches();
      _Pragma("unroll 4") for (int i = 0; i < 8; ++i) GLDS_Q(HW * 8 + i);
      ldB1b(wb2 + 20, IC<0>{}, IC<B1E>{});
      asm volatile("s_waitcnt vmcnt(%0)" ::"i"(8 + B1E) : "memory");
      if (HW == 3) {
        pin2(hv);
        u32 sent = h0.x;
        for (u32 sp_ = 0; sp_ < SPIN_CAP; ++sp_) {
          if (hv.x != sent) { sent = hv.x; if (lane < 32) gst8_sc1(ws, HEL_PEER, u32x2{h0.y, hv.x + 1u}); }
          if (__all(hv.y != h0.y)) break;
          gld8_sc1(hv, ws, HEL_OWN); asm volatile("s_waitcnt vmcnt(0)" : "+v"(hv)::"memory");
        }
      }
      STAMP(32);
      sfor<5, 5 + NEARLY>(ldB2);
      STAMP(39);
      asm volatile("s_waitcnt vmcnt(%0)" ::"i"(B1E + 4 * NEARLY) : "memory");
      RAWBAR();
      { f32x4 a = z4, b = z4; colsum8(lds + 65536 + HW * 8192 + TRA, 1024, 512, a, b); putS(2048 + l16 + 4 * HW, a[0] + b[0]); }
      BAR();
      if (TPOS == 2) touches();
    }
    sfor<0, 2>([&](auto L) {
      constexpr int layer = L.v;
      FRESH();
      if constexpr (layer == 1) {
        RAWBAR();
        ldB1b(wa1, IC<0>{}, IC<16>{});
        _Pragma("unroll 4") for (int i = 0; i < 8; ++i) GLDS_A3(32 + HW * 8 + i);
        STAMP(42);
        RAWBAR();
        RAWBAR();
      }
      if (j < 14) { _Pragma("unroll 4") for (int i = 0; i < 8; ++i) GLDS_B1(HW * 16 + i); }
      if constexpr (layer == 0) { _Pragma("unroll 4") for (int i = 0; i < 8; ++i) GLDS_A3(32 + HW * 8 + i); }
      STAMP(36);
      RAWBAR();
      _Pragma("unroll 4") for (int i = 0; i < A3PRE; ++i) GLDS_A3(HW * 8 + i);
      STAMP(48);
      RAWBAR();
      _Pragma("unroll 4") for (int i = A3PRE; i < 8; ++i) GLDS_A3(HW * 8 + i);
      RAWBAR();
      if constexpr (layer == 0) { ldB1b(wb2 + 20, IC<B1E>{}, IC<16>{}); sfor<5 + NEARLY, 5 + NEARLY + NW4>(ldB2); asm volatile("s_waitcnt vmcnt(%0)" ::"i"(4 * NW4 + 16 - B1E) : "memory"); } else DRAIN();
      RAWBAR();
      __builtin_amdgcn_s_sleep(A3SLEEP);
      if (j < 14) { _Pragma("unroll 4") for (int i = 8; i < 16; ++i) GLDS_B1(HW * 16 + i); }
      if ((TMASK & 256) && HW == 1) T3_B2();
      if constexpr (layer == 0) sfor<5 + NEARLY + NW4, 10 - NW6>(ldB2);
      RAWBAR();
      if constexpr (layer == 0 && B1E < 16) asm volatile("s_waitcnt vmcnt(%0)" ::"i"(4 * (5 - NEARLY - NW6)) : "memory");
      putB1b(layer == 0 ? wb2 + 20 : wa1);
      DRAIN();
      BAR();
      FRESH();
      if constexpr (layer == 0) sfor<10 - NW6, 10>(ldB2);
      if (layer == 0 && j < 14) {
        f32x4 aS = z4, aT = z4; LAS char* sp = lds + (HW >> 1) * 65536 + ((HW & 1) ? TRB1 : TRB0);
        sfor<0, 4>([&](auto C) { colsum8(sp + C.v * 16384, 2048, 1024, aS, aT); });
        putS(3072 + HW * 256 + lane * 4, aS[0] + aT[0]);
      }
      BAR();
      RAWBAR();
      auto fill = [&](auto T) { sfor<0, 4>([&](auto I) { glds(lb + b2slot(T.v) + (HW * 4 + I.v) * 1024, B2SRC(T.v, HW) + I.v * 8192, vo1024); }); };
      fill(IC<0>{}); fill(IC<1>{}); fill(IC<2>{}); fill(IC<4>{});
      if constexpr (layer == 0) sfor<10, 10 + NB4>(ldB2);
      STAMP(25);
      RAWBAR();
      fill(IC<3>{});
      if constexpr (layer == 0) sfor<10 + NB4, 14>(ldB2);
      DRAIN();
      sfor<0, 36>([&](auto I) { pin(wb2[I.v]); });
      STAMP(26);
      RAWBAR();
      if (HW == 1) PSIG(2);
      if (HW == 0) { __builtin_amdgcn_s_sleep(PW2SLEEP); PWAIT(2); }
      STAMP(35);
      RAWBAR();
      FRESH();
      putB2(IC<5>{}); putB2(IC<6>{}); putB2(IC<7>{}); if (P8EARLY) putB2(IC<8>{});
      STAMP(56);
      BAR();
      if (HW == 1) PSIG(5);
      if (!P8EARLY) putB2(IC<8>{});
      if (HW == 0) { __builtin_amdgcn_s_sleep(PW5SLEEP); PWAIT(5); }
      STAMP(57);
      BAR();
      putB2(IC<9>{}); putB2(IC<10>{}); putB2(IC<11>{});
      STAMP(59);
      BAR();
      if constexpr (layer == 0) { if ((TMASK & 128) && HW == 1) T3_A(); if ((TMASK & 64) && HW == 2) T3_W(w1); }
      putB2(IC<12>{}); putB2(IC<13>{});
      STAMP(61);
      BAR();
      if constexpr (layer == 0) { _Pragma("unroll 4") for (int i = 0; i < 8; ++i) GLDS_Q(HW * 8 + i); }
      if constexpr (layer == 0) sfor<0, 8>([&](auto M) { putA1(IC<0>{}, M); });
      RAWBAR();
      if constexpr (layer == 0) {
        sfor<0, 8>([&](auto M) { putA1(IC<1>{}, M); });
        RAWBAR(); DRAIN(); BAR();
      }
    });
  } else {
    u32x4 resid[2];
    u32x4 wph[2];
    u32x4 xf[32];
    f32x16 acc[4];
    float rstd; u32 nmean;
    auto nop = [&](auto) {};
#define LDF(i, n) gld16_sc1<((n) & 3) * 1024>(xf[i], xb + ((n) >> 2) * 4096, l16)
#define FINSTATS(sum, ssq) { red2sum(sum, ssq); const float mean = sum * (1.f / 512.f); rstd = __builtin_amdgcn_rsqf(mx2(ssq * (1.f / 512.f) - mean * mean, 0.f) + 1e-5f); nmean = split2(-mean); }
#define STATS() { _Pragma("unroll") for (int i = 0; i < 4; ++i) pin(st[i]); \
      const f32x4 v = (__builtin_bit_cast(f32x4, st[0]) + __builtin_bit_cast(f32x4, st[1])) + (__builtin_bit_cast(f32x4, st[2]) + __builtin_bit_cast(f32x4, st[3])); \
      float a = v[0], b = v[1], c = v[2], d = v[3]; swap16(a, c); swap16(b, d); float sum = a + c, ssq = b + d; FINSTATS(sum, ssq) }
#define LDSTATS(SB_) { const char* sb = (SB_) + (size_t)(4 * P + w) * 4096; \
      gld16_sc1<0>(st[0], sb, l16); gld16_sc1<1024>(st[1], sb, l16); gld16_sc1<2048>(st[2], sb, l16); gld16_sc1<3072>(st[3], sb, l16); }
    constexpr int N0 = N0V;
#define STREAM32(NT, SB_) { constexpr int kb_ = s / NT; if constexpr (s % NT == 0) waitv<(N0 + kb_ < 36 ? N0 + kb_ : 36) - kb_ - 1>(xf[kb_]); \
      if constexpr (s % NT == 1 && N0 + kb_ < 32) LDF(N0 + kb_, N0 + kb_); \
      if constexpr (s % NT == 1 && N0 + kb_ >= 32 && N0 + kb_ < 36) gld16_sc1<(N0 + kb_ - 32) * 1024>(st[(N0 + kb_) & 3], (SB_) + (size_t)(4 * P + w) * 4096, l16); }
#define RES2ACC() { _Pragma("unroll") for (int i = 0; i < 8; ++i) { const u32 r = resid[i >> 2][i & 3]; acc[0][2 * i] = bflo(r); acc[0][2 * i + 1] = bfhi(r); } acc[1] = z16; asm volatile("" : "+v"(acc[0])); }
    for (int layer = 0; layer < 2; ++layer) {
      FRESH();
      acc[0] = z16; acc[1] = z16; acc[2] = z16;
      LAS char* wa1p = opaque(lds + TRA);
      auto ldkv = [&](auto KB, auto T) { return trfrag(wa1p + T.v * 32768 + KB.v * 1024, 512); };
      u32x4 sk, sv; LAS char* kt; LAS char* ka; u32 kvo;
      char* kb_ = ws + WS_KVP + (size_t)((layer * 16 + j) * 4 + w) * KVP_UNIT; asm volatile("" : "+s"(kb_));
      auto kvtail = [&](auto I) {
        if constexpr (I.v == 50) { kt = lds + LD_SC + (32 * w + tk) * 200 + 8 * hh; asm volatile("" : "+v"(kt)); }
        if constexpr (I.v == 52) { ka = lds + LD_SC + (4 * g + q) * 200 + 8 * p + (w >> 1) * 96; asm volatile("" : "+v"(ka)); }
        if constexpr (I.v == 54) { kvo = P * 512u + hh * 256u + t16 * 16u; asm volatile("" : "+v"(kvo)); }
        if constexpr (I.v == 56) { sk = *(LAS u32x4*)(lds + LD_S + l16); sv = *(LAS u32x4*)(lds + LD_S + 1024 + l16); }
        if constexpr (I.v == 61) { acc[0] = mfma(asb(sk), asb(u32x4{nmean, nmean, nmean, nmean}), acc[0]); asm volatile("" : "+v"(acc[0])); }
        if constexpr (I.v == 62) { acc[1] = mfma(asb(sv), asb(u32x4{nmean, nmean, nmean, nmean}), acc[1]); asm volatile("" : "+v"(acc[1])); }
      };
      if (layer == 0) {
        const char* xb = x + (size_t)(128 * P + 32 * w) * 1024;
        u32 xvo[4];
#pragma unroll
        for (int i = 0; i < 4; ++i) xvo[i] = g * 1024u + ((t16 ^ (4 * i + g)) * 16u);
        const u32 stg = lb + w * 8192;
        auto ldx = [&](auto R) { sfor<0, 8>([&](auto I) { glds(stg + R.v * 32768 + I.v * 1024, xb + R.v * 256 + I.v * 4096, xvo[I.v & 3]); }); };
        ldx(IC<0>{}); ldx(IC<1>{}); ldx(IC<2>{}); ldx(IC<3>{});
        u32 wp[8];
        const u16* wsrc = (const u16*)wphi + (2 * j + (tk >> 4)) * 256 + (4 * hh) * 16 + t16;
        auto ldwp = [&] { sfor<0, 8>([&](auto I) { gld2<(I.v >> 2) * 256 + (I.v & 3) * 32>(wp[I.v], wsrc); }); };
        if (WPOS == 0) ldwp();
        STAMP(32);
        const u32 str = w * 8192 + tk * 256 + t16 * 16 + 8 * hh;
        float sm[4] = {0.f, 0.f, 0.f, 0.f}, sq[4] = {0.f, 0.f, 0.f, 0.f};
        auto stats = [&](auto R) {
          constexpr int b = 8 * R.v;
          sfor<b, b + 8>([&](auto I) {
#pragma unroll
            for (int e = 0; e < 4; ++e) { sm[e] = dot2(xf[I.v][e], 0x3f803f80u, sm[e]); sq[e] = dot2(xf[I.v][e], xf[I.v][e], sq[e]); }
          });
          asm volatile("" : "+v"(sm[0]), "+v"(sm[1]), "+v"(sm[2]), "+v"(sm[3]), "+v"(sq[0]), "+v"(sq[1]), "+v"(sq[2]), "+v"(sq[3]));
          if ((cg >> 1) == R.v && (w >> 1) == th) {
            LAS char* rx = lds + LD_SC + cc * 4096 + l16;
            if (cg & 1) { _Pragma("unroll") for (int i = 0; i < 4; ++i) *(LAS u32x4*)(rx + i * 1024) = xf[b + 4 + i]; }
            else { _Pragma("unroll") for (int i = 0; i < 4; ++i) *(LAS u32x4*)(rx + i * 1024) = xf[b + i]; }
          }
        };
        sfor<0, 4>([&](auto R) {
          constexpr int b = 8 * R.v;
          asm volatile("s_waitcnt vmcnt(%0)" ::"i"((WPOS == 0 ? 32 : 24) - b) : "memory");
#pragma unroll
          for (int i = 0; i < 8; ++i) {
            u32x2 lo = *(LAS u32x2*)(lds + ((str ^ (32 * i)) + R.v * 32768)), hi = *(LAS u32x2*)(lds + ((str ^ (32 * i + 16)) + R.v * 32768));
            xf[b + i] = u32x4{lo.x, lo.y, hi.x, hi.y};
          }
          SB();
          if constexpr (R.v > 0) stats(IC<R.v - 1>{});
          STAMP(29 + (R.v == 3 ? 2 : R.v));
          if constexpr (R.v == BXR) RAWBAR();
        });
        stats(IC<3>{});
        float sum = (sm[0] + sm[1]) + (sm[2] + sm[3]), ssq = (sq[0] + sq[1]) + (sq[2] + sq[3]);
        FINSTATS(sum, ssq)
        auto mkwph = [&] {
          DRAIN();
#pragma unroll
          for (int i = 0; i < 8; ++i) pin1(wp[i]);
          const u32x4 wv = {wp[0] | (wp[1] << 16), wp[2] | (wp[3] << 16), wp[4] | (wp[5] << 16), wp[6] | (wp[7] << 16)};
          const u32x4 zz = {0u, 0u, 0u, 0u};
          wph[0] = tk < 16 ? wv : zz; wph[1] = tk < 16 ? zz : wv;
          pin(wph[0]); pin(wph[1]);
        };
        asm volatile("" : "+v"(rstd), "+v"(nmean));
        if (WPOS == 0) mkwph(); else ldwp();
        STAMP(33);
        BAR();
        STAMP(54);
        resid[0] = *(LAS u32x4*)(lds + LD_SC + cc * 4096 + kh * 2048 + l16); resid[1] = *(LAS u32x4*)(lds + LD_SC + cc * 4096 + kh * 2048 + 1024 + l16);
        gemm<2, 0, 32, 4, 32>(acc, xf, ldkv, [&](auto I) { if constexpr (I.v == 28) { pin(resid[0]); pin(resid[1]); if (WPOS == 1) mkwph(); STAMP(44); RAWBAR(); STAMP(45); }   }, kvtail);
      } else {
        u32x4 st[4];
        const char* xb = ws + WS_X2 + (size_t)(4 * P + w) * 32768;
        asm volatile("" : "+s"(xb));
        { constexpr int layer = 0; HANDOFF(4); }
        STAMP(40);
        sfor<0, N0>([&](auto I) { LDF(I.v, I.v); });
        STAMP(41);
        gemm<2, 0, 32, 4, 32>(acc, xf, ldkv, [&](auto I) {
          constexpr int s = I.v;
          if constexpr (s == 2 * (36 - N0)) RAWBAR();
          STREAM32(2, ws + WS_ST2)
          if constexpr (s == 59) DRAIN();
        }, [&](auto I) { if constexpr (I.v == 59) { STATS() asm volatile("" : "+v"(rstd), "+v"(nmean)); } kvtail(I); });
      }
      STAMP(1 + 12 * layer);
      {
        u32x4 yk[2], yv[2], f[2][2];
        pack16(acc[0], yk);
        f32x16 pp = mfma(asb(wph[0]), asb(yk[0]), z16); pp = mfma(asb(wph[1]), asb(yk[1]), pp);
        pack16(acc[1] * rstd, yv);
#pragma unroll
        for (int hd = 0; hd < 2; ++hd) { *(LAS u32x2*)(kt + hd * 96 + 64) = u32x2{yv[hd].x, yv[hd].y}; *(LAS u32x2*)(kt + hd * 96 + 80) = u32x2{yv[hd].z, yv[hd].w}; }
        softmax2(pp, rstd * 1.4426950408889634f, f, [](float*, float*) {});
#pragma unroll
        for (int hd = 0; hd < 2; ++hd) {
          *(LAS u32x2*)(kt + hd * 96) = u32x2{f[hd][0].x, f[hd][0].y}; *(LAS u32x2*)(kt + hd * 96 + 16) = u32x2{f[hd][0].z, f[hd][0].w};
          *(LAS u32x2*)(kt + hd * 96 + 32) = u32x2{f[hd][1].x, f[hd][1].y}; *(LAS u32x2*)(kt + hd * 96 + 48) = u32x2{f[hd][1].z, f[hd][1].w};
        }
      }
      STAMP(42);
      BAR();
      STAMP(43);
      {
        f32x4 akv = {0.f, 0.f, 0.f, 0.f}, az = {0.f, 0.f, 0.f, 0.f};
        bf16x8 A[4], B[4];
#pragma unroll
        for (int ks = 0; ks < 4; ++ks) { A[ks] = trfrag(ka + ks * 6400 + (w & 1) * 32, 3200); B[ks] = trfrag(ka + ks * 6400 + 64, 3200); }
        SB();
#pragma unroll
        for (int ks = 0; ks < 4; ++ks) { az = mfma16(asb(ones4), A[ks], az); akv = mfma16(A[ks], B[ks], akv); }
        const u32x2 kp = pk4(akv);
        auto r0 = __builtin_amdgcn_permlane16_swap(kp.x, kp.x, false, false), r1 = __builtin_amdgcn_permlane16_swap(kp.y, kp.y, false, false);
        gst16_sc1<0>(kb_, kvo, u32x4{r0[0], r1[0], r0[1], r1[1]});
        gst4_sc1(kb_, 8192u + (kvo >> 9) * 64u + ((kvo >> 2) & 60u), __float_as_uint(az[0]));
      }
      STAMP(37);
      FRESH();
      {
        u32x4 phq[2][2], yq[2], sq_, sel[2];
        u32 kc, zz[4], gvo;
        LAS char* gi; LAS char* gr; char* ob;
        LAS char* wq = opaque(lds + 65536 + TRA);
        gemm<1, 0, 32, 4, 32>(acc + 2, xf, [&](auto KB, auto) { return trfrag(wq + KB.v * 1024, 512); }, [&](auto I) {
          if constexpr (I.v == KSTEP) { DRAIN(); if (lane < 16) gadd1(ws, KOFF(lane)); STAMP(2 + 12 * layer); RAWBAR();   }
        }, [&](auto I) {
          if constexpr (I.v == 2) { gi = lds + LD_SC + 16384 + w * 680 + 8 * g; asm volatile("" : "+v"(gi)); }
          if constexpr (I.v == 4) { gr = lds + LD_SC + 16384 + (tk < 16 ? tk : 16) * 40 + 8 * hh; asm volatile("" : "+v"(gr)); }
          if constexpr (I.v == 6) { ob = ws + WS_O + (size_t)layer * (2u << 20) + ((size_t)(4 * P + w) * 32 + 2 * j) * 1024; gvo = g * 512u + t16 * 16u; asm volatile("" : "+s"(ob), "+v"(gvo)); }
          if constexpr (I.v == 24) sq_ = *(LAS u32x4*)(lds + LD_S + 2048 + l16);
          if constexpr (I.v == 30) { acc[2] = mfma(asb(sq_), asb(u32x4{nmean, nmean, nmean, nmean}), acc[2]); asm volatile("" : "+v"(acc[2])); }
        });
        STAMP(36);
        gld4_sc1(kc, ws, KOFF(P));
        sel[0] = *(LAS u32x4*)(lds + LD_ID + t16 * 32); sel[1] = *(LAS u32x4*)(lds + LD_ID + t16 * 32 + 16);
        pack16(acc[2], yq);
        f32x16 pp = mfma(asb(wph[0]), asb(yq[0]), z16); pp = mfma(asb(wph[1]), asb(yq[1]), pp);
        softmax2(pp, rstd * 1.4426950408889634f, phq, [&](float* mx, float* mn) {
          asm volatile("" : "+v"(mx[0]), "+v"(mx[1]), "+v"(mn[0]), "+v"(mn[1])); SB();
          STAMP(55);
          asm volatile("s_waitcnt vmcnt(0)" : "+v"(kc)::"memory");
          if (__builtin_amdgcn_readfirstlane(kc) < 16u * (layer + 1)) cnt_wait(ws, KOFF(P), 16u * (layer + 1));
          STAMP(3 + 12 * layer);
          sfor<0, 4>([&](auto I) { gld4o_sc1<I.v * 256>(zz[I.v], kb_ + 8192, lane * 4u); });
          sfor<0, 8>([&](auto I) { gld16_sc1<((I.v >> 1) & 1) * 2048 + (I.v & 1) * 256>(xf[I.v], kb_ + (I.v >> 2) * 4096, gvo); });
          RAWBAR();
          STAMP(46);
          SB();
        });
        pin(phq[0][0]); pin(phq[0][1]); pin(phq[1][0]); pin(phq[1][1]);
        STAMP(47);
        asm volatile("s_waitcnt vmcnt(8)" : "+v"(zz[0]), "+v"(zz[1]), "+v"(zz[2]), "+v"(zz[3]));
        f32x4 sz = __builtin_amdgcn_mfma_f32_16x16x4f32((__uint_as_float(zz[0]) + __uint_as_float(zz[1])) + (__uint_as_float(zz[2]) + __uint_as_float(zz[3])), 1.f, z4, 0, 0, 0);
        asm volatile("" : "+v"(sz)); SB();
        f32x4 s0 = z4, s1 = z4;
        sfor<0, 4>([&](auto I) {
          waitv<7 - 2 * I.v>(xf[2 * I.v]); s0 = mfma16(asb(sel[0]), asb(xf[2 * I.v]), s0);
          waitv<6 - 2 * I.v>(xf[2 * I.v + 1]); s1 = mfma16(asb(sel[1]), asb(xf[2 * I.v + 1]), s1);
          SB();
        });
        *(LAS u32x2*)(gi + 640) = pk4(sz);
        s0 += s1;
        *(LAS u32x2*)(gi + t16 * 40) = pk4(s0);
        STAMP(48);
        BAR();
        STAMP(49);
        f32x16 o[2]; u32x4 kf[4];
#pragma unroll
        for (int i = 0; i < 4; ++i) { const u32x2 a = *(LAS u32x2*)(gr + i * 680), b = *(LAS u32x2*)(gr + i * 680 + 16); kf[i] = u32x4{a.x, a.y, b.x, b.y}; }
        SB();
        o[0] = mfma(asb(kf[0]), asb(phq[0][0]), z16); o[1] = mfma(asb(kf[2]), asb(phq[1][0]), z16);
        o[0] = mfma(asb(kf[1]), asb(phq[0][1]), o[0]); o[1] = mfma(asb(kf[3]), asb(phq[1][1]), o[1]);
        asm volatile("" : "+v"(o[0]), "+v"(o[1])); SB();
        sfor<0, 2>([&](auto HD) {
          constexpr int hd = HD.v;
          const float r = __builtin_amdgcn_rcpf(o[hd][8] + 1e-6f);
          gst16_sc1<hd * 1024>(ob, l16, u32x4{pk2v(pr(o[hd], 0) * r), pk2v(pr(o[hd], 1) * r), pk2v(pr(o[hd], 2) * r), pk2v(pr(o[hd], 3) * r)});
        });
        STAMP(50);
        RES2ACC()
      }
#define TILE_REDUCE(EXOFF) { LAS char* ex = lds + (EXOFF) + l16; \
        _Pragma("unroll") for (int i = 0; i < 4; ++i) *(LAS f32x4*)(ex + w * 4096 + i * 1024) = f32x4{acc[1][4 * i], acc[1][4 * i + 1], acc[1][4 * i + 2], acc[1][4 * i + 3]}; \
        BAR(); \
        f32x4 rd[4]; _Pragma("unroll") for (int i = 0; i < 4; ++i) rd[i] = *(LAS f32x4*)(ex + (w ^ 2) * 4096 + i * 1024); SB(); \
        _Pragma("unroll") for (int i = 0; i < 4; ++i) { const f32x4 v = rd[i] + f32x4{acc[0][4 * i], acc[0][4 * i + 1], acc[0][4 * i + 2], acc[0][4 * i + 3]}; \
          resid[i >> 1][2 * (i & 1)] = pk2(v[0], v[1]); resid[i >> 1][2 * (i & 1) + 1] = pk2(v[2], v[3]); } }
#define TILE_PUBLISH(xo, so) { gst16_sc1<0>(xo, l16, resid[0]); gst16_sc1<1024>(xo, l16, resid[1]); \
        float sum = 0.f, ssq = 0.f, sum2 = 0.f, ssq2 = 0.f; \
        _Pragma("unroll") for (int e = 0; e < 4; ++e) { sum = dot2(resid[0][e], 0x3f803f80u, sum); ssq = dot2(resid[0][e], resid[0][e], ssq); sum2 = dot2(resid[1][e], 0x3f803f80u, sum2); ssq2 = dot2(resid[1][e], resid[1][e], ssq2); } \
        sum += sum2; ssq += ssq2; red2sum(sum, ssq); sum2 = sum; ssq2 = ssq; swap16(sum, sum2); swap16(ssq, ssq2); \
        if (lane < 16) gst16_sc1<0>(so, l16, u32x4{__float_as_uint(sum), __float_as_uint(ssq), __float_as_uint(sum2), __float_as_uint(ssq2)}); }
      FRESH();
      {
        const size_t ch = (size_t)(4 * P + 2 * th + cc);
        const char* xb = ws + WS_O + (size_t)layer * (2u << 20) + (ch * 32 + 16 * kh) * 1024;
        char* xo = ws + WS_X1 + (size_t)layer * (2u << 20) + (ch * 32 + 4 * cg + 2 * kh) * 1024;
        char* so = ws + WS_ST1 + (size_t)layer * (256u << 10) + (ch * 16 + 2 * cg + kh) * 256;
        LAS char* wa[2] = {opaque(lds + LD_WA3 + kh * 32768 + (TRB0 ^ (64 * kh))), opaque(lds + LD_WA3 + kh * 32768 + (TRB1 ^ (64 * kh)))};
        asm volatile("" : "+s"(xb), "+s"(xo), "+s"(so));
        HANDOFF(0);
        STAMP(5 + 12 * layer);
        sfor<0, N0>([&](auto I) { LDF(I.v, I.v); });
        STAMP(51);
        gemm<2, 0, 16, 4, 32>(acc, xf, [&](auto KB, auto T) { return trfrag(wa[T.v] + KB.v * 2048, 1024); }, [&](auto I) {
          constexpr int s = I.v;
          if constexpr (s % 2 == 0) waitv<(N0 + s / 2 < 16 ? N0 + s / 2 : 16) - s / 2 - 1>(xf[s / 2]);
          if constexpr (s % 2 == 1 && N0 + s / 2 < 16) LDF(N0 + s / 2, N0 + s / 2);
        }, nop);
        STAMP(52);
        TILE_REDUCE(LD_SC)
        STAMP(53);
        TILE_PUBLISH(xo, so)
      }
      FRESH();
      const char* hx = ws + WS_H + (size_t)layer * (7u << 20) + ((size_t)(4 * P + 2 * th + cc) * 112 + 56 * kh) * 1024;
#define LDH(i) gld16_sc1<((2 * ((i) / 14) + (i) % 2) & 3) * 1024>(xf[(i) % 32], hx + (2 * (((i) % 14) / 2) + (i) / 28) * 4096, l16)
      {
        const char* xb = ws + WS_X1 + (size_t)layer * (2u << 20) + (size_t)(4 * P + w) * 32768;
        char* hb = ws + WS_H + (size_t)layer * (7u << 20) + ((size_t)(4 * P + w) * 112 + 8 * j) * 1024;
        LAS char* wa[4] = {opaque(lds + TRB0), opaque(lds + TRB1), opaque(lds + 65536 + TRB0), opaque(lds + 65536 + TRB1)};
        asm volatile("" : "+s"(xb), "+s"(hb), "+s"(hx));
        HANDOFF(1);
        STAMP(7 + 12 * layer);
        if (j < 14) {
          u32x4 st[4]; u32 sb[4]; float c1, sigma;
          auto fix = [&](auto T) { acc[T.v] = mfma(asb(u32x4{sb[T.v], 0u, 0u, 0u}), asb(u32x4{nmean, 0u, 0u, 0u}), acc[T.v]); asm volatile("" : "+v"(acc[T.v])); };
          sfor<0, N0>([&](auto I) { LDF(I.v, I.v); });
          STAMP(28);
          acc[0] = z16; acc[1] = z16; acc[2] = z16; acc[3] = z16;
          gemm<2, 0, 32, 4, 32>(acc, xf, [&](auto KB, auto T) { return trfrag(wa[T.v] + KB.v * 2048, 1024); }, [&](auto I) {
            constexpr int s = I.v;
            if constexpr (s == 2 * (36 - N0)) RAWBAR();
            STREAM32(2, ws + WS_ST1 + (size_t)layer * (256u << 10))
            if constexpr (s == 59) DRAIN();
          }, [&](auto I) {
            if constexpr (I.v == 54) { _Pragma("unroll") for (int i = 0; i < 4; ++i) sb[i] = *(LAS u32*)(lds + LD_S + 3072 + i * 256 + lane * 4); }
            if constexpr (I.v == 59) { STATS() asm volatile("" : "+v"(rstd), "+v"(nmean)); }
            if constexpr (I.v == 61) fix(IC<0>{});
            if constexpr (I.v == 62) fix(IC<1>{});
            if constexpr (I.v == 63) { c1 = rstd * -1.4426950408889634f; sigma = __builtin_amdgcn_rcpf(rstd); asm volatile("" : "+v"(c1), "+v"(sigma)); }
          });
          STAMP(25);
          fix(IC<2>{});
          float tt[8]; u32x4 hp;
          auto uop = [&](auto M) {
            constexpr int st_ = M.v % 6, sl = M.v / 6, e = sl < 37 ? sl - st_ : sl - 5 - st_, t = e / 16, r = e % 16;
            if constexpr (sl < 37 ? (e >= 0 && e < 32) : (e >= 32 && e < 64)) {
              if constexpr (st_ == 0) tt[e & 7] = acc[t][r] * c1;
              if constexpr (st_ == 1) tt[e & 7] = __builtin_amdgcn_exp2f(tt[e & 7]);
              if constexpr (st_ == 2) tt[e & 7] = __builtin_fmaf(tt[e & 7], sigma, sigma);
              if constexpr (st_ == 3) tt[e & 7] = __builtin_amdgcn_rcpf(tt[e & 7]);
              if constexpr (st_ == 4) tt[e & 7] = acc[t][r] * tt[e & 7];
              if constexpr (st_ < 5) asm volatile("" : "+v"(tt[e & 7]));
              if constexpr (st_ == 5 && (e & 1)) {
                u32 pkd = pk2(tt[(e - 1) & 7], tt[e & 7]); pin1(pkd); hp[(e >> 1) & 3] = pkd;
                if constexpr ((e & 7) == 7) gst16_sc1<((e >> 3) & 3) * 1024>(hb + (e >> 5) * 4096, l16, hp);
              }
            }
          };
          constexpr int UB = 37 * 6, UC = 53 * 6;
          gemm<1, 0, 32, 4, 32>(acc + 2, xf, [&](auto KB, auto) { return trfrag(wa[2] + KB.v * 2048, 1024); }, [&](auto I) { if constexpr (I.v == B5S) RAWBAR();   },
                                [&](auto I) { constexpr int i = I.v, u0 = i < 2 ? 12 * i : i < 29 ? 12 + 6 * i : 186 + 12 * (i - 29), u1 = i + 1 < 2 ? 12 : i + 1 < 29 ? 18 + 6 * i : 186 + 12 * (i - 28); sfor<u0, u1>(uop); });
          STAMP(27);
          fix(IC<3>{});
          gemm<1, 0, 32, 4, 32>(acc + 3, xf, [&](auto KB, auto) { return trfrag(wa[3] + KB.v * 2048, 1024); }, [&](auto I) {
            if constexpr (I.v == B5C) { STAMP(34); DRAIN(); RAWBAR(); STAMP(21); }
          }, [&](auto I) { if constexpr (I.v >= 1) sfor<UB + (I.v - 1) * (UC - UB) / 31, UB + I.v * (UC - UB) / 31>(uop); });
          STAMP(8 + 12 * layer);
          sfor<UC, 75 * 6>(uop);
        } else { RAWBAR(); RAWBAR(); RAWBAR(); }
      }
      FRESH();
      {
        const size_t ch = (size_t)(4 * P + 2 * th + cc);
        const int t0 = kh * 8192 + (TRB0 ^ (64 * kh)), t1 = t0 ^ 64;
        LAS char* wa[6] = {opaque(lds + t0), opaque(lds + t1), opaque(lds + 65536 + t0), opaque(lds + 65536 + t1), opaque(lds + 131072 + t0), opaque(lds + 131072 + t1)};
        char* xo = layer == 0 ? ws + WS_X2 + (ch * 32 + 4 * cg + 2 * kh) * 1024 : out + (size_t)(128 * P + 64 * th + 32 * cc) * 1024 + 128 * cg + 64 * kh;
        char* so = ws + WS_ST2 + (ch * 16 + 2 * cg + kh) * 256;
        asm volatile("" : "+s"(xo), "+s"(so));
        RES2ACC()
        STAMP(35);
        RAWBAR();
        STAMP(22);
        sfor<0, NB>([&](auto I) { LDH(I.v); });
        STAMP(56);
        if (layer == 0) STAMP(10);
        gemm<2, 0, 56, 4, 32>(acc, xf, [&](auto KB, auto T) { constexpr int so = b2slot(KB.v >> 2); return trfrag(wa[(so >> 16) * 2 + T.v] + (so & 65535) + (KB.v & 3) * 2048, 1024); }, [&](auto I) {
          constexpr int s = I.v, kb = s / 2;
          if constexpr (s == X2S) STAMP(57);
          if constexpr (s == 68) STAMP(59);
          if constexpr (s == 92) STAMP(61);
          if constexpr (s == 1 || s == X2S || s == 68 || s == 92) RAWBAR();
          if constexpr (s == 1) STAMP(38);
          if constexpr (s == X2S) STAMP(58);
          if constexpr (s == 68) STAMP(60);
          if constexpr (s == X2S) sfor<28, NB + kb>([&](auto J) { LDH(J.v); });
          constexpr int lim = s < X2S ? 28 : 56;
          if constexpr (s % 2 == 0) waitv<(NB + kb < lim ? NB + kb : lim) - kb - 1>(xf[kb % 32]);
          if constexpr (s % 2 == 1 && NB + kb < lim) LDH(NB + kb);
        }, nop);
        STAMP(11 + 12 * layer);
        TILE_REDUCE(98304)
        if (layer == 0) {
          TILE_PUBLISH(xo, so)
        } else {
          sfor<0, 2>([&](auto S) {
            auto r0 = __builtin_amdgcn_permlane32_swap(resid[S.v].x, resid[S.v].z, false, false);
            auto r1 = __builtin_amdgcn_permlane32_swap(resid[S.v].y, resid[S.v].w, false, false);
            gst16<32 * S.v>(xo, tk * 1024u + 16u * hh, u32x4{r0[0], r1[0], r0[1], r1[1]});
          });
        }
      }
    }
  }
}

extern "C" void kernel_launch(void* const* d_in, const int* in_sizes, int n_in, void* d_out, int out_size, void* d_ws, size_t ws_size, hipStream_t stream) {
  mega<<<256, 512, 0, stream>>>((const char*)d_in[0], (const char*)d_in[1], (const char*)d_in[2], (const char*)d_in[3], (const char*)d_in[5], (const char*)d_in[6], (char*)d_out, (char*)d_ws);
}
```

```cpp
#include <hip/hip_runtime.h>
#include <hip/hip_bf16.h>

typedef unsigned u32;
typedef unsigned short u16;
typedef u32 u32x4 __attribute__((ext_vector_type(4)));
typedef u32 u32x2 __attribute__((ext_vector_type(2)));
typedef float f32x16 __attribute__((ext_vector_type(16)));
typedef float f32x4 __attribute__((ext_vector_type(4)));
typedef float f32x2 __attribute__((ext_vector_type(2)));
typedef short s16x4 __attribute__((ext_vector_type(4)));
typedef __bf16 bf16x2 __attribute__((ext_vector_type(2)));
typedef __bf16 bf16x8 __attribute__((ext_vector_type(8)));
#define LAS __attribute__((address_space(3)))
#define DI __device__ __forceinline__

constexpr size_t WS_CTL = 0;
constexpr size_t WS_ST1 = 512u << 10;
constexpr size_t WS_ST2 = 1024u << 10;
constexpr size_t WS_KVP = 1280u << 10;
constexpr u32 KVP_UNIT = 9216;
constexpr size_t WS_O = 4u << 20;
constexpr size_t WS_X1 = 8u << 20;
constexpr size_t WS_X2 = 12u << 20;
constexpr size_t WS_H = 14u << 20;
constexpr size_t WS_STAMP = 40u << 20;
constexpr u32 C_HEL = 1024;

constexpr u32 LD_WA3 = 64 * 1024;
constexpr u32 LD_SC = 128 * 1024;
constexpr u32 LD_S = 153 * 1024;
constexpr u32 LD_STAMP = 157 * 1024;
constexpr u32 LD_ID = 159 * 1024 + 512;
#define TOUCH 1
#define HSLEEP 14
#define A3SLEEP 12
#define PFX 1
#define PFW 0
#define NEARLY 0
#define PFC 48
#define BSSTEP 56
#define HPRIO 3
#define PSLEEP 0
#define REP 16
#define KSTEP 12
#define NW4 2
#define NW6 2
#define A3PRE 4
#define SIG0 14
#define SIG1 0
#define B5B 8
#define NB4 0
#define B5S 2
#define HP0 20
#define SPRIO 0
#define X2S 44
#define CHK 30
#define SIG2 10
#define X2SLEEP 8
#define B5C 12
#define NB 12
#define TPOS 0
#define B1E 16
#define BXR 2
#define WPOS 1
#define TMASK 399
#define PW2SLEEP 17
#define PW5SLEEP 8
#define P8EARLY 0
#define HOSLEEP 13
#define CT2 1
#define N0V 12
constexpr u32 SPIN_CAP = 1u << 17;

#define BAR() do { asm volatile("s_waitcnt lgkmcnt(0)" ::: "memory"); __builtin_amdgcn_s_barrier(); asm volatile("" ::: "memory"); } while (0)
#define RAWBAR() do { asm volatile("" ::: "memory"); __builtin_amdgcn_s_barrier(); asm volatile("" ::: "memory"); } while (0)
#define FRESH() do { asm volatile("" : "+s"(ws), "+s"(P), "+s"(j), "+s"(w), "+s"(x), "+s"(wqkv), "+s"(wo), "+s"(w1), "+s"(w2)); asm volatile("" : "+v"(lane)); th = j >> 3; cg = j & 7; cc = w & 1; kh = (w >> 1) & 1; \
  hh = lane >> 5; tk = lane & 31; g = lane >> 4; t16 = lane & 15; q = (lane >> 2) & 3; p = lane & 3; l16 = lane * 16u; } while (0)
#define TRB0 ((4 * hh + q) * 128 + 64 * (q >> 1) + 32 * (g & 1) + 8 * p)
#define TRB1 (TRB0 ^ 64)
#define TRA ((4 * hh + q) * 64 + 32 * (g & 1) + 8 * p)
#define HEL_OWN ((u32)(P * 16 + j) * 2048u + C_HEL + 8u * tk)
#define HEL_PEER (C_HEL + (tk < 16 ? (u32)(P * 16 + tk) * 2048u + 8u * j : (u32)((tk - 16) * 16 + j) * 2048u + 128u + 8u * P))
#define PSIG(n) do { if (lane < REP) gadd1(ws, (u32)(P * 16 + lane * (16 / REP)) * 2048u + (n) * 128u); } while (0)
#define PWAIT(n) cnt_wait(ws, (u32)(P * 16 + (j & -(16 / REP))) * 2048u + (n) * 128u, 16u * (layer + 1))
constexpr int b2row(int u) { return 128 * (u % 7) + 32 * (u / 7); }
#define B2SRC(t, c) (w2 + cg * 128 + (size_t)(896 * ((c) >> 1) + (((c) & 1) ? b2row(2 * (t) + 1) : b2row(2 * (t)))) * 1024)
#define VO1024 ((lane >> 3) * 1024u + 16u * ((lane & 7) ^ ((g & 1) << 2)))
#define KOFF(PP) ((u32)((PP) * 16 + j) * 2048u + 1536u + w * 128u)
#define DRAIN() asm volatile("s_waitcnt vmcnt(0)" ::: "memory")

#define STAMP(i) do {} while (0)

template <int N> DI void waitv(u32x4& a) { asm volatile("s_waitcnt vmcnt(%1)" : "+v"(a) : "i"(N)); }
template <int N> DI void waitv4(u32x4& a, u32x4& b, u32x4& c, u32x4& d) { asm volatile("s_waitcnt vmcnt(%4)" : "+v"(a), "+v"(b), "+v"(c), "+v"(d) : "i"(N)); }
DI void waitl8(u32x4* f) { asm volatile("" : "+v"(f[0]), "+v"(f[1]), "+v"(f[2]), "+v"(f[3]), "+v"(f[4]), "+v"(f[5]), "+v"(f[6]), "+v"(f[7])::"memory"); }
DI void pin(u32x4& a) { asm volatile("" : "+v"(a)); }
DI void pin2(u32x2& a) { asm volatile("" : "+v"(a)); }
DI void pin1(u32& a) { asm volatile("" : "+v"(a)); }

template <int OFF> DI void gld16_sc1(u32x4& d, const char* sb, u32 vo) { asm volatile("global_load_dwordx4 %0, %1, %2 offset:%3 sc1" : "=v"(d) : "v"(vo), "s"(sb), "i"(OFF) : "memory"); }
template <int OFF> DI void gld16(u32x4& d, const char* sb, u32 vo) { asm volatile("global_load_dwordx4 %0, %1, %2 offset:%3" : "=v"(d) : "v"(vo), "s"(sb), "i"(OFF) : "memory"); }
template <int OFF> DI void gld8(u32x2& d, const char* sb, u32 vo) { asm volatile("global_load_dwordx2 %0, %1, %2 offset:%3" : "=v"(d) : "v"(vo), "s"(sb), "i"(OFF) : "memory"); }
DI void gld8_sc1(u32x2& d, const char* sb, u32 vo) { asm volatile("global_load_dwordx2 %0, %1, %2 sc1" : "=v"(d) : "v"(vo), "s"(sb) : "memory"); }
template <int OFF> DI void gld4o_sc1(u32& d, const char* sb, u32 vo) { asm volatile("global_load_dword %0, %1, %2 offset:%3 sc1" : "=v"(d) : "v"(vo), "s"(sb), "i"(OFF) : "memory"); }
template <int OFF> DI void gld2(u32& d, const u16* a) { asm volatile("global_load_ushort %0, %1, off offset:%2" : "=v"(d) : "v"(a), "i"(OFF) : "memory"); }
DI void gld4_sc1(u32& d, const char* sb, u32 vo) { asm volatile("global_load_dword %0, %1, %2 sc1" : "=v"(d) : "v"(vo), "s"(sb) : "memory"); }
template <int OFF> DI void gst16_sc1(char* sb, u32 vo, u32x4 d) { asm volatile("global_store_dwordx4 %0, %1, %2 offset:%3 sc1\n\ts_nop 1" ::"v"(vo), "v"(d), "s"(sb), "i"(OFF) : "memory"); }
DI void gst8_sc1(char* sb, u32 vo, u32x2 d) { asm volatile("global_store_dwordx2 %0, %1, %2 sc1" ::"v"(vo), "v"(d), "s"(sb) : "memory"); }
template <int OFF> DI void gst8(char* sb, u32 vo, u32x2 d) { asm volatile("global_store_dwordx2 %0, %1, %2 offset:%3" ::"v"(vo), "v"(d), "s"(sb), "i"(OFF) : "memory"); }
DI void gadd1(char* sb, u32 vo) { asm volatile("global_atomic_add %0, %1, %2 sc1" ::"v"(vo), "v"(1u), "s"(sb) : "memory"); }
#define OUTMOD ""
template <int OFF> DI void gst16(char* sb, u32 vo, u32x4 d) { asm volatile("global_store_dwordx4 %0, %1, %2 offset:%3 " OUTMOD "\n\ts_nop 1" ::"v"(vo), "v"(d), "s"(sb), "i"(OFF) : "memory"); }
DI void gst4_sc1(char* sb, u32 vo, u32 d) { asm volatile("global_store_dword %0, %1, %2 sc1" ::"v"(vo), "v"(d), "s"(sb) : "memory"); }
DI void glds(u32 ldsaddr, const char* sb, u32 vo) { asm volatile("s_mov_b32 m0, %0\n\ts_nop 0\n\tglobal_load_lds_dwordx4 %1, %2" ::"s"(ldsaddr), "v"(vo), "s"(sb) : "memory"); }
DI void touch(u32 ldsaddr, const char* a) { asm volatile("s_mov_b32 m0, %0\n\ts_nop 0\n\tglobal_load_lds_dword %1, off" ::"s"(ldsaddr), "v"(a) : "memory"); }
DI void touch3(u32 ldsaddr, const char* a) { touch(ldsaddr, a + (((3u - ((u32)(size_t)a >> 7)) & 7u) << 7)); }

DI bf16x8 trfrag(LAS char* p, int second) {
  s16x4 a = __builtin_amdgcn_ds_read_tr16_b64_v4i16((LAS s16x4*)p);
  s16x4 b = __builtin_amdgcn_ds_read_tr16_b64_v4i16((LAS s16x4*)(p + second));
  return __builtin_bit_cast(bf16x8, __builtin_shufflevector(a, b, 0, 1, 2, 3, 4, 5, 6, 7));
}
DI bf16x8 asb(u32x4 v) { return __builtin_bit_cast(bf16x8, v); }
DI f32x4 mfma16(bf16x8 a, bf16x8 b, f32x4 c) { return __builtin_amdgcn_mfma_f32_16x16x32_bf16(a, b, c, 0, 0, 0); }
DI f32x16 mfma(bf16x8 a, bf16x8 b, f32x16 c) { return __builtin_amdgcn_mfma_f32_32x32x16_bf16(a, b, c, 0, 0, 0); }
DI u32 pk2(float a, float b) { f32x2 v = {a, b}; return __builtin_bit_cast(u32, __builtin_convertvector(v, bf16x2)); }
DI u32 pk2v(f32x2 v) { return __builtin_bit_cast(u32, __builtin_convertvector(v, bf16x2)); }
DI u32x2 pk4(f32x4 v) { u32x2 r; r.x = pk2(v[0], v[1]); r.y = pk2(v[2], v[3]); return r; }
DI float bflo(u32 u) { return __uint_as_float(u << 16); }
DI float bfhi(u32 u) { return __uint_as_float(u & 0xffff0000u); }
DI float dot2(u32 a, u32 b, float c) { return __builtin_amdgcn_fdot2_f32_bf16(__builtin_bit_cast(bf16x2, a), __builtin_bit_cast(bf16x2, b), c, false); }
DI void swap32(float& a, float& b) { auto r = __builtin_amdgcn_permlane32_swap(__float_as_uint(a), __float_as_uint(b), false, false); a = __uint_as_float(r[0]); b = __uint_as_float(r[1]); }
DI void swap16(float& a, float& b) { auto r = __builtin_amdgcn_permlane16_swap(__float_as_uint(a), __float_as_uint(b), false, false); a = __uint_as_float(r[0]); b = __uint_as_float(r[1]); }
DI float mx2(float a, float b) { return __builtin_amdgcn_fmed3f(a, b, __builtin_inff()); }
DI float mx3(float a, float b, float c) { return __builtin_fmaxf(__builtin_fmaxf(a, b), c); }
DI void red2max(float& a, float& b) { swap32(a, b); float u = mx2(a, b); a = u; b = u; swap32(a, b); }
DI void red2sum(float& a, float& b) { swap32(a, b); float u = a + b; a = u; b = u; swap32(a, b); }
template <int CTRL> DI float dppadd(float v) { return v + __uint_as_float(__builtin_amdgcn_update_dpp(0u, __float_as_uint(v), CTRL, 0xf, 0xf, true)); }
DI float rowsum16(float v) { v = dppadd<0xB1>(v); v = dppadd<0x4E>(v); v = dppadd<0x141>(v); v = dppadd<0x140>(v); return v; }
DI f32x2 pr(const f32x16& a, int i) { return f32x2{a[2 * i], a[2 * i + 1]}; }

template <class MID> DI void softmax2(const f32x16& p, const float L2E  , u32x4 (&f)[2][2], MID&& mid) {
  float mx[2], mn[2];
#pragma unroll
  for (int hd = 0; hd < 2; ++hd) {
    const int b = 8 * hd;
    mx[hd] = mx2(mx3(mx3(mx3(p[b], p[b + 1], p[b + 2]), p[b + 3], p[b + 4]), p[b + 5], p[b + 6]), p[b + 7]);
    mn[hd] = mx2(mx3(mx3(mx3(-p[b], -p[b + 1], -p[b + 2]), -p[b + 3], -p[b + 4]), -p[b + 5], -p[b + 6]), -p[b + 7]);
    red2max(mx[hd], mn[hd]);
  }
  mid(mx, mn);
  f32x2 e[2][2][4]; float s[2][2];
#pragma unroll
  for (int hd = 0; hd < 2; ++hd) {
    const float cp = -mx[hd] * L2E, cn = -mn[hd] * L2E;
#pragma unroll
    for (int i = 0; i < 4; ++i) {
      const f32x2 v = pr(p, 4 * hd + i), a = v * L2E + cp, b = v * -L2E + cn;
      e[hd][0][i] = f32x2{__builtin_amdgcn_exp2f(a.x), __builtin_amdgcn_exp2f(a.y)};
      e[hd][1][i] = f32x2{__builtin_amdgcn_exp2f(b.x), __builtin_amdgcn_exp2f(b.y)};
    }
#pragma unroll
    for (int sg = 0; sg < 2; ++sg) { const f32x2 t = (e[hd][sg][0] + e[hd][sg][1]) + (e[hd][sg][2] + e[hd][sg][3]); s[hd][sg] = t.x + t.y; }
    red2sum(s[hd][0], s[hd][1]);
#pragma unroll
    for (int sg = 0; sg < 2; ++sg) {
      const float r = __builtin_amdgcn_rcpf(s[hd][sg]);
#pragma unroll
      for (int i = 0; i < 4; ++i) f[hd][sg][i] = pk2v(e[hd][sg][i] * r);
    }
  }
}
DI void pack16(const f32x16& a, u32x4 (&y)[2]) {
#pragma unroll
  for (int i = 0; i < 8; ++i) y[i >> 2][i & 3] = pk2v(pr(a, i));
}
DI void lnfix(f32x16& a, LAS char* Sp, u32 negmean) { a = mfma(asb(u32x4{*(LAS u32*)Sp, 0u, 0u, 0u}), asb(u32x4{negmean, 0u, 0u, 0u}), a); }
DI void lnfix4(f32x16& a, LAS char* Sp, u32 negmean) { a = mfma(asb(*(LAS u32x4*)Sp), asb(u32x4{negmean, negmean, negmean, negmean}), a); }
DI u32 split2(float v) { const u32 hi = pk2(v, v) & 0xffffu; return hi | (pk2(v - bflo(hi), 0.f) << 16); }

template <int V> struct IC { static constexpr int v = V; };
template <int B, int E, class F> DI void sfor(F&& f) { if constexpr (B < E) { f(IC<B>{}); sfor<B + 1, E>(f); } }
#define SB() __builtin_amdgcn_sched_barrier(0)
template <int NT, int K0, int K1, int D, int XM, class LD, class PRE, class POST> DI void gemm(f32x16* acc, u32x4* xf, LD&& ld, PRE&& pre, POST&& post) {
  bf16x8 wf[D + 1];
  constexpr int S0 = K0 * NT, S1 = K1 * NT;
  sfor<S0, S0 + D>([&](auto I) { constexpr int s = I.v; wf[s % (D + 1)] = ld(IC<s / NT>{}, IC<s % NT>{}); });
  SB();
  sfor<S0, S1>([&](auto I) {
    constexpr int s = I.v;
    pre(I);
    if constexpr (s + D < S1) wf[(s + D) % (D + 1)] = ld(IC<(s + D) / NT>{}, IC<(s + D) % NT>{});
    SB();
    post(I);
    acc[s % NT] = mfma(wf[s % (D + 1)], asb(xf[(s / NT) % XM]), acc[s % NT]);
    asm volatile("" : "+v"(acc[s % NT]));
    SB();
  });
}
constexpr int b2slot(int t) { return t < 4 ? t * 16384 : t == 4 || t == 13 ? 131072 : t < 9 ? 65536 + (t - 5) * 16384 : (t - 9) * 16384; }
DI LAS char* opaque(LAS char* p) { asm volatile("" : "+v"(p)); return p; }

DI void cnt_wait(const char* ctl, u32 off, u32 val) {
  for (u32 sp = 0; sp < SPIN_CAP; ++sp) {
    u32 v; gld4_sc1(v, ctl, off); asm volatile("s_waitcnt vmcnt(0)" : "+v"(v)::"memory");
    if (__builtin_amdgcn_readfirstlane(v) >= val) break;
    if (PSLEEP) __builtin_amdgcn_s_sleep(PSLEEP);
  }
}
#define HANDOFF(n) do { DRAIN(); STAMP(n == 0 ? 4 + 12 * layer : n == 1 ? 6 + 12 * layer : 12); RAWBAR(); if (w == 0) { PSIG(n); if (HOSLEEP) __builtin_amdgcn_s_sleep(HOSLEEP); PWAIT(n); } RAWBAR(); } while (0)

__global__ __launch_bounds__(512, 2) void mega(const char* x, const char* wqkv, const char* wphi, const char* wo, const char* w1, const char* w2, char* out, char* ws) {
  __shared__ __attribute__((aligned(16))) char smem[163840];
  LAS char* lds = (LAS char*)smem;
  const u32 lb = (u32)(size_t)lds;
  int lane = threadIdx.x & 63; int w = __builtin_amdgcn_readfirstlane(threadIdx.x >> 6);
  int hh = lane >> 5, tk = lane & 31, g = lane >> 4, t16 = lane & 15, q = (lane >> 2) & 3, p = lane & 3;
  const int bid = blockIdx.x, loc = bid >> 3;
  int P = (bid & 7) * 2 + (loc >> 4), j = loc & 15;
  int th = j >> 3, cg = j & 7, cc = w & 1, kh = (w >> 1) & 1;
  u32 l16 = lane * 16u;
  const u32x4 ones4 = {0x3f803f80u, 0x3f803f80u, 0x3f803f80u, 0x3f803f80u};
  const f32x16 z16 = {0.f, 0.f, 0.f, 0.f, 0.f, 0.f, 0.f, 0.f, 0.f, 0.f, 0.f, 0.f, 0.f, 0.f, 0.f, 0.f};
  const f32x4 z4 = {0.f, 0.f, 0.f, 0.f};
  STAMP(0);
  size_t pc_; asm volatile("s_getpc_b64 %0" : "=s"(pc_));
  const char* pc = (const char*)(pc_ & ~(size_t)255);
  {
    const int id = (w ^ 4) * 32 + loc;
    if (id < PFC) glds(lb + LD_SC + 8192 + w * 1024, pc + id * 1024, l16);
  }
  glds(lb + LD_SC + 8192 + w * 1024, x + (size_t)(128 * P + 8 * j + w) * 1024, l16);
  if (w >= 4) {
    const u32 swz = 16u * ((lane & 7) ^ ((g & 1) << 2));
    const u32 vo1024 = (lane >> 3) * 1024u + swz, vo3584 = (lane >> 3) * 3584u + swz, voA1 = (lane >> 2) * 3072u + 16u * (lane & 3);
    u32x4 wa1[16];
    u32x4 wb2[36];
    LAS char* wl = lds + l16;
#define HW (w & 3)
#define GLDS_Q(n) glds(lb + 65536 + (n) * 1024, wqkv + 64 * j + (size_t)((n) * 16) * 3072, voA1)
#define GLDS_B1(n) glds(lb + (n) * 1024, w1 + (size_t)(((n) & 63) * 8) * 3584 + j * 256 + ((n) >> 6) * 128, vo3584)
#define GLDS_A3(n) glds(lb + LD_WA3 + (n) * 1024, wo + (size_t)(n) * 8192 + cg * 128, vo1024)
    auto putA1 = [&](auto R, auto M) { *(LAS u32x4*)(wl + R.v * 32768 + (4 * M.v + HW) * 1024) = wa1[R.v * 8 + M.v]; };
    auto putB2 = [&](auto T) { sfor<0, 4>([&](auto I) { *(LAS u32x4*)(wl + b2slot(T.v) + (4 * HW + I.v) * 1024) = wb2[(T.v - 5) * 4 + I.v]; }); };
    auto ldB2 = [&](auto T) { sfor<0, 4>([&](auto I) { gld16<0>(wb2[(T.v - 5) * 4 + I.v], B2SRC(T.v, HW) + I.v * 8192, vo1024); }); };
    auto ldB1b = [&](u32x4* r, auto I0, auto I1) { const char* b = w1 + (size_t)(HW * 128) * 3584 + (j < 14 ? j : 13) * 256 + 128; sfor<I0.v, I1.v>([&](auto I) { gld16<0>(r[I.v], b + (size_t)(I.v * 8) * 3584, vo3584); }); };
    auto putB1b = [&](u32x4* r) { sfor<0, 16>([&](auto I) { pin(r[I.v]); *(LAS u32x4*)(wl + 65536 + (HW * 16 + I.v) * 1024) = r[I.v]; }); };
    auto colsum8 = [&](LAS char* sp, int kstride, int second, f32x4& a, f32x4& b) {
      const u32 o = ((t16 >> 2) & 1) == (g & 1) ? 0x3f803f80u : 0u; const u32x4 sel = {o, o, o, o};
      bf16x8 wf[8];
      sfor<0, 8>([&](auto K) { wf[K.v] = trfrag(sp + K.v * kstride, second); });
      sfor<0, 4>([&](auto K) { a = mfma16(asb(sel), wf[2 * K.v], a); b = mfma16(asb(sel), wf[2 * K.v + 1], b); });
      asm volatile("" : "+v"(a), "+v"(b)); SB();
    };
    auto putS = [&](u32 off, float a) { const u32 sp = split2(a); *(LAS u32*)(lds + LD_S + off) = __builtin_amdgcn_perm(sp, sp, hh ? 0x03020302u : 0x01000100u); };
    const int lc = (P & 1) * 16 + j;
#define T3_KV() do { if (lane < 32) touch3(lb + 159 * 1024, wqkv + (size_t)(16 * lc + t16) * 3072 + 1024 + g * 1024); } while (0)
#define T3_A() do { if (lane < 32) touch3(lb + 159 * 1024, g == 0 ? wqkv + (size_t)(16 * lc + t16) * 3072 : wo + (size_t)(16 * lc + t16) * 1024); } while (0)
#define T3_B2() do { const int i_ = 20 * lc + lane; if (lane < 20) touch3(lb + 159 * 1024, w2 + (size_t)(896 * ((i_ >> 5) & 1) + 128 * (i_ >> 6) - (i_ < 448 ? 0 : 864) + (i_ & 31)) * 1024); } while (0)
#define T3_W(wm) touch3(lb + 159 * 1024, (wm) + (size_t)(56 * lc + (lane < 56 ? lane : 55)) * 1024)
    {
      if (HW == 0) T3_KV();
      if (HW == 1) *(LAS u32x2*)(lds + LD_ID + lane * 8) = g == p ? u32x2{q == 0 ? 0x3f80u : q == 1 ? 0x3f800000u : 0u, q == 2 ? 0x3f80u : q == 3 ? 0x3f800000u : 0u} : u32x2{0u, 0u};
      u32x2 h0 = {0u, 0u};
      if (HW == 3) {
        gld8_sc1(h0, ws, HEL_OWN); gst16_sc1<0>(ws + (size_t)(P * 16 + j) * 2048, l16, u32x4{0u, 0u, 0u, 0u}); gst16_sc1<1536>(ws + (size_t)(P * 16 + j) * 2048, tk * 16u, u32x4{0u, 0u, 0u, 0u});
      }
      const char* wb = wqkv + 64 * j + (size_t)(16 * HW) * 3072;
      __builtin_amdgcn_s_sleep(HSLEEP);
      sfor<0, 8>([&](auto M) { gld16<1024>(wa1[M.v], wb + (size_t)(64 * M.v) * 3072, voA1); gld16<2048>(wa1[8 + M.v], wb + (size_t)(64 * M.v) * 3072, voA1); });
      auto touches = [&] {
        const u32 dump = lb + 159 * 1024;
        const int id = P * 16 + j;
        if ((TMASK & 1) && HW == 3) { touch(dump, lane < 56 ? w2 + (size_t)id * 7168 + lane * 128 : wo + (size_t)id * 2048 + (lane - 56) * 256); }
        if ((TMASK & 8) && HW == 0) { touch(dump, lane < 56 ? w1 + (size_t)id * 7168 + lane * 128 : wo + (size_t)id * 2048 + (lane - 56) * 256 + 128); }
        const u32 ly = HW & 1, pg = lane & 7;
        const u32 to = HW < 2 ? (u32)WS_H + ly * (7u << 20) + P * 458752u + (pg < 7 ? pg : 6) * 65536u
                              : (pg < 4 ? (u32)WS_O + (pg >> 1) * (4u << 20) + ly * (2u << 20) : pg < 6 ? (u32)WS_X2 : (u32)WS_ST1 + ly * (1u << 20)) + (pg < 6 ? P * 131072u + (pg & 1) * 65536u : (pg & 1) * (2u << 20));
        if ((TMASK & 2) && lane < 8) touch(dump, ws + to);
        if ((TMASK & 4) && HW == 1) T3_A();
        if ((TMASK & 16) && HW == 1) T3_W(w1);
        if ((TMASK & 32) && HW == 2) T3_W(w2);
      };
      if (TPOS == 0) touches();
      STAMP(44);
      RAWBAR();
      f32x4 sk = z4, sv = z4;
      u32x4 sel;
#pragma unroll
      for (int e = 0; e < 4; ++e) sel[e] = ((t16 & 7) >> 1) == e ? ((t16 & 1) ? 0x3f800000u : 0x00003f80u) : 0u;
      sfor<0, 4>([&](auto C) {
        constexpr int c = C.v;
        waitv4<(TPOS == 0 && (TMASK & 2) ? 13 : 12) - 4 * c>(wa1[2 * c], wa1[2 * c + 1], wa1[8 + 2 * c], wa1[8 + 2 * c + 1]);
        if (c == 0 && HW == 3) { pin2(h0); if (lane < 32) gst8_sc1(ws, HEL_PEER, u32x2{h0.y, h0.x + 2u}); }
        putA1(IC<0>{}, IC<2 * c>{}); putA1(IC<0>{}, IC<2 * c + 1>{}); putA1(IC<1>{}, IC<2 * c>{}); putA1(IC<1>{}, IC<2 * c + 1>{});
        sk = mfma16(asb(sel), asb(wa1[2 * c]), sk); sv = mfma16(asb(sel), asb(wa1[8 + 2 * c]), sv);
        sk = mfma16(asb(sel), asb(wa1[2 * c + 1]), sk); sv = mfma16(asb(sel), asb(wa1[8 + 2 * c + 1]), sv);
        asm volatile("" : "+v"(sk), "+v"(sv));
        if (c == 3) {
          LAS char* tb = lds + LD_SC + 16384 + HW * 2048;
          *(LAS f32x4*)(tb + l16) = sk; *(LAS f32x4*)(tb + 1024 + l16) = sv;
          LAS float* tr = (LAS float*)(tb + (q & 1) * 256 + (tk >> 3) * 16 + p * 4);
          float a = tr[0], b = tr[16], c_ = tr[32], d = tr[48], e = tr[256], f = tr[272], g_ = tr[288], h = tr[304];
          a += b; asm volatile("" : "+v"(a)); c_ += d; asm volatile("" : "+v"(c_)); e += f; asm volatile("" : "+v"(e)); g_ += h; asm volatile("" : "+v"(g_));
          a += c_; asm volatile("" : "+v"(a)); e += g_;
          putS(l16 + 4 * HW, a); putS(1024 + l16 + 4 * HW, e);
        }
        STAMP(29 + (c == 3 ? 4 : c));
        if (c & 1) BAR();
        if (c & 1) __builtin_amdgcn_s_setprio(c == 1 ? 3 : 0);
      });
      u32x2 hv;
      if (HW == 3) gld8_sc1(hv, ws, HEL_OWN);
      if (TPOS == 1) touches();
      _Pragma("unroll 4") for (int i = 0; i < 8; ++i) GLDS_Q(HW * 8 + i);
      ldB1b(wb2 + 20, IC<0>{}, IC<B1E>{});
      asm volatile("s_waitcnt vmcnt(%0)" ::"i"(8 + B1E) : "memory");
      if (HW == 3) {
        pin2(hv);
        u32 sent = h0.x;
        for (u32 sp_ = 0; sp_ < SPIN_CAP; ++sp_) {
          if (hv.x != sent) { sent = hv.x; if (lane < 32) gst8_sc1(ws, HEL_PEER, u32x2{h0.y, hv.x + 1u}); }
          if (__all(hv.y != h0.y)) break;
          gld8_sc1(hv, ws, HEL_OWN); asm volatile("s_waitcnt vmcnt(0)" : "+v"(hv)::"memory");
        }
      }
      STAMP(32);
      sfor<5, 5 + NEARLY>(ldB2);
      STAMP(39);
      asm volatile("s_waitcnt vmcnt(%0)" ::"i"(B1E + 4 * NEARLY) : "memory");
      RAWBAR();
      { f32x4 a = z4, b = z4; colsum8(lds + 65536 + HW * 8192 + TRA, 1024, 512, a, b); putS(2048 + l16 + 4 * HW, a[0] + b[0]); }
      BAR();
      if (TPOS == 2) touches();
    }
    sfor<0, 2>([&](auto L) {
      constexpr int layer = L.v;
      FRESH();
      if constexpr (layer == 1) {
        RAWBAR();
        ldB1b(wa1, IC<0>{}, IC<16>{});
        _Pragma("unroll 4") for (int i = 0; i < 8; ++i) GLDS_A3(32 + HW * 8 + i);
        STAMP(42);
        RAWBAR();
        RAWBAR();
      }
      if (j < 14) { _Pragma("unroll 4") for (int i = 0; i < 8; ++i) GLDS_B1(HW * 16 + i); }
      if constexpr (layer == 0) { _Pragma("unroll 4") for (int i = 0; i < 8; ++i) GLDS_A3(32 + HW * 8 + i); }
      STAMP(36);
      RAWBAR();
      _Pragma("unroll 4") for (int i = 0; i < A3PRE; ++i) GLDS_A3(HW * 8 + i);
      STAMP(48);
      RAWBAR();
      _Pragma("unroll 4") for (int i = A3PRE; i < 8; ++i) GLDS_A3(HW * 8 + i);
      RAWBAR();
      if (CT2 && layer == 0 && lane < 3) touch(lb + 159 * 1024, pc + (lane * 128 + HW * 32 + lc) * 128);
      if constexpr (layer == 0) { ldB1b(wb2 + 20, IC<B1E>{}, IC<16>{}); sfor<5 + NEARLY, 5 + NEARLY + NW4>(ldB2); asm volatile("s_waitcnt vmcnt(%0)" ::"i"(4 * NW4 + 16 - B1E) : "memory"); } else DRAIN();
      RAWBAR();
      __builtin_amdgcn_s_sleep(A3SLEEP);
      if (j < 14) { _Pragma("unroll 4") for (int i = 8; i < 16; ++i) GLDS_B1(HW * 16 + i); }
      if ((TMASK & 256) && HW == 1) T3_B2();
      if constexpr (layer == 0) sfor<5 + NEARLY + NW4, 10 - NW6>(ldB2);
      RAWBAR();
      if constexpr (layer == 0 && B1E < 16) asm volatile("s_waitcnt vmcnt(%0)" ::"i"(4 * (5 - NEARLY - NW6)) : "memory");
      putB1b(layer == 0 ? wb2 + 20 : wa1);
      DRAIN();
      BAR();
      FRESH();
      if constexpr (layer == 0) sfor<10 - NW6, 10>(ldB2);
      if (layer == 0 && j < 14) {
        f32x4 aS = z4, aT = z4; LAS char* sp = lds + (HW >> 1) * 65536 + ((HW & 1) ? TRB1 : TRB0);
        sfor<0, 4>([&](auto C) { colsum8(sp + C.v * 16384, 2048, 1024, aS, aT); });
        putS(3072 + HW * 256 + lane * 4, aS[0] + aT[0]);
      }
      BAR();
      RAWBAR();
      auto fill = [&](auto T) { sfor<0, 4>([&](auto I) { glds(lb + b2slot(T.v) + (HW * 4 + I.v) * 1024, B2SRC(T.v, HW) + I.v * 8192, vo1024); }); };
      fill(IC<0>{}); fill(IC<1>{}); fill(IC<2>{}); fill(IC<4>{});
      if constexpr (layer == 0) sfor<10, 10 + NB4>(ldB2);
      STAMP(25);
      RAWBAR();
      fill(IC<3>{});
      if constexpr (layer == 0) sfor<10 + NB4, 14>(ldB2);
      DRAIN();
      sfor<0, 36>([&](auto I) { pin(wb2[I.v]); });
      STAMP(26);
      RAWBAR();
      if (HW == 1) PSIG(2);
      if (HW == 0) { __builtin_amdgcn_s_sleep(PW2SLEEP); PWAIT(2); }
      STAMP(35);
      RAWBAR();
      FRESH();
      putB2(IC<5>{}); putB2(IC<6>{}); putB2(IC<7>{}); if (P8EARLY) putB2(IC<8>{});
      STAMP(56);
      BAR();
      if (HW == 1) PSIG(5);
      if (!P8EARLY) putB2(IC<8>{});
      if (HW == 0) { __builtin_amdgcn_s_sleep(PW5SLEEP); PWAIT(5); }
      STAMP(57);
      BAR();
      putB2(IC<9>{}); putB2(IC<10>{}); putB2(IC<11>{});
      STAMP(59);
      BAR();
      if constexpr (layer == 0) { if ((TMASK & 128) && HW == 1) T3_A(); if ((TMASK & 64) && HW == 2) T3_W(w1); }
      putB2(IC<12>{}); putB2(IC<13>{});
      STAMP(61);
      BAR();
      if constexpr (layer == 0) { _Pragma("unroll 4") for (int i = 0; i < 8; ++i) GLDS_Q(HW * 8 + i); }
      if constexpr (layer == 0) sfor<0, 8>([&](auto M) { putA1(IC<0>{}, M); });
      RAWBAR();
      if constexpr (layer == 0) {
        sfor<0, 8>([&](auto M) { putA1(IC<1>{}, M); });
        RAWBAR(); DRAIN(); BAR();
      }
    });
  } else {
    u32x4 resid[2];
    u32x4 wph[2];
    u32x4 xf[32];
    f32x16 acc[4];
    float rstd; u32 nmean;
    auto nop = [&](auto) {};
#define LDF(i, n) gld16_sc1<((n) & 3) * 1024>(xf[i], xb + ((n) >> 2) * 4096, l16)
#define FINSTATS(sum, ssq) { red2sum(sum, ssq); const float mean = sum * (1.f / 512.f); rstd = __builtin_amdgcn_rsqf(mx2(ssq * (1.f / 512.f) - mean * mean, 0.f) + 1e-5f); nmean = split2(-mean); }
#define STATS() { _Pragma("unroll") for (int i = 0; i < 4; ++i) pin(st[i]); \
      const f32x4 v = (__builtin_bit_cast(f32x4, st[0]) + __builtin_bit_cast(f32x4, st[1])) + (__builtin_bit_cast(f32x4, st[2]) + __builtin_bit_cast(f32x4, st[3])); \
      float a = v[0], b = v[1], c = v[2], d = v[3]; swap16(a, c); swap16(b, d); float sum = a + c, ssq = b + d; FINSTATS(sum, ssq) }
#define LDSTATS(SB_) { const char* sb = (SB_) + (size_t)(4 * P + w) * 4096; \
      gld16_sc1<0>(st[0], sb, l16); gld16_sc1<1024>(st[1], sb, l16); gld16_sc1<2048>(st[2], sb, l16); gld16_sc1<3072>(st[3], sb, l16); }
    constexpr int N0 = N0V;
#define STREAM32(NT, SB_) { constexpr int kb_ = s / NT; if constexpr (s % NT == 0) waitv<(N0 + kb_ < 36 ? N0 + kb_ : 36) - kb_ - 1>(xf[kb_]); \
      if constexpr (s % NT == 1 && N0 + kb_ < 32) LDF(N0 + kb_, N0 + kb_); \
      if constexpr (s % NT == 1 && N0 + kb_ >= 32 && N0 + kb_ < 36) gld16_sc1<(N0 + kb_ - 32) * 1024>(st[(N0 + kb_) & 3], (SB_) + (size_t)(4 * P + w) * 4096, l16); }
#define RES2ACC() { _Pragma("unroll") for (int i = 0; i < 8; ++i) { const u32 r = resid[i >> 2][i & 3]; acc[0][2 * i] = bflo(r); acc[0][2 * i + 1] = bfhi(r); } acc[1] = z16; asm volatile("" : "+v"(acc[0])); }
    for (int layer = 0; layer < 2; ++layer) {
      FRESH();
      acc[0] = z16; acc[1] = z16; acc[2] = z16;
      LAS char* wa1p = opaque(lds + TRA);
      auto ldkv = [&](auto KB, auto T) { return trfrag(wa1p + T.v * 32768 + KB.v * 1024, 512); };
      u32x4 sk, sv; LAS char* kt; LAS char* ka; u32 kvo;
      char* kb_ = ws + WS_KVP + (size_t)((layer * 16 + j) * 4 + w) * KVP_UNIT; asm volatile("" : "+s"(kb_));
      auto kvtail = [&](auto I) {
        if constexpr (I.v == 50) { kt = lds + LD_SC + (32 * w + tk) * 200 + 8 * hh; asm volatile("" : "+v"(kt)); }
        if constexpr (I.v == 52) { ka = lds + LD_SC + (4 * g + q) * 200 + 8 * p + (w >> 1) * 96; asm volatile("" : "+v"(ka)); }
        if constexpr (I.v == 54) { kvo = P * 512u + hh * 256u + t16 * 16u; asm volatile("" : "+v"(kvo)); }
        if constexpr (I.v == 56) { sk = *(LAS u32x4*)(lds + LD_S + l16); sv = *(LAS u32x4*)(lds + LD_S + 1024 + l16); }
        if constexpr (I.v == 61) { acc[0] = mfma(asb(sk), asb(u32x4{nmean, nmean, nmean, nmean}), acc[0]); asm volatile("" : "+v"(acc[0])); }
        if constexpr (I.v == 62) { acc[1] = mfma(asb(sv), asb(u32x4{nmean, nmean, nmean, nmean}), acc[1]); asm volatile("" : "+v"(acc[1])); }
      };
      if (layer == 0) {
        const char* xb = x + (size_t)(128 * P + 32 * w) * 1024;
        u32 xvo[4];
#pragma unroll
        for (int i = 0; i < 4; ++i) xvo[i] = g * 1024u + ((t16 ^ (4 * i + g)) * 16u);
        const u32 stg = lb + w * 8192;
        auto ldx = [&](auto R) { sfor<0, 8>([&](auto I) { glds(stg + R.v * 32768 + I.v * 1024, xb + R.v * 256 + I.v * 4096, xvo[I.v & 3]); }); };
        ldx(IC<0>{}); ldx(IC<1>{}); ldx(IC<2>{}); ldx(IC<3>{});
        u32 wp[8];
        const u16* wsrc = (const u16*)wphi + (2 * j + (tk >> 4)) * 256 + (4 * hh) * 16 + t16;
        auto ldwp = [&] { sfor<0, 8>([&](auto I) { gld2<(I.v >> 2) * 256 + (I.v & 3) * 32>(wp[I.v], wsrc); }); };
        if (WPOS == 0) ldwp();
        STAMP(32);
        const u32 str = w * 8192 + tk * 256 + t16 * 16 + 8 * hh;
        float sm[4] = {0.f, 0.f, 0.f, 0.f}, sq[4] = {0.f, 0.f, 0.f, 0.f};
        auto stats = [&](auto R) {
          constexpr int b = 8 * R.v;
          sfor<b, b + 8>([&](auto I) {
#pragma unroll
            for (int e = 0; e < 4; ++e) { sm[e] = dot2(xf[I.v][e], 0x3f803f80u, sm[e]); sq[e] = dot2(xf[I.v][e], xf[I.v][e], sq[e]); }
          });
          asm volatile("" : "+v"(sm[0]), "+v"(sm[1]), "+v"(sm[2]), "+v"(sm[3]), "+v"(sq[0]), "+v"(sq[1]), "+v"(sq[2]), "+v"(sq[3]));
          if ((cg >> 1) == R.v && (w >> 1) == th) {
            LAS char* rx = lds + LD_SC + cc * 4096 + l16;
            if (cg & 1) { _Pragma("unroll") for (int i = 0; i < 4; ++i) *(LAS u32x4*)(rx + i * 1024) = xf[b + 4 + i]; }
            else { _Pragma("unroll") for (int i = 0; i < 4; ++i) *(LAS u32x4*)(rx + i * 1024) = xf[b + i]; }
          }
        };
        sfor<0, 4>([&](auto R) {
          constexpr int b = 8 * R.v;
          asm volatile("s_waitcnt vmcnt(%0)" ::"i"((WPOS == 0 ? 32 : 24) - b) : "memory");
#pragma unroll
          for (int i = 0; i < 8; ++i) {
            u32x2 lo = *(LAS u32x2*)(lds + ((str ^ (32 * i)) + R.v * 32768)), hi = *(LAS u32x2*)(lds + ((str ^ (32 * i + 16)) + R.v * 32768));
            xf[b + i] = u32x4{lo.x, lo.y, hi.x, hi.y};
          }
          SB();
          if constexpr (R.v > 0) stats(IC<R.v - 1>{});
          STAMP(29 + (R.v == 3 ? 2 : R.v));
          if constexpr (R.v == BXR) RAWBAR();
        });
        stats(IC<3>{});
        float sum = (sm[0] + sm[1]) + (sm[2] + sm[3]), ssq = (sq[0] + sq[1]) + (sq[2] + sq[3]);
        FINSTATS(sum, ssq)
        auto mkwph = [&] {
          DRAIN();
#pragma unroll
          for (int i = 0; i < 8; ++i) pin1(wp[i]);
          const u32x4 wv = {wp[0] | (wp[1] << 16), wp[2] | (wp[3] << 16), wp[4] | (wp[5] << 16), wp[6] | (wp[7] << 16)};
          const u32x4 zz = {0u, 0u, 0u, 0u};
          wph[0] = tk < 16 ? wv : zz; wph[1] = tk < 16 ? zz : wv;
          pin(wph[0]); pin(wph[1]);
        };
        asm volatile("" : "+v"(rstd), "+v"(nmean));
        if (WPOS == 0) mkwph(); else ldwp();
        STAMP(33);
        BAR();
        STAMP(54);
        resid[0] = *(LAS u32x4*)(lds + LD_SC + cc * 4096 + kh * 2048 + l16); resid[1] = *(LAS u32x4*)(lds + LD_SC + cc * 4096 + kh * 2048 + 1024 + l16);
        gemm<2, 0, 32, 4, 32>(acc, xf, ldkv, [&](auto I) { if constexpr (I.v == 28) { pin(resid[0]); pin(resid[1]); if (WPOS == 1) mkwph(); STAMP(44); RAWBAR(); STAMP(45); }   }, kvtail);
      } else {
        u32x4 st[4];
        const char* xb = ws + WS_X2 + (size_t)(4 * P + w) * 32768;
        asm volatile("" : "+s"(xb));
        { constexpr int layer = 0; HANDOFF(4); }
        STAMP(40);
        sfor<0, N0>([&](auto I) { LDF(I.v, I.v); });
        STAMP(41);
        gemm<2, 0, 32, 4, 32>(acc, xf, ldkv, [&](auto I) {
          constexpr int s = I.v;
          if constexpr (s == 2 * (36 - N0)) RAWBAR();
          STREAM32(2, ws + WS_ST2)
          if constexpr (s == 59) DRAIN();
        }, [&](auto I) { if constexpr (I.v == 59) { STATS() asm volatile("" : "+v"(rstd), "+v"(nmean)); } kvtail(I); });
      }
      STAMP(1 + 12 * layer);
      {
        u32x4 yk[2], yv[2], f[2][2];
        pack16(acc[0], yk);
        f32x16 pp = mfma(asb(wph[0]), asb(yk[0]), z16); pp = mfma(asb(wph[1]), asb(yk[1]), pp);
        pack16(acc[1] * rstd, yv);
#pragma unroll
        for (int hd = 0; hd < 2; ++hd) { *(LAS u32x2*)(kt + hd * 96 + 64) = u32x2{yv[hd].x, yv[hd].y}; *(LAS u32x2*)(kt + hd * 96 + 80) = u32x2{yv[hd].z, yv[hd].w}; }
        softmax2(pp, rstd * 1.4426950408889634f, f, [](float*, float*) {});
#pragma unroll
        for (int hd = 0; hd < 2; ++hd) {
          *(LAS u32x2*)(kt + hd * 96) = u32x2{f[hd][0].x, f[hd][0].y}; *(LAS u32x2*)(kt + hd * 96 + 16) = u32x2{f[hd][0].z, f[hd][0].w};
          *(LAS u32x2*)(kt + hd * 96 + 32) = u32x2{f[hd][1].x, f[hd][1].y}; *(LAS u32x2*)(kt + hd * 96 + 48) = u32x2{f[hd][1].z, f[hd][1].w};
        }
      }
      STAMP(42);
      BAR();
      STAMP(43);
      {
        f32x4 akv = {0.f, 0.f, 0.f, 0.f}, az = {0.f, 0.f, 0.f, 0.f};
        bf16x8 A[4], B[4];
#pragma unroll
        for (int ks = 0; ks < 4; ++ks) { A[ks] = trfrag(ka + ks * 6400 + (w & 1) * 32, 3200); B[ks] = trfrag(ka + ks * 6400 + 64, 3200); }
        SB();
#pragma unroll
        for (int ks = 0; ks < 4; ++ks) { az = mfma16(asb(ones4), A[ks], az); akv = mfma16(A[ks], B[ks], akv); }
        const u32x2 kp = pk4(akv);
        auto r0 = __builtin_amdgcn_permlane16_swap(kp.x, kp.x, false, false), r1 = __builtin_amdgcn_permlane16_swap(kp.y, kp.y, false, false);
        gst16_sc1<0>(kb_, kvo, u32x4{r0[0], r1[0], r0[1], r1[1]});
        gst4_sc1(kb_, 8192u + (kvo >> 9) * 64u + ((kvo >> 2) & 60u), __float_as_uint(az[0]));
      }
      STAMP(37);
      FRESH();
      {
        u32x4 phq[2][2], yq[2], sq_, sel[2];
        u32 kc, zz[4], gvo;
        LAS char* gi; LAS char* gr; char* ob;
        LAS char* wq = opaque(lds + 65536 + TRA);
        gemm<1, 0, 32, 4, 32>(acc + 2, xf, [&](auto KB, auto) { return trfrag(wq + KB.v * 1024, 512); }, [&](auto I) {
          if constexpr (I.v == KSTEP) { DRAIN(); if (lane < 16) gadd1(ws, KOFF(lane)); STAMP(2 + 12 * layer); RAWBAR();   }
        }, [&](auto I) {
          if constexpr (I.v == 2) { gi = lds + LD_SC + 16384 + w * 680 + 8 * g; asm volatile("" : "+v"(gi)); }
          if constexpr (I.v == 4) { gr = lds + LD_SC + 16384 + (tk < 16 ? tk : 16) * 40 + 8 * hh; asm volatile("" : "+v"(gr)); }
          if constexpr (I.v == 6) { ob = ws + WS_O + (size_t)layer * (2u << 20) + ((size_t)(4 * P + w) * 32 + 2 * j) * 1024; gvo = g * 512u + t16 * 16u; asm volatile("" : "+s"(ob), "+v"(gvo)); }
          if constexpr (I.v == 24) sq_ = *(LAS u32x4*)(lds + LD_S + 2048 + l16);
          if constexpr (I.v == 30) { acc[2] = mfma(asb(sq_), asb(u32x4{nmean, nmean, nmean, nmean}), acc[2]); asm volatile("" : "+v"(acc[2])); }
        });
        STAMP(36);
        gld4_sc1(kc, ws, KOFF(P));
        sel[0] = *(LAS u32x4*)(lds + LD_ID + t16 * 32); sel[1] = *(LAS u32x4*)(lds + LD_ID + t16 * 32 + 16);
        pack16(acc[2], yq);
        f32x16 pp = mfma(asb(wph[0]), asb(yq[0]), z16); pp = mfma(asb(wph[1]), asb(yq[1]), pp);
        softmax2(pp, rstd * 1.4426950408889634f, phq, [&](float* mx, float* mn) {
          asm volatile("" : "+v"(mx[0]), "+v"(mx[1]), "+v"(mn[0]), "+v"(mn[1])); SB();
          STAMP(55);
          asm volatile("s_waitcnt vmcnt(0)" : "+v"(kc)::"memory");
          if (__builtin_amdgcn_readfirstlane(kc) < 16u * (layer + 1)) cnt_wait(ws, KOFF(P), 16u * (layer + 1));
          STAMP(3 + 12 * layer);
          sfor<0, 4>([&](auto I) { gld4o_sc1<I.v * 256>(zz[I.v], kb_ + 8192, lane * 4u); });
          sfor<0, 8>([&](auto I) { gld16_sc1<((I.v >> 1) & 1) * 2048 + (I.v & 1) * 256>(xf[I.v], kb_ + (I.v >> 2) * 4096, gvo); });
          RAWBAR();
          STAMP(46);
          SB();
        });
        pin(phq[0][0]); pin(phq[0][1]); pin(phq[1][0]); pin(phq[1][1]);
        STAMP(47);
        asm volatile("s_waitcnt vmcnt(8)" : "+v"(zz[0]), "+v"(zz[1]), "+v"(zz[2]), "+v"(zz[3]));
        f32x4 sz = __builtin_amdgcn_mfma_f32_16x16x4f32((__uint_as_float(zz[0]) + __uint_as_float(zz[1])) + (__uint_as_float(zz[2]) + __uint_as_float(zz[3])), 1.f, z4, 0, 0, 0);
        asm volatile("" : "+v"(sz)); SB();
        f32x4 s0 = z4, s1 = z4;
        sfor<0, 4>([&](auto I) {
          waitv<7 - 2 * I.v>(xf[2 * I.v]); s0 = mfma16(asb(sel[0]), asb(xf[2 * I.v]), s0);
          waitv<6 - 2 * I.v>(xf[2 * I.v + 1]); s1 = mfma16(asb(sel[1]), asb(xf[2 * I.v + 1]), s1);
          SB();
        });
        *(LAS u32x2*)(gi + 640) = pk4(sz);
        s0 += s1;
        *(LAS u32x2*)(gi + t16 * 40) = pk4(s0);
        STAMP(48);
        BAR();
        STAMP(49);
        f32x16 o[2]; u32x4 kf[4];
#pragma unroll
        for (int i = 0; i < 4; ++i) { const u32x2 a = *(LAS u32x2*)(gr + i * 680), b = *(LAS u32x2*)(gr + i * 680 + 16); kf[i] = u32x4{a.x, a.y, b.x, b.y}; }
        SB();
        o[0] = mfma(asb(kf[0]), asb(phq[0][0]), z16); o[1] = mfma(asb(kf[2]), asb(phq[1][0]), z16);
        o[0] = mfma(asb(kf[1]), asb(phq[0][1]), o[0]); o[1] = mfma(asb(kf[3]), asb(phq[1][1]), o[1]);
        asm volatile("" : "+v"(o[0]), "+v"(o[1])); SB();
        sfor<0, 2>([&](auto HD) {
          constexpr int hd = HD.v;
          const float r = __builtin_amdgcn_rcpf(o[hd][8] + 1e-6f);
          gst16_sc1<hd * 1024>(ob, l16, u32x4{pk2v(pr(o[hd], 0) * r), pk2v(pr(o[hd], 1) * r), pk2v(pr(o[hd], 2) * r), pk2v(pr(o[hd], 3) * r)});
        });
        STAMP(50);
        RES2ACC()
      }
#define TILE_REDUCE(EXOFF) { LAS char* ex = lds + (EXOFF) + l16; \
        _Pragma("unroll") for (int i = 0; i < 4; ++i) *(LAS f32x4*)(ex + w * 4096 + i * 1024) = f32x4{acc[1][4 * i], acc[1][4 * i + 1], acc[1][4 * i + 2], acc[1][4 * i + 3]}; \
        BAR(); \
        f32x4 rd[4]; _Pragma("unroll") for (int i = 0; i < 4; ++i) rd[i] = *(LAS f32x4*)(ex + (w ^ 2) * 4096 + i * 1024); SB(); \
        _Pragma("unroll") for (int i = 0; i < 4; ++i) { const f32x4 v = rd[i] + f32x4{acc[0][4 * i], acc[0][4 * i + 1], acc[0][4 * i + 2], acc[0][4 * i + 3]}; \
          resid[i >> 1][2 * (i & 1)] = pk2(v[0], v[1]); resid[i >> 1][2 * (i & 1) + 1] = pk2(v[2], v[3]); } }
#define TILE_PUBLISH(xo, so) { gst16_sc1<0>(xo, l16, resid[0]); gst16_sc1<1024>(xo, l16, resid[1]); \
        float sum = 0.f, ssq = 0.f, sum2 = 0.f, ssq2 = 0.f; \
        _Pragma("unroll") for (int e = 0; e < 4; ++e) { sum = dot2(resid[0][e], 0x3f803f80u, sum); ssq = dot2(resid[0][e], resid[0][e], ssq); sum2 = dot2(resid[1][e], 0x3f803f80u, sum2); ssq2 = dot2(resid[1][e], resid[1][e], ssq2); } \
        sum += sum2; ssq += ssq2; red2sum(sum, ssq); sum2 = sum; ssq2 = ssq; swap16(sum, sum2); swap16(ssq, ssq2); \
        if (lane < 16) gst16_sc1<0>(so, l16, u32x4{__float_as_uint(sum), __float_as_uint(ssq), __float_as_uint(sum2), __float_as_uint(ssq2)}); }
      FRESH();
      {
        const size_t ch = (size_t)(4 * P + 2 * th + cc);
        const char* xb = ws + WS_O + (size_t)layer * (2u << 20) + (ch * 32 + 16 * kh) * 1024;
        char* xo = ws + WS_X1 + (size_t)layer * (2u << 20) + (ch * 32 + 4 * cg + 2 * kh) * 1024;
        char* so = ws + WS_ST1 + (size_t)layer * (256u << 10) + (ch * 16 + 2 * cg + kh) * 256;
        LAS char* wa[2] = {opaque(lds + LD_WA3 + kh * 32768 + (TRB0 ^ (64 * kh))), opaque(lds + LD_WA3 + kh * 32768 + (TRB1 ^ (64 * kh)))};
        asm volatile("" : "+s"(xb), "+s"(xo), "+s"(so));
        HANDOFF(0);
        STAMP(5 + 12 * layer);
        sfor<0, N0>([&](auto I) { LDF(I.v, I.v); });
        STAMP(51);
        gemm<2, 0, 16, 4, 32>(acc, xf, [&](auto KB, auto T) { return trfrag(wa[T.v] + KB.v * 2048, 1024); }, [&](auto I) {
          constexpr int s = I.v;
          if constexpr (s % 2 == 0) waitv<(N0 + s / 2 < 16 ? N0 + s / 2 : 16) - s / 2 - 1>(xf[s / 2]);
          if constexpr (s % 2 == 1 && N0 + s / 2 < 16) LDF(N0 + s / 2, N0 + s / 2);
        }, nop);
        STAMP(52);
        TILE_REDUCE(LD_SC)
        STAMP(53);
        TILE_PUBLISH(xo, so)
      }
      FRESH();
      const char* hx = ws + WS_H + (size_t)layer * (7u << 20) + ((size_t)(4 * P + 2 * th + cc) * 112 + 56 * kh) * 1024;
#define LDH(i) gld16_sc1<((2 * ((i) / 14) + (i) % 2) & 3) * 1024>(xf[(i) % 32], hx + (2 * (((i) % 14) / 2) + (i) / 28) * 4096, l16)
      {
        const char* xb = ws + WS_X1 + (size_t)layer * (2u << 20) + (size_t)(4 * P + w) * 32768;
        char* hb = ws + WS_H + (size_t)layer * (7u << 20) + ((size_t)(4 * P + w) * 112 + 8 * j) * 1024;
        LAS char* wa[4] = {opaque(lds + TRB0), opaque(lds + TRB1), opaque(lds + 65536 + TRB0), opaque(lds + 65536 + TRB1)};
        asm volatile("" : "+s"(xb), "+s"(hb), "+s"(hx));
        HANDOFF(1);
        STAMP(7 + 12 * layer);
        if (j < 14) {
          u32x4 st[4]; u32 sb[4]; float c1, sigma;
          auto fix = [&](auto T) { acc[T.v] = mfma(asb(u32x4{sb[T.v], 0u, 0u, 0u}), asb(u32x4{nmean, 0u, 0u, 0u}), acc[T.v]); asm volatile("" : "+v"(acc[T.v])); };
          sfor<0, N0>([&](auto I) { LDF(I.v, I.v); });
          STAMP(28);
          acc[0] = z16; acc[1] = z16; acc[2] = z16; acc[3] = z16;
          gemm<2, 0, 32, 4, 32>(acc, xf, [&](auto KB, auto T) { return trfrag(wa[T.v] + KB.v * 2048, 1024); }, [&](auto I) {
            constexpr int s = I.v;
            if constexpr (s == 2 * (36 - N0)) RAWBAR();
            STREAM32(2, ws + WS_ST1 + (size_t)layer * (256u << 10))
            if constexpr (s == 59) DRAIN();
          }, [&](auto I) {
            if constexpr (I.v == 54) { _Pragma("unroll") for (int i = 0; i < 4; ++i) sb[i] = *(LAS u32*)(lds + LD_S + 3072 + i * 256 + lane * 4); }
            if constexpr (I.v == 59) { STATS() asm volatile("" : "+v"(rstd), "+v"(nmean)); }
            if constexpr (I.v == 61) fix(IC<0>{});
            if constexpr (I.v == 62) fix(IC<1>{});
            if constexpr (I.v == 63) { c1 = rstd * -1.4426950408889634f; sigma = __builtin_amdgcn_rcpf(rstd); asm volatile("" : "+v"(c1), "+v"(sigma)); }
          });
          STAMP(25);
          fix(IC<2>{});
          float tt[8]; u32x4 hp;
          auto uop = [&](auto M) {
            constexpr int st_ = M.v % 6, sl = M.v / 6, e = sl < 37 ? sl - st_ : sl - 5 - st_, t = e / 16, r = e % 16;
            if constexpr (sl < 37 ? (e >= 0 && e < 32) : (e >= 32 && e < 64)) {
              if constexpr (st_ == 0) tt[e & 7] = acc[t][r] * c1;
              if constexpr (st_ == 1) tt[e & 7] = __builtin_amdgcn_exp2f(tt[e & 7]);
              if constexpr (st_ == 2) tt[e & 7] = __builtin_fmaf(tt[e & 7], sigma, sigma);
              if constexpr (st_ == 3) tt[e & 7] = __builtin_amdgcn_rcpf(tt[e & 7]);
              if constexpr (st_ == 4) tt[e & 7] = acc[t][r] * tt[e & 7];
              if constexpr (st_ < 5) asm volatile("" : "+v"(tt[e & 7]));
              if constexpr (st_ == 5 && (e & 1)) {
                u32 pkd = pk2(tt[(e - 1) & 7], tt[e & 7]); pin1(pkd); hp[(e >> 1) & 3] = pkd;
                if constexpr ((e & 7) == 7) gst16_sc1<((e >> 3) & 3) * 1024>(hb + (e >> 5) * 4096, l16, hp);
              }
            }
          };
          constexpr int UB = 37 * 6, UC = 53 * 6;
          gemm<1, 0, 32, 4, 32>(acc + 2, xf, [&](auto KB, auto) { return trfrag(wa[2] + KB.v * 2048, 1024); }, [&](auto I) { if constexpr (I.v == B5S) RAWBAR();   },
                                [&](auto I) { constexpr int i = I.v, u0 = i < 2 ? 12 * i : i < 29 ? 12 + 6 * i : 186 + 12 * (i - 29), u1 = i + 1 < 2 ? 12 : i + 1 < 29 ? 18 + 6 * i : 186 + 12 * (i - 28); sfor<u0, u1>(uop); });
          STAMP(27);
          fix(IC<3>{});
          gemm<1, 0, 32, 4, 32>(acc + 3, xf, [&](auto KB, auto) { return trfrag(wa[3] + KB.v * 2048, 1024); }, [&](auto I) {
            if constexpr (I.v == B5C) { STAMP(34); DRAIN(); RAWBAR(); STAMP(21); }
          }, [&](auto I) { if constexpr (I.v >= 1) sfor<UB + (I.v - 1) * (UC - UB) / 31, UB + I.v * (UC - UB) / 31>(uop); });
          STAMP(8 + 12 * layer);
          sfor<UC, 75 * 6>(uop);
        } else { RAWBAR(); RAWBAR(); RAWBAR(); }
      }
      FRESH();
      {
        const size_t ch = (size_t)(4 * P + 2 * th + cc);
        const int t0 = kh * 8192 + (TRB0 ^ (64 * kh)), t1 = t0 ^ 64;
        LAS char* wa[6] = {opaque(lds + t0), opaque(lds + t1), opaque(lds + 65536 + t0), opaque(lds + 65536 + t1), opaque(lds + 131072 + t0), opaque(lds + 131072 + t1)};
        char* xo = layer == 0 ? ws + WS_X2 + (ch * 32 + 4 * cg + 2 * kh) * 1024 : out + (size_t)(128 * P + 64 * th + 32 * cc) * 1024 + 128 * cg + 64 * kh;
        char* so = ws + WS_ST2 + (ch * 16 + 2 * cg + kh) * 256;
        asm volatile("" : "+s"(xo), "+s"(so));
        RES2ACC()
        STAMP(35);
        RAWBAR();
        STAMP(22);
        sfor<0, NB>([&](auto I) { LDH(I.v); });
        STAMP(56);
        if (layer == 0) STAMP(10);
        gemm<2, 0, 56, 4, 32>(acc, xf, [&](auto KB, auto T) { constexpr int so = b2slot(KB.v >> 2); return trfrag(wa[(so >> 16) * 2 + T.v] + (so & 65535) + (KB.v & 3) * 2048, 1024); }, [&](auto I) {
          constexpr int s = I.v, kb = s / 2;
          if constexpr (s == X2S) STAMP(57);
          if constexpr (s == 68) STAMP(59);
          if constexpr (s == 92) STAMP(61);
          if constexpr (s == 1 || s == X2S || s == 68 || s == 92) RAWBAR();
          if constexpr (s == 1) STAMP(38);
          if constexpr (s == X2S) STAMP(58);
          if constexpr (s == 68) STAMP(60);
          if constexpr (s == X2S) sfor<28, NB + kb>([&](auto J) { LDH(J.v); });
          constexpr int lim = s < X2S ? 28 : 56;
          if constexpr (s % 2 == 0) waitv<(NB + kb < lim ? NB + kb : lim) - kb - 1>(xf[kb % 32]);
          if constexpr (s % 2 == 1 && NB + kb < lim) LDH(NB + kb);
        }, nop);
        STAMP(11 + 12 * layer);
        TILE_REDUCE(98304)
        if (layer == 0) {
          TILE_PUBLISH(xo, so)
        } else {
          sfor<0, 2>([&](auto S) {
            auto r0 = __builtin_amdgcn_permlane32_swap(resid[S.v].x, resid[S.v].z, false, false);
            auto r1 = __builtin_amdgcn_permlane32_swap(resid[S.v].y, resid[S.v].w, false, false);
            gst16<32 * S.v>(xo, tk * 1024u + 16u * hh, u32x4{r0[0], r1[0], r0[1], r1[1]});
          });
        }
      }
    }
  }
}

extern "C" void kernel_launch(void* const* d_in, const int* in_sizes, int n_in, void* d_out, int out_size, void* d_ws, size_t ws_size, hipStream_t stream) {
  mega<<<256, 512, 0, stream>>>((const char*)d_in[0], (const char*)d_in[1], (const char*)d_in[2], (const char*)d_in[3], (const char*)d_in[5], (const char*)d_in[6], (char*)d_out, (char*)d_ws);
}
```

```cpp
#include <hip/hip_runtime.h>
#include <hip/hip_bf16.h>

typedef unsigned u32;
typedef unsigned short u16;
typedef u32 u32x4 __attribute__((ext_vector_type(4)));
typedef u32 u32x2 __attribute__((ext_vector_type(2)));
typedef float f32x16 __attribute__((ext_vector_type(16)));
typedef float f32x4 __attribute__((ext_vector_type(4)));
typedef float f32x2 __attribute__((ext_vector_type(2)));
typedef short s16x4 __attribute__((ext_vector_type(4)));
typedef __bf16 bf16x2 __attribute__((ext_vector_type(2)));
typedef __bf16 bf16x8 __attribute__((ext_vector_type(8)));
#define LAS __attribute__((address_space(3)))
#define DI __device__ __forceinline__

constexpr size_t WS_ST1 = 512u << 10;
constexpr size_t WS_ST2 = 1024u << 10;
constexpr size_t WS_KVP = 1280u << 10;
constexpr u32 KVP_UNIT = 9216;
constexpr size_t WS_O = 4u << 20;
constexpr size_t WS_X1 = 8u << 20;
constexpr size_t WS_X2 = 12u << 20;
constexpr size_t WS_H = 14u << 20;
constexpr u32 C_HEL = 1024;

constexpr u32 LD_WA3 = 64 * 1024;
constexpr u32 LD_SC = 128 * 1024;
constexpr u32 LD_S = 153 * 1024;
constexpr u32 LD_ID = 159 * 1024 + 512;
constexpr u32 LD_DUMP = 159 * 1024;
constexpr int CODE_KB = 40;
constexpr int N0 = 12;
constexpr int KSTEP = 12, B5C = 12, X2S = 44, NB = 8;
constexpr u32 SPIN_CAP = 1u << 17;

#define BAR() do { asm volatile("s_waitcnt lgkmcnt(0)" ::: "memory"); __builtin_amdgcn_s_barrier(); asm volatile("" ::: "memory"); } while (0)
#define RAWBAR() do { asm volatile("" ::: "memory"); __builtin_amdgcn_s_barrier(); asm volatile("" ::: "memory"); } while (0)
#define FRESH() do { asm volatile("" : "+s"(ws), "+s"(P), "+s"(j), "+s"(w), "+s"(x), "+s"(wqkv), "+s"(wo), "+s"(w1), "+s"(w2)); asm volatile("" : "+v"(lane)); th = j >> 3; cg = j & 7; cc = w & 1; kh = (w >> 1) & 1; \
  hh = lane >> 5; tk = lane & 31; g = lane >> 4; t16 = lane & 15; q = (lane >> 2) & 3; p = lane & 3; l16 = lane * 16u; } while (0)
#define TRB0 ((4 * hh + q) * 128 + 64 * (q >> 1) + 32 * (g & 1) + 8 * p)
#define TRB1 (TRB0 ^ 64)
#define TRA ((4 * hh + q) * 64 + 32 * (g & 1) + 8 * p)
#define HEL_OWN ((u32)(P * 16 + j) * 2048u + C_HEL + 8u * tk)
#define HEL_PEER (C_HEL + (tk < 16 ? (u32)(P * 16 + tk) * 2048u + 8u * j : (u32)((tk - 16) * 16 + j) * 2048u + 128u + 8u * P))
#define CNT(n) ((u32)(P * 16 + j) * 2048u + (n) * 128u)
#define PSIG(n) do { if (lane < 16) gadd1(ws, (u32)(P * 16 + lane) * 2048u + (n) * 128u); } while (0)
#define KSIG() do { if (lane < 16) gadd1(ws, (u32)(lane * 16 + j) * 2048u + 6u * 128u); } while (0)
#define PWAIT(n) cnt_wait(ws, CNT(n), 16u * (layer + 1))
constexpr int b2row(int u) { return 128 * (u % 7) + 32 * (u / 7); }
#define B2SRC(t, c) (w2 + cg * 128 + (size_t)(896 * ((c) >> 1) + (((c) & 1) ? b2row(2 * (t) + 1) : b2row(2 * (t)))) * 1024)
#define CGS ((3 - (int)((u32)(size_t)w2 >> 7)) & 7)
#define JB (j - th - (cg > CGS || cg == 7 ? 1 : 0))
#define DRAIN() asm volatile("s_waitcnt vmcnt(0)" ::: "memory")

template <int N> DI void waitv(u32x4& a) { asm volatile("s_waitcnt vmcnt(%1)" : "+v"(a) : "i"(N)); }
template <int N> DI void waitv4(u32x4& a, u32x4& b, u32x4& c, u32x4& d) { asm volatile("s_waitcnt vmcnt(%4)" : "+v"(a), "+v"(b), "+v"(c), "+v"(d) : "i"(N)); }
DI void pin(u32x4& a) { asm volatile("" : "+v"(a)); }
DI void pin2(u32x2& a) { asm volatile("" : "+v"(a)); }
DI void pin1(u32& a) { asm volatile("" : "+v"(a)); }

template <int OFF> DI void gld16_sc1(u32x4& d, const char* sb, u32 vo) { asm volatile("global_load_dwordx4 %0, %1, %2 offset:%3 sc1" : "=v"(d) : "v"(vo), "s"(sb), "i"(OFF) : "memory"); }
template <int OFF> DI void gld16(u32x4& d, const char* sb, u32 vo) { asm volatile("global_load_dwordx4 %0, %1, %2 offset:%3" : "=v"(d) : "v"(vo), "s"(sb), "i"(OFF) : "memory"); }
DI void gld8_sc1(u32x2& d, const char* sb, u32 vo) { asm volatile("global_load_dwordx2 %0, %1, %2 sc1" : "=v"(d) : "v"(vo), "s"(sb) : "memory"); }
template <int OFF> DI void gld4o_sc1(u32& d, const char* sb, u32 vo) { asm volatile("global_load_dword %0, %1, %2 offset:%3 sc1" : "=v"(d) : "v"(vo), "s"(sb), "i"(OFF) : "memory"); }
template <int OFF> DI void gld2(u32& d, const u16* a) { asm volatile("global_load_ushort %0, %1, off offset:%2" : "=v"(d) : "v"(a), "i"(OFF) : "memory"); }
DI void gld4_sc1(u32& d, const char* sb, u32 vo) { asm volatile("global_load_dword %0, %1, %2 sc1" : "=v"(d) : "v"(vo), "s"(sb) : "memory"); }
template <int OFF> DI void gst16_sc1(char* sb, u32 vo, u32x4 d) { asm volatile("global_store_dwordx4 %0, %1, %2 offset:%3 sc1\n\ts_nop 1" ::"v"(vo), "v"(d), "s"(sb), "i"(OFF) : "memory"); }
DI void gst8_sc1(char* sb, u32 vo, u32x2 d) { asm volatile("global_store_dwordx2 %0, %1, %2 sc1" ::"v"(vo), "v"(d), "s"(sb) : "memory"); }
DI void gadd1(char* sb, u32 vo) { asm volatile("global_atomic_add %0, %1, %2 sc1" ::"v"(vo), "v"(1u), "s"(sb) : "memory"); }
template <int OFF> DI void gst16(char* sb, u32 vo, u32x4 d) { asm volatile("global_store_dwordx4 %0, %1, %2 offset:%3\n\ts_nop 1" ::"v"(vo), "v"(d), "s"(sb), "i"(OFF) : "memory"); }
DI void glds(u32 ldsaddr, const char* sb, u32 vo) { asm volatile("s_mov_b32 m0, %0\n\ts_nop 0\n\tglobal_load_lds_dwordx4 %1, %2" ::"s"(ldsaddr), "v"(vo), "s"(sb) : "memory"); }
DI void touch(u32 ldsaddr, const char* a) { asm volatile("s_mov_b32 m0, %0\n\ts_nop 0\n\tglobal_load_lds_dword %1, off" ::"s"(ldsaddr), "v"(a) : "memory"); }
DI void touch3(u32 ldsaddr, const char* a) { touch(ldsaddr, a + (((3u - ((u32)(size_t)a >> 7)) & 7u) << 7)); }

DI bf16x8 trfrag2(LAS char* p, LAS char* p2) {
  s16x4 a = __builtin_amdgcn_ds_read_tr16_b64_v4i16((LAS s16x4*)p);
  s16x4 b = __builtin_amdgcn_ds_read_tr16_b64_v4i16((LAS s16x4*)p2);
  return __builtin_bit_cast(bf16x8, __builtin_shufflevector(a, b, 0, 1, 2, 3, 4, 5, 6, 7));
}
DI bf16x8 trfrag(LAS char* p, int second) { return trfrag2(p, p + second); }
DI bf16x8 asb(u32x4 v) { return __builtin_bit_cast(bf16x8, v); }
DI f32x4 mfma16(bf16x8 a, bf16x8 b, f32x4 c) { return __builtin_amdgcn_mfma_f32_16x16x32_bf16(a, b, c, 0, 0, 0); }
DI f32x16 mfma(bf16x8 a, bf16x8 b, f32x16 c) { return __builtin_amdgcn_mfma_f32_32x32x16_bf16(a, b, c, 0, 0, 0); }
DI u32 pk2(float a, float b) { f32x2 v = {a, b}; return __builtin_bit_cast(u32, __builtin_convertvector(v, bf16x2)); }
DI u32 pk2v(f32x2 v) { return __builtin_bit_cast(u32, __builtin_convertvector(v, bf16x2)); }
DI u32x2 pk4(f32x4 v) { u32x2 r; r.x = pk2(v[0], v[1]); r.y = pk2(v[2], v[3]); return r; }
DI float bflo(u32 u) { return __uint_as_float(u << 16); }
DI float bfhi(u32 u) { return __uint_as_float(u & 0xffff0000u); }
DI float dot2(u32 a, u32 b, float c) { return __builtin_amdgcn_fdot2_f32_bf16(__builtin_bit_cast(bf16x2, a), __builtin_bit_cast(bf16x2, b), c, false); }
DI void swap32(float& a, float& b) { auto r = __builtin_amdgcn_permlane32_swap(__float_as_uint(a), __float_as_uint(b), false, false); a = __uint_as_float(r[0]); b = __uint_as_float(r[1]); }
DI void swap16(float& a, float& b) { auto r = __builtin_amdgcn_permlane16_swap(__float_as_uint(a), __float_as_uint(b), false, false); a = __uint_as_float(r[0]); b = __uint_as_float(r[1]); }
DI float mx2(float a, float b) { return __builtin_fmaxf(a, b); }
DI float mx3(float a, float b, float c) { float r = mx2(mx2(a, b), c); asm("" : "+v"(r)); return r; }
DI void red2max(float& a, float& b, float ninf) { swap32(a, b); float u = mx3(a, b, ninf); a = u; b = u; swap32(a, b); }
DI void red2sum(float& a, float& b) { swap32(a, b); float u = a + b; a = u; b = u; swap32(a, b); }
DI f32x2 pr(const f32x16& a, int i) { return f32x2{a[2 * i], a[2 * i + 1]}; }

template <class MID, class MID2> DI void softmax2(const f32x16& p, const float L2E  , u32x4 (&f)[2][2], MID&& mid, MID2&& mid2) {
  float mx[2], mn[2], ninf = -__builtin_inff();
  asm("" : "+v"(ninf));
#pragma unroll
  for (int hd = 0; hd < 2; ++hd) {
    const int b = 8 * hd;
    mx[hd] = mx3(mx3(p[b], p[b + 1], p[b + 2]), mx3(p[b + 3], p[b + 4], p[b + 5]), mx3(p[b + 6], p[b + 7], ninf));
    mn[hd] = mx3(mx3(-p[b], -p[b + 1], -p[b + 2]), mx3(-p[b + 3], -p[b + 4], -p[b + 5]), mx3(-p[b + 6], -p[b + 7], ninf));
    red2max(mx[hd], mn[hd], ninf);
  }
  mid(mx, mn);
  f32x2 e[2][2][4]; float s[2][2];
#pragma unroll
  for (int hd = 0; hd < 2; ++hd) {
    const float cp = -mx[hd] * L2E, cn = -mn[hd] * L2E;
#pragma unroll
    for (int i = 0; i < 4; ++i) {
      const f32x2 v = pr(p, 4 * hd + i), a = v * L2E + cp, b = v * -L2E + cn;
      e[hd][0][i] = f32x2{__builtin_amdgcn_exp2f(a.x), __builtin_amdgcn_exp2f(a.y)};
      e[hd][1][i] = f32x2{__builtin_amdgcn_exp2f(b.x), __builtin_amdgcn_exp2f(b.y)};
    }
#pragma unroll
    for (int sg = 0; sg < 2; ++sg) { const f32x2 t = (e[hd][sg][0] + e[hd][sg][1]) + (e[hd][sg][2] + e[hd][sg][3]); s[hd][sg] = t.x + t.y; }
    red2sum(s[hd][0], s[hd][1]);
#pragma unroll
    for (int sg = 0; sg < 2; ++sg) {
      const float r = __builtin_amdgcn_rcpf(s[hd][sg]);
#pragma unroll
      for (int i = 0; i < 4; ++i) f[hd][sg][i] = pk2v(e[hd][sg][i] * r);
    }
    if (hd == 0) mid2();
  }
}
DI void pack16(const f32x16& a, u32x4 (&y)[2]) {
#pragma unroll
  for (int i = 0; i < 8; ++i) y[i >> 2][i & 3] = pk2v(pr(a, i));
}
DI u32 split2(float v) { const u32 hi = pk2(v, v) & 0xffffu; return hi | (pk2(v - bflo(hi), 0.f) << 16); }

template <int V> struct IC { static constexpr int v = V; };
template <int B, int E, class F> DI void sfor(F&& f) { if constexpr (B < E) { f(IC<B>{}); sfor<B + 1, E>(f); } }
#define SB() __builtin_amdgcn_sched_barrier(0)
template <int NT, int K0, int K1, int D, int XM, class LD, class PRE, class POST> DI void gemm(f32x16* acc, u32x4* xf, LD&& ld, PRE&& pre, POST&& post) {
  bf16x8 wf[D + 1];
  constexpr int S0 = K0 * NT, S1 = K1 * NT;
  sfor<S0, S0 + D>([&](auto I) { constexpr int s = I.v; wf[s % (D + 1)] = ld(IC<s / NT>{}, IC<s % NT>{}); });
  SB();
  sfor<S0, S1>([&](auto I) {
    constexpr int s = I.v;
    pre(I);
    if constexpr (s + D < S1) wf[(s + D) % (D + 1)] = ld(IC<(s + D) / NT>{}, IC<(s + D) % NT>{});
    SB();
    post(I);
    acc[s % NT] = mfma(wf[s % (D + 1)], asb(xf[(s / NT) % XM]), acc[s % NT]);
    asm volatile("" : "+v"(acc[s % NT]));
    SB();
  });
}
constexpr int g1off(int s) { return (s & 31) * 2048 + (s >> 5) * 1024; }
template <class PRE, class POST> DI void gemm1(f32x16* acc, u32x4* xf, u32x4* wf, LAS char* wp, PRE&& pre, POST&& post) {
  sfor<0, 64>([&](auto I) {
    constexpr int s = I.v;
    pre(I);
    if constexpr (s % 2 == 0 && s + 4 < 64) { wf[(s + 5) % 6] = *(LAS u32x4*)(wp + g1off(s + 5)); SB(); wf[(s + 4) % 6] = *(LAS u32x4*)(wp + g1off(s + 4)); }
    SB();
    post(I);
    acc[s >> 5] = mfma(asb(wf[s % 6]), asb(xf[s & 31]), acc[s >> 5]);
    asm volatile("" : "+v"(acc[s >> 5]));
    SB();
  });
}
constexpr int b2slot(int t) { return t < 4 ? t * 16384 : t == 4 || t == 13 ? 131072 : t < 9 ? 65536 + (t - 5) * 16384 : (t - 9) * 16384; }
DI LAS char* opaque(LAS char* p) { asm volatile("" : "+v"(p)); return p; }

DI void cnt_wait(const char* ctl, u32 off, u32 val) {
  for (u32 sp = 0; sp < SPIN_CAP; ++sp) {
    u32 v; gld4_sc1(v, ctl, off); asm volatile("s_waitcnt vmcnt(0)" : "+v"(v)::"memory");
    if (__builtin_amdgcn_readfirstlane(v) >= val) break;
  }
}
#define HANDOFF(n) do { u32 to_ = (u32)(P * 16 + lane) * 2048u + (n) * 128u; pin1(to_); DRAIN(); RAWBAR(); \
    if (w == 0) { if (lane < 16) gadd1(ws, to_); __builtin_amdgcn_s_sleep(13); PWAIT(n); } RAWBAR(); } while (0)

__host__ __device__ constexpr int map_p(int b) { return ((b & 7) >> 1) * 4 + (b >> 6); }
__host__ __device__ constexpr int map_j(int b) { return (b & 1) * 8 + ((b >> 3) & 7); }
__global__ __launch_bounds__(512, 2) void mega(const char* x, const char* wqkv, const char* wphi, const char* wo, const char* w1, const char* w2, char* out, char* ws) {
  __shared__ __attribute__((aligned(16))) char smem[163840];
  LAS char* lds = (LAS char*)smem;
  const u32 lb = (u32)(size_t)lds;
  int lane = threadIdx.x & 63; int w = __builtin_amdgcn_readfirstlane(threadIdx.x >> 6);
  int hh = lane >> 5, tk = lane & 31, g = lane >> 4, t16 = lane & 15, q = (lane >> 2) & 3, p = lane & 3;
  const int bid = blockIdx.x, loc = bid >> 3;
  int P = map_p(bid), j = map_j(bid);
  int th = j >> 3, cg = j & 7, cc = w & 1, kh = (w >> 1) & 1;
  u32 l16 = lane * 16u;
  const u32x4 ones4 = {0x3f803f80u, 0x3f803f80u, 0x3f803f80u, 0x3f803f80u};
  const f32x16 z16 = {0.f, 0.f, 0.f, 0.f, 0.f, 0.f, 0.f, 0.f, 0.f, 0.f, 0.f, 0.f, 0.f, 0.f, 0.f, 0.f};
  const f32x4 z4 = {0.f, 0.f, 0.f, 0.f};
  size_t pc_; asm volatile("s_getpc_b64 %0" : "=s"(pc_));
  const char* pc = (const char*)(pc_ & ~(size_t)255);
  {
    const int id = (w ^ 4) * 32 + loc;
    if (id < CODE_KB) glds(lb + LD_SC + 8192 + w * 1024, pc + id * 1024, l16);
  }
  if (lane < 16) touch(lb + LD_DUMP, x + (size_t)(128 * P + (loc & 7) * 16 + w * 2) * 1024 + lane * 128);
  if (w >= 4) {
    const u32 swz = 16u * ((lane & 7) ^ ((g & 1) << 2));
    const u32 vo1024 = (lane >> 3) * 1024u + swz, vo3584 = (lane >> 3) * 3584u + swz, voA1 = (lane >> 2) * 3072u + 16u * (lane & 3);
    u32x4 wa1[16];
    u32x4 wb2[36];
    LAS char* wl = lds + l16;
#define HW (w & 3)
#define GLDS_Q(n) glds(lb + 65536 + (n) * 1024, wqkv + 64 * j + (size_t)((n) * 16) * 3072, voA1)
#define GLDS_B1(n) glds(lb + (n) * 1024, w1 + (size_t)(((n) & 63) * 8) * 3584 + JB * 256 + ((n) >> 6) * 128, vo3584)
#define GLDS_A3(n) glds(lb + LD_WA3 + (n) * 1024, wo + (size_t)(n) * 8192 + cg * 128, vo1024)
    auto putA1 = [&](auto R, auto M) { *(LAS u32x4*)(wl + R.v * 32768 + (4 * M.v + HW) * 1024) = wa1[R.v * 8 + M.v]; };
    auto putB2 = [&](auto T) { sfor<0, 4>([&](auto I) { *(LAS u32x4*)(wl + b2slot(T.v) + (4 * HW + I.v) * 1024) = wb2[(T.v - 5) * 4 + I.v]; }); };
    auto ldB2 = [&](auto T) { sfor<0, 4>([&](auto I) { gld16<0>(wb2[(T.v - 5) * 4 + I.v], B2SRC(T.v, HW) + I.v * 8192, vo1024); }); };
    auto ldB1b = [&](u32x4* r) { const char* b = w1 + (size_t)(HW * 128) * 3584 + JB * 256 + 128; sfor<0, 16>([&](auto I) { gld16<0>(r[I.v], b + (size_t)(I.v * 8) * 3584, vo3584); }); };
    auto putB1b = [&](u32x4* r) { sfor<0, 16>([&](auto I) { pin(r[I.v]); *(LAS u32x4*)(wl + 65536 + (HW * 16 + I.v) * 1024) = r[I.v]; }); };
    auto colsum8 = [&](LAS char* sp, int kstride, int second, f32x4& a, f32x4& b) {
      const u32 o = ((t16 >> 2) & 1) == (g & 1) ? 0x3f803f80u : 0u; const u32x4 sel = {o, o, o, o};
      bf16x8 wf[8];
      sfor<0, 8>([&](auto K) { wf[K.v] = trfrag(sp + K.v * kstride, second); });
      sfor<0, 4>([&](auto K) { a = mfma16(asb(sel), wf[2 * K.v], a); b = mfma16(asb(sel), wf[2 * K.v + 1], b); });
      asm volatile("" : "+v"(a), "+v"(b)); SB();
    };
    auto putS = [&](u32 off, float a) { const u32 sp = split2(a); *(LAS u32*)(lds + LD_S + off) = __builtin_amdgcn_perm(sp, sp, hh ? 0x03020302u : 0x01000100u); };
#define T3_KV() do { if (lane < 32) touch3(lb + LD_DUMP, wqkv + (size_t)(16 * loc + t16) * 3072 + 1024 + g * 1024); } while (0)
#define T3_A() do { if (lane < 32) touch3(lb + LD_DUMP, g == 0 ? wqkv + (size_t)(16 * loc + t16) * 3072 : wo + (size_t)(16 * loc + t16) * 1024); } while (0)
#define T3_B2() do { const int i_ = 20 * loc + lane; if (lane < 20) touch3(lb + LD_DUMP, w2 + (size_t)(896 * ((i_ >> 5) & 1) + 128 * (i_ >> 6) - (i_ < 448 ? 0 : 864) + (i_ & 31)) * 1024); } while (0)
    {
      if (HW == 0) T3_KV();
      if (HW == 1) *(LAS u32x2*)(lds + LD_ID + lane * 8) = g == p ? u32x2{q == 0 ? 0x3f80u : q == 1 ? 0x3f800000u : 0u, q == 2 ? 0x3f80u : q == 3 ? 0x3f800000u : 0u} : u32x2{0u, 0u};
      u32x2 h0 = {0u, 0u};
      if (HW == 3) {
        gld8_sc1(h0, ws, HEL_OWN); gst16_sc1<0>(ws + (size_t)(P * 16 + j) * 2048, l16, u32x4{0u, 0u, 0u, 0u});
      }
      __builtin_amdgcn_s_sleep(14);
      const char* wb = wqkv + 64 * j + (size_t)(16 * HW) * 3072;
      sfor<0, 8>([&](auto M) { gld16<1024>(wa1[M.v], wb + (size_t)(64 * M.v) * 3072, voA1); gld16<2048>(wa1[8 + M.v], wb + (size_t)(64 * M.v) * 3072, voA1); });
      {
        const int id = P * 16 + j;
        if (HW == 1) T3_A();
        else if (HW == 3) { if (lane < 8) touch(lb + LD_DUMP, wphi + j * 1024 + lane * 128); }
        else touch(lb + LD_DUMP, lane < 56 ? (HW ? w2 : w1) + (size_t)id * 7168 + lane * 128 : wo + (size_t)id * 2048 + (lane - 56) * 256 + (HW ? 0 : 128));
      }
      f32x4 sk = z4, sv = z4;
      u32x4 sel;
#pragma unroll
      for (int e = 0; e < 4; ++e) sel[e] = ((t16 & 7) >> 1) == e ? ((t16 & 1) ? 0x3f800000u : 0x00003f80u) : 0u;
      auto sum = [&](auto C) {
        constexpr int c = C.v;
        waitv4<13 - 4 * c>(wa1[2 * c], wa1[2 * c + 1], wa1[8 + 2 * c], wa1[8 + 2 * c + 1]);
        sk = mfma16(asb(sel), asb(wa1[2 * c]), sk); sv = mfma16(asb(sel), asb(wa1[8 + 2 * c]), sv);
        sk = mfma16(asb(sel), asb(wa1[2 * c + 1]), sk); sv = mfma16(asb(sel), asb(wa1[8 + 2 * c + 1]), sv);
        asm volatile("" : "+v"(sk), "+v"(sv));
      };
      auto put = [&](auto M) { putA1(IC<0>{}, M); putA1(IC<1>{}, M); };
      sfor<0, 2>(sum);
      RAWBAR();
      sfor<0, 4>(put);
      BAR();
      __builtin_amdgcn_s_setprio(3);
      sfor<2, 4>(sum);
      sfor<4, 8>(put);
      {
        LAS char* tb = lds + LD_SC + 16384 + HW * 2048;
        *(LAS f32x4*)(tb + l16) = sk; *(LAS f32x4*)(tb + 1024 + l16) = sv;
        LAS float* tr = (LAS float*)(tb + (q & 1) * 256 + (tk >> 3) * 16 + p * 4);
        float a = tr[0], b = tr[16], c_ = tr[32], d = tr[48], e = tr[256], f = tr[272], g_ = tr[288], h = tr[304];
        a += b; asm volatile("" : "+v"(a)); c_ += d; asm volatile("" : "+v"(c_)); e += f; asm volatile("" : "+v"(e)); g_ += h; asm volatile("" : "+v"(g_));
        a += c_; asm volatile("" : "+v"(a)); e += g_;
        putS(l16 + 4 * HW, a); putS(1024 + l16 + 4 * HW, e);
      }
      if (HW == 3) { asm volatile("s_waitcnt vmcnt(0)" : "+v"(h0)::"memory"); if (lane < 32) gst8_sc1(ws, HEL_PEER, u32x2{h0.y, h0.x + 2u}); }
      BAR();
      __builtin_amdgcn_s_setprio(0);
      _Pragma("unroll 4") for (int i = 0; i < 8; ++i) GLDS_Q(HW * 8 + i);
      u32x2 hv;
      ldB1b(wb2 + 20);
      gld8_sc1(hv, ws, HW == 3 ? HEL_OWN : CNT(7));
      asm volatile("s_waitcnt vmcnt(17)" ::: "memory");
      RAWBAR();
      { f32x4 a = z4, b = z4; colsum8(lds + 65536 + HW * 8192 + TRA, 1024, 512, a, b); putS(2048 + l16 + 4 * HW, a[0] + b[0]); }
      asm volatile("s_waitcnt vmcnt(0)" : "+v"(hv)::"memory");
      if (HW == 3) {
        u32 sent = h0.x;
        for (u32 sp_ = 0; sp_ < SPIN_CAP; ++sp_) {
          if (hv.x != sent) { sent = hv.x; if (lane < 32) gst8_sc1(ws, HEL_PEER, u32x2{h0.y, hv.x + 1u}); }
          if (__all(hv.y != h0.y)) break;
          gld8_sc1(hv, ws, HEL_OWN); asm volatile("s_waitcnt vmcnt(0)" : "+v"(hv)::"memory");
        }
      }
      BAR();
    }
    sfor<0, 2>([&](auto L) {
      constexpr int layer = L.v;
      FRESH();
      if constexpr (layer == 1) {
        RAWBAR();
        ldB1b(wa1);
        _Pragma("unroll 4") for (int i = 0; i < 8; ++i) GLDS_A3(32 + HW * 8 + i);
        RAWBAR();
        RAWBAR();
      }
      if (HW == 1) KSIG();
      if (cg != CGS) { _Pragma("unroll 4") for (int i = 0; i < 8; ++i) GLDS_B1(HW * 16 + i); }
      if constexpr (layer == 0) { _Pragma("unroll 4") for (int i = 0; i < 8; ++i) GLDS_A3(32 + HW * 8 + i); }
      RAWBAR();
      RAWBAR();
      _Pragma("unroll 4") for (int i = 0; i < 4; ++i) GLDS_A3(HW * 8 + i);
      RAWBAR();
      _Pragma("unroll 4") for (int i = 4; i < 8; ++i) GLDS_A3(HW * 8 + i);
      RAWBAR();
      if (layer == 0 && lane < 3) touch(lb + LD_DUMP, pc + min(lane * 128 + HW * 32 + loc, CODE_KB * 8 - 1) * 128);
      if constexpr (layer == 0) { ldB2(IC<5>{}); asm volatile("s_waitcnt vmcnt(5)" ::: "memory"); } else DRAIN();
      RAWBAR();
      __builtin_amdgcn_s_sleep(12);
      if (cg != CGS) { _Pragma("unroll 4") for (int i = 8; i < 16; ++i) GLDS_B1(HW * 16 + i); }
      if (HW == 1) T3_B2();
      if constexpr (layer == 0) sfor<6, 8>(ldB2);
      RAWBAR();
      putB1b(layer == 0 ? wb2 + 20 : wa1);
      RAWBAR();
      FRESH();
      if constexpr (layer == 0) { ldB2(IC<8>{}); asm volatile("s_waitcnt vmcnt(4)" ::: "memory"); } else DRAIN();
      {
        u32x4* r = layer == 0 ? wb2 + 20 : wa1;
        LAS char* t0 = opaque(lds + HW * 16384 + TRB0); LAS char* t1 = opaque(lds + HW * 16384 + TRB1);
        sfor<0, 16>([&](auto I) { r[I.v] = __builtin_bit_cast(u32x4, trfrag(((I.v & 1) ? t1 : t0) + 65536 + (I.v >> 1) * 2048, 1024)); });
        sfor<0, 16>([&](auto I) { pin(r[I.v]); });
        if constexpr (layer == 0) {
          const u32 o = ((t16 >> 2) & 1) == (g & 1) ? 0x3f803f80u : 0u; const u32x4 sel = {o, o, o, o};
          f32x4 a = z4, b = z4, c = z4, d = z4, e = z4, f = z4;
          sfor<0, 8>([&](auto K) { a = mfma16(asb(sel), asb(r[2 * K.v]), a); b = mfma16(asb(sel), asb(r[2 * K.v + 1]), b); });
          colsum8(t0, 2048, 1024, c, d); colsum8(t1, 2048, 1024, e, f);
          LAS float* pp = (LAS float*)(lds + LD_SC + 16384 + HW * 256 + lane * 4);
          pp[0] = c[0] + d[0]; pp[256] = e[0] + f[0]; pp[512] = a[0]; pp[768] = b[0];
        }
      }
      BAR();
      sfor<0, 16>([&](auto I) { *(LAS u32x4*)(wl + 65536 + HW * 16384 + I.v * 1024) = (layer == 0 ? wb2 + 20 : wa1)[I.v]; });
      if constexpr (layer == 0) { LAS float* pp = (LAS float*)(lds + LD_SC + 16384 + HW * 1024 + lane * 4); putS(3072 + HW * 256 + lane * 4, (pp[0] + pp[64]) + (pp[128] + pp[192])); }
      BAR();
      auto fill = [&](auto T) { sfor<0, 4>([&](auto I) { glds(lb + b2slot(T.v) + (HW * 4 + I.v) * 1024, B2SRC(T.v, HW) + I.v * 8192, vo1024); }); };
      fill(IC<0>{}); fill(IC<1>{}); fill(IC<2>{}); fill(IC<4>{});
      if constexpr (layer == 0) sfor<9, 14>(ldB2);
      RAWBAR();
      if (HW == 1) PSIG(2);
      fill(IC<3>{});
      if (HW == 0) { __builtin_amdgcn_s_sleep(10); PWAIT(2); }
      DRAIN();
      sfor<0, 36>([&](auto I) { pin(wb2[I.v]); });
      RAWBAR();
      FRESH();
      putB2(IC<5>{}); putB2(IC<6>{}); putB2(IC<7>{});
      BAR();
      if (HW == 1) PSIG(5);
      putB2(IC<8>{});
      if (HW == 0) { __builtin_amdgcn_s_sleep(8); PWAIT(5); }
      BAR();
      putB2(IC<9>{}); putB2(IC<10>{}); putB2(IC<11>{});
      BAR();
      if (layer == 0 && HW == 1) T3_A();
      putB2(IC<12>{}); putB2(IC<13>{});
      BAR();
      if constexpr (layer == 0) { _Pragma("unroll 4") for (int i = 0; i < 8; ++i) GLDS_Q(HW * 8 + i); }
      if constexpr (layer == 0) sfor<0, 8>([&](auto M) { putA1(IC<0>{}, M); });
      RAWBAR();
      if constexpr (layer == 0) {
        sfor<0, 8>([&](auto M) { putA1(IC<1>{}, M); });
        RAWBAR(); DRAIN(); BAR();
      }
    });
  } else {
    u32x4 resid[2];
    u32x4 wph[2];
    u32x4 xf[32];
    f32x16 acc[4];
    float rstd; u32 nmean;
    auto nop = [&](auto) {};
#define LDF(i, n) gld16_sc1<((n) & 3) * 1024>(xf[i], xb + ((n) >> 2) * 4096, l16)
#define FINSTATS(sum, ssq) { red2sum(sum, ssq); const float mean = sum * (1.f / 512.f); rstd = __builtin_amdgcn_rsqf(mx2(ssq * (1.f / 512.f) - mean * mean, 0.f) + 1e-5f); nmean = split2(-mean); }
#define STATS() { _Pragma("unroll") for (int i = 0; i < 4; ++i) pin(st[i]); \
      const f32x4 v = (__builtin_bit_cast(f32x4, st[0]) + __builtin_bit_cast(f32x4, st[1])) + (__builtin_bit_cast(f32x4, st[2]) + __builtin_bit_cast(f32x4, st[3])); \
      float a = v[0], b = v[1], c = v[2], d = v[3]; swap16(a, c); swap16(b, d); float sum = a + c, ssq = b + d; FINSTATS(sum, ssq) }
#define STREAM32(NT, SB_) { constexpr int kb_ = s / NT; if constexpr (s % NT == 0) waitv<(N0 + kb_ < 36 ? N0 + kb_ : 36) - kb_ - 1>(xf[kb_]); \
      if constexpr (s % NT == 1 && N0 + kb_ < 32) LDF(N0 + kb_, N0 + kb_); \
      if constexpr (s % NT == 1 && N0 + kb_ >= 32 && N0 + kb_ < 36) gld16_sc1<(N0 + kb_ - 32) * 1024>(st[(N0 + kb_) & 3], (SB_) + (size_t)(4 * P + w) * 4096, l16); }
#define RES2ACC() { _Pragma("unroll") for (int i = 0; i < 8; ++i) { const u32 r = resid[i >> 2][i & 3]; acc[0][2 * i] = bflo(r); acc[0][2 * i + 1] = bfhi(r); } acc[1] = z16; asm volatile("" : "+v"(acc[0])); }
    for (int layer = 0; layer < 2; ++layer) {
      FRESH();
      acc[0] = z16; acc[1] = z16; acc[2] = z16;
      LAS char* wa1p = opaque(lds + TRA);
      auto ldkv = [&](auto KB, auto T) { return trfrag(wa1p + T.v * 32768 + KB.v * 1024, 512); };
      u32x4 sk, sv; LAS char* kt; LAS char* ka; u32 kvo;
      char* kb_ = ws + WS_KVP + (size_t)((layer * 16 + j) * 4 + w) * KVP_UNIT; asm volatile("" : "+s"(kb_));
      auto kvtail = [&](auto I) {
        if constexpr (I.v == 50) { kt = lds + LD_SC + (32 * w + tk) * 200 + 8 * hh; asm volatile("" : "+v"(kt)); }
        if constexpr (I.v == 52) { ka = lds + LD_SC + (4 * g + q) * 200 + 8 * p + (w >> 1) * 96; asm volatile("" : "+v"(ka)); }
        if constexpr (I.v == 54) { kvo = P * 512u + hh * 256u + t16 * 16u; asm volatile("" : "+v"(kvo)); }
        if constexpr (I.v == 56) { sk = *(LAS u32x4*)(lds + LD_S + l16); sv = *(LAS u32x4*)(lds + LD_S + 1024 + l16); }
        if constexpr (I.v == 61) { acc[0] = mfma(asb(sk), asb(u32x4{nmean, nmean, nmean, nmean}), acc[0]); asm volatile("" : "+v"(acc[0])); }
        if constexpr (I.v == 62) { acc[1] = mfma(asb(sv), asb(u32x4{nmean, nmean, nmean, nmean}), acc[1]); asm volatile("" : "+v"(acc[1])); }
      };
      if (layer == 0) {
        const char* xb = x + (size_t)(128 * P + 32 * w) * 1024;
        u32 xvo[4];
#pragma unroll
        for (int i = 0; i < 4; ++i) xvo[i] = g * 1024u + ((t16 ^ (4 * i + g)) * 16u);
        const u32 stg = lb + w * 8192;
        auto ldx = [&](auto R) { sfor<0, 8>([&](auto I) { glds(stg + R.v * 32768 + I.v * 1024, xb + R.v * 256 + I.v * 4096, xvo[I.v & 3]); }); };
        ldx(IC<0>{}); ldx(IC<1>{}); ldx(IC<2>{}); ldx(IC<3>{});
        u32 wp[8];
        const u16* wsrc = (const u16*)wphi + (2 * j + (tk >> 4)) * 256 + (4 * hh) * 16 + t16;
        auto ldwp = [&] { sfor<0, 8>([&](auto I) { gld2<(I.v >> 2) * 256 + (I.v & 3) * 32>(wp[I.v], wsrc); }); };
        const u32 str = w * 8192 + tk * 256 + (t16 ^ hh) * 16;
        f32x4 sm[2] = {z4, z4}; float sq[4] = {0.f, 0.f, 0.f, 0.f};
        const u32 o1 = ((t16 >> 2) & 1) == (g & 1) ? 0x3f803f80u : 0u; const u32x4 selx = {o1, o1, o1, o1};
        auto stats = [&](auto R) {
          constexpr int b = 8 * R.v;
          sfor<b, b + 8>([&](auto I) {
#pragma unroll
            for (int e = 0; e < 4; ++e) sq[e] = dot2(xf[I.v][e], xf[I.v][e], sq[e]);
          });
          asm volatile("" : "+v"(sq[0]), "+v"(sq[1]), "+v"(sq[2]), "+v"(sq[3])); SB();
          sfor<b, b + 8>([&](auto I) { sm[I.v & 1] = mfma16(asb(selx), asb(xf[I.v]), sm[I.v & 1]); });
          asm volatile("" : "+v"(sm[0]), "+v"(sm[1])); SB();
          if ((cg >> 1) == R.v && (w >> 1) == th) {
            LAS char* rx = lds + LD_SC + cc * 4096 + l16;
            if (cg & 1) { _Pragma("unroll") for (int i = 0; i < 4; ++i) *(LAS u32x4*)(rx + i * 1024) = xf[b + 4 + i]; }
            else { _Pragma("unroll") for (int i = 0; i < 4; ++i) *(LAS u32x4*)(rx + i * 1024) = xf[b + i]; }
          }
        };
        sfor<0, 4>([&](auto R) {
          constexpr int b = 8 * R.v;
          asm volatile("s_waitcnt vmcnt(%0)" ::"i"(24 - b) : "memory");
#pragma unroll
          for (int i = 0; i < 8; ++i) xf[b + i] = *(LAS u32x4*)(lds + ((str ^ (32 * i)) + R.v * 32768));
          SB();
          if constexpr (R.v > 0) stats(IC<R.v - 1>{});
        });
        stats(IC<3>{});
        float sum = 0.5f * (sm[0][0] + sm[1][0]), ssq = (sq[0] + sq[1]) + (sq[2] + sq[3]);
        FINSTATS(sum, ssq)
        auto mkwph = [&] {
          DRAIN();
#pragma unroll
          for (int i = 0; i < 8; ++i) pin1(wp[i]);
          const u32x4 wv = {wp[0] | (wp[1] << 16), wp[2] | (wp[3] << 16), wp[4] | (wp[5] << 16), wp[6] | (wp[7] << 16)};
          const u32x4 zz = {0u, 0u, 0u, 0u};
          wph[0] = tk < 16 ? wv : zz; wph[1] = tk < 16 ? zz : wv;
          pin(wph[0]); pin(wph[1]);
        };
        asm volatile("" : "+v"(rstd), "+v"(nmean));
        RAWBAR();
        ldwp();
        BAR();
#pragma unroll
        for (int i = 0; i < 2; ++i) { LAS char* rp = lds + LD_SC + cc * 4096 + kh * 2048 + i * 1024 + tk * 16 + 8 * hh; const u32x2 lo = *(LAS u32x2*)rp, hi = *(LAS u32x2*)(rp + 512); resid[i] = u32x4{lo.x, lo.y, hi.x, hi.y}; }
        LAS char* wa0 = opaque(lds + TRA + 256 * hh);
        gemm<2, 0, 32, 4, 32>(acc, xf, [&](auto KB, auto T) { return trfrag(wa0 + T.v * 32768 + KB.v * 1024, 256); }, [&](auto I) { if constexpr (I.v == 28) { pin(resid[0]); pin(resid[1]); RAWBAR(); }   if constexpr (I.v == 27) mkwph(); }, kvtail);
      } else {
        u32x4 st[4];
        const char* xb = ws + WS_X2 + (size_t)(4 * P + w) * 32768;
        asm volatile("" : "+s"(xb));
        { constexpr int layer = 0; HANDOFF(4); }
        sfor<0, N0>([&](auto I) { LDF(I.v, I.v); });
        gemm<2, 0, 32, 4, 32>(acc, xf, ldkv, [&](auto I) {
          constexpr int s = I.v;
          if constexpr (s == 2 * (36 - N0)) RAWBAR();
          STREAM32(2, ws + WS_ST2)
          if constexpr (s == 59) DRAIN();
        }, [&](auto I) { if constexpr (I.v == 59) { STATS() asm volatile("" : "+v"(rstd), "+v"(nmean)); } kvtail(I); });
      }
      {
        u32x4 yk[2], yv[2], f[2][2];
        pack16(acc[0], yk);
        f32x16 pp = mfma(asb(wph[0]), asb(yk[0]), z16); pp = mfma(asb(wph[1]), asb(yk[1]), pp);
        pack16(acc[1] * rstd, yv);
#pragma unroll
        for (int hd = 0; hd < 2; ++hd) { *(LAS u32x2*)(kt + hd * 96 + 64) = u32x2{yv[hd].x, yv[hd].y}; *(LAS u32x2*)(kt + hd * 96 + 80) = u32x2{yv[hd].z, yv[hd].w}; }
        softmax2(pp, rstd * 1.4426950408889634f, f, [](float*, float*) {}, [] {});
#pragma unroll
        for (int hd = 0; hd < 2; ++hd) {
          *(LAS u32x2*)(kt + hd * 96) = u32x2{f[hd][0].x, f[hd][0].y}; *(LAS u32x2*)(kt + hd * 96 + 16) = u32x2{f[hd][0].z, f[hd][0].w};
          *(LAS u32x2*)(kt + hd * 96 + 32) = u32x2{f[hd][1].x, f[hd][1].y}; *(LAS u32x2*)(kt + hd * 96 + 48) = u32x2{f[hd][1].z, f[hd][1].w};
        }
      }
      BAR();
      {
        f32x4 akv = {0.f, 0.f, 0.f, 0.f}, az = {0.f, 0.f, 0.f, 0.f};
        bf16x8 A[4], B[4];
#pragma unroll
        for (int ks = 0; ks < 4; ++ks) { A[ks] = trfrag(ka + ks * 6400 + (w & 1) * 32, 3200); B[ks] = trfrag(ka + ks * 6400 + 64, 3200); }
        SB();
#pragma unroll
        for (int ks = 0; ks < 4; ++ks) { az = mfma16(A[ks], asb(ones4), az); akv = mfma16(A[ks], B[ks], akv); }
        const u32x2 kp = pk4(akv);
        auto r0 = __builtin_amdgcn_permlane16_swap(kp.x, kp.x, false, false), r1 = __builtin_amdgcn_permlane16_swap(kp.y, kp.y, false, false);
        if (!(g & 1)) gst16_sc1<0>(kb_, kvo, u32x4{r0[0], r1[0], r0[1], r1[1]});
        asm volatile("s_nop 7" : "+v"(az));
        if (t16 == 0) gst16_sc1<0>(kb_, 8192u + (kvo >> 9) * 64u + g * 16u, __builtin_bit_cast(u32x4, az));
      }
      FRESH();
      {
        u32x4 phq[2][2], yq[2], sq_, sel[2];
        u32 kc, zz[4], gvo;
        LAS char* gi; LAS char* gr; char* ob;
        LAS char* wq = opaque(lds + 65536 + TRA + (layer == 0 ? 256 * hh : 0)); LAS char* wq2 = opaque(wq + (layer == 0 ? 256 : 512));
        gemm<1, 0, 32, 4, 32>(acc + 2, xf, [&](auto KB, auto) { return trfrag2(wq + KB.v * 1024, wq2 + KB.v * 1024); }, [&](auto I) {
          if constexpr (I.v == KSTEP) { DRAIN(); RAWBAR();   }
        }, [&](auto I) {
          if constexpr (I.v == 2) { gi = lds + LD_SC + 16384 + w * 680 + 8 * g; asm volatile("" : "+v"(gi)); }
          if constexpr (I.v == 4) { gr = lds + LD_SC + 16384 + (tk < 16 ? tk : 16) * 40 + 8 * hh; asm volatile("" : "+v"(gr)); }
          if constexpr (I.v == 6) { ob = ws + WS_O + (size_t)layer * (2u << 20) + ((size_t)(4 * P + w) * 32 + 2 * j) * 1024; gvo = g * 512u + t16 * 16u; asm volatile("" : "+s"(ob), "+v"(gvo)); }
          if constexpr (I.v == 24) sq_ = *(LAS u32x4*)(lds + LD_S + 2048 + l16);
          if constexpr (I.v == 30) { acc[2] = mfma(asb(sq_), asb(u32x4{nmean, nmean, nmean, nmean}), acc[2]); asm volatile("" : "+v"(acc[2])); }
        });
        sel[0] = *(LAS u32x4*)(lds + LD_ID + t16 * 32); sel[1] = *(LAS u32x4*)(lds + LD_ID + t16 * 32 + 16);
        pack16(acc[2], yq);
        f32x16 pp = mfma(asb(wph[0]), asb(yq[0]), z16); pp = mfma(asb(wph[1]), asb(yq[1]), pp);
        softmax2(pp, rstd * 1.4426950408889634f, phq, [&](float* mx, float* mn) {
          asm volatile("" : "+v"(mx[0]), "+v"(mx[1]), "+v"(mn[0]), "+v"(mn[1])); SB();
          gld4_sc1(kc, ws, w == 0 ? CNT(6) : CNT(7));
          SB();
        }, [&] {
          pin(phq[0][0]); pin(phq[0][1]); SB();
          asm volatile("s_waitcnt vmcnt(0)" : "+v"(kc)::"memory");
          if (w == 0 && __builtin_amdgcn_readfirstlane(kc) < 16u * (layer + 1)) PWAIT(6);
          RAWBAR();
          sfor<0, 4>([&](auto I) { gld4o_sc1<I.v * 256>(zz[I.v], kb_ + 8192, lane * 4u); });
          sfor<0, 8>([&](auto I) { gld16_sc1<((I.v >> 1) & 1) * 2048 + (I.v & 1) * 256>(xf[I.v], kb_ + (I.v >> 2) * 4096, gvo); });
          RAWBAR();
          SB();
        });
        pin(phq[0][0]); pin(phq[0][1]); pin(phq[1][0]); pin(phq[1][1]);
        asm volatile("s_waitcnt vmcnt(8)" : "+v"(zz[0]), "+v"(zz[1]), "+v"(zz[2]), "+v"(zz[3]));
        f32x4 sz = __builtin_amdgcn_mfma_f32_16x16x4f32((__uint_as_float(zz[0]) + __uint_as_float(zz[1])) + (__uint_as_float(zz[2]) + __uint_as_float(zz[3])), 1.f, z4, 0, 0, 0);
        asm volatile("" : "+v"(sz)); SB();
        f32x4 s0 = z4, s1 = z4;
        sfor<0, 4>([&](auto I) {
          waitv<7 - 2 * I.v>(xf[2 * I.v]); s0 = mfma16(asb(sel[0]), asb(xf[2 * I.v]), s0);
          waitv<6 - 2 * I.v>(xf[2 * I.v + 1]); s1 = mfma16(asb(sel[1]), asb(xf[2 * I.v + 1]), s1);
          SB();
        });
        *(LAS u32x2*)(gi + 640) = pk4(sz);
        s0 += s1;
        *(LAS u32x2*)(gi + t16 * 40) = pk4(s0);
        BAR();
        f32x16 o[2]; u32x4 kf[4];
#pragma unroll
        for (int i = 0; i < 4; ++i) { const u32x2 a = *(LAS u32x2*)(gr + i * 680), b = *(LAS u32x2*)(gr + i * 680 + 16); kf[i] = u32x4{a.x, a.y, b.x, b.y}; }
        SB();
        o[0] = mfma(asb(kf[0]), asb(phq[0][0]), z16); o[1] = mfma(asb(kf[2]), asb(phq[1][0]), z16);
        o[0] = mfma(asb(kf[1]), asb(phq[0][1]), o[0]); o[1] = mfma(asb(kf[3]), asb(phq[1][1]), o[1]);
        asm volatile("" : "+v"(o[0]), "+v"(o[1])); SB();
        sfor<0, 2>([&](auto HD) {
          constexpr int hd = HD.v;
          const float r = __builtin_amdgcn_rcpf(o[hd][8] + 1e-6f);
          gst16_sc1<hd * 1024>(ob, l16, u32x4{pk2v(pr(o[hd], 0) * r), pk2v(pr(o[hd], 1) * r), pk2v(pr(o[hd], 2) * r), pk2v(pr(o[hd], 3) * r)});
        });
        RES2ACC()
      }
#define TILE_REDUCE(EXOFF) { LAS char* ex = lds + (EXOFF) + l16; \
        _Pragma("unroll") for (int i = 0; i < 4; ++i) *(LAS f32x4*)(ex + w * 4096 + i * 1024) = f32x4{acc[1][4 * i], acc[1][4 * i + 1], acc[1][4 * i + 2], acc[1][4 * i + 3]}; \
        BAR(); \
        f32x4 rd[4]; _Pragma("unroll") for (int i = 0; i < 4; ++i) rd[i] = *(LAS f32x4*)(ex + (w ^ 2) * 4096 + i * 1024); SB(); \
        _Pragma("unroll") for (int i = 0; i < 4; ++i) { const f32x4 v = rd[i] + f32x4{acc[0][4 * i], acc[0][4 * i + 1], acc[0][4 * i + 2], acc[0][4 * i + 3]}; \
          resid[i >> 1][2 * (i & 1)] = pk2(v[0], v[1]); resid[i >> 1][2 * (i & 1) + 1] = pk2(v[2], v[3]); } }
#define TILE_PUBLISH(xo, so) { gst16_sc1<0>(xo, l16, resid[0]); gst16_sc1<1024>(xo, l16, resid[1]); \
        float sum = 0.f, ssq = 0.f, sum2 = 0.f, ssq2 = 0.f; \
        _Pragma("unroll") for (int e = 0; e < 4; ++e) { sum = dot2(resid[0][e], 0x3f803f80u, sum); ssq = dot2(resid[0][e], resid[0][e], ssq); sum2 = dot2(resid[1][e], 0x3f803f80u, sum2); ssq2 = dot2(resid[1][e], resid[1][e], ssq2); } \
        sum += sum2; ssq += ssq2; red2sum(sum, ssq); sum2 = sum; ssq2 = ssq; swap16(sum, sum2); swap16(ssq, ssq2); \
        if (lane < 16) gst16_sc1<0>(so, l16, u32x4{__float_as_uint(sum), __float_as_uint(ssq), __float_as_uint(sum2), __float_as_uint(ssq2)}); }
      FRESH();
      {
        const size_t ch = (size_t)(4 * P + 2 * th + cc);
        const char* xb = ws + WS_O + (size_t)layer * (2u << 20) + (ch * 32 + 16 * kh) * 1024;
        char* xo = ws + WS_X1 + (size_t)layer * (2u << 20) + (ch * 32 + 4 * cg + 2 * kh) * 1024;
        char* so = ws + WS_ST1 + (size_t)layer * (256u << 10) + (ch * 16 + 2 * cg + kh) * 256;
        LAS char* wa[2] = {opaque(lds + LD_WA3 + kh * 32768 + (TRB0 ^ (64 * kh))), opaque(lds + LD_WA3 + kh * 32768 + (TRB1 ^ (64 * kh)))};
        asm volatile("" : "+s"(xb), "+s"(xo), "+s"(so));
        HANDOFF(0);
        sfor<0, N0>([&](auto I) { LDF(I.v, I.v); });
        gemm<2, 0, 16, 4, 32>(acc, xf, [&](auto KB, auto T) { return trfrag(wa[T.v] + KB.v * 2048, 1024); }, [&](auto I) {
          constexpr int s = I.v;
          if constexpr (s % 2 == 0) waitv<(N0 + s / 2 < 16 ? N0 + s / 2 : 16) - s / 2 - 1>(xf[s / 2]);
          if constexpr (s % 2 == 1 && N0 + s / 2 < 16) LDF(N0 + s / 2, N0 + s / 2);
        }, nop);
        TILE_REDUCE(LD_SC)
        TILE_PUBLISH(xo, so)
      }
      FRESH();
      const char* hx = ws + WS_H + (size_t)layer * (7u << 20) + ((size_t)(4 * P + 2 * th + cc) * 112 + 56 * kh) * 1024;
#define LDH(i) gld16_sc1<((2 * ((i) / 14) + (i) % 2) & 3) * 1024>(xf[(i) % 32], hx + (2 * (((i) % 14) / 2) + (i) / 28) * 4096, l16)
      {
        const char* xb = ws + WS_X1 + (size_t)layer * (2u << 20) + (size_t)(4 * P + w) * 32768;
        char* hb = ws + WS_H + (size_t)layer * (7u << 20) + ((size_t)(4 * P + w) * 112 + 8 * JB) * 1024;
        LAS char* wa[3] = {opaque(lds + TRB0), opaque(lds + TRB1), opaque(lds + 65536 + l16)};
        asm volatile("" : "+s"(xb), "+s"(hb), "+s"(hx));
        HANDOFF(1);
        if (cg != CGS) {
          u32x4 st[4], wg[6]; u32 sb[4]; float c1, sigma;
          auto fix = [&](auto T) { acc[T.v] = mfma(asb(u32x4{sb[T.v], 0u, 0u, 0u}), asb(u32x4{nmean, 0u, 0u, 0u}), acc[T.v]); asm volatile("" : "+v"(acc[T.v])); };
          sfor<0, N0>([&](auto I) { LDF(I.v, I.v); });
          acc[0] = z16; acc[1] = z16; acc[2] = z16; acc[3] = z16;
          gemm<2, 0, 32, 4, 32>(acc, xf, [&](auto KB, auto T) { return trfrag(wa[T.v] + KB.v * 2048, 1024); }, [&](auto I) {
            constexpr int s = I.v;
            if constexpr (s == 2 * (36 - N0)) RAWBAR();
            STREAM32(2, ws + WS_ST1 + (size_t)layer * (256u << 10))
            if constexpr (s == 59) DRAIN();
          }, [&](auto I) {
            if constexpr (I.v == 54) { _Pragma("unroll") for (int i = 0; i < 4; ++i) sb[i] = *(LAS u32*)(lds + LD_S + 3072 + i * 256 + lane * 4); }
            if constexpr (I.v == 59) { STATS() asm volatile("" : "+v"(rstd), "+v"(nmean)); }
            if constexpr (I.v >= 60) { wg[(I.v - 60) ^ 1] = *(LAS u32x4*)(wa[2] + g1off((I.v - 60) ^ 1)); SB(); }
            if constexpr (I.v == 61) fix(IC<0>{});
            if constexpr (I.v == 62) fix(IC<1>{});
            if constexpr (I.v == 63) { c1 = rstd * -1.4426950408889634f; sigma = __builtin_amdgcn_rcpf(rstd); asm volatile("" : "+v"(c1), "+v"(sigma)); }
          });
          fix(IC<2>{});
          float tt[8]; u32x4 hp;
          auto uop = [&](auto M) {
            constexpr int st_ = M.v % 6, sl = M.v / 6, e = sl < 37 ? sl - st_ : sl - 5 - st_, t = e / 16, r = e % 16;
            if constexpr (sl < 37 ? (e >= 0 && e < 32) : (e >= 32 && e < 64)) {
              if constexpr (st_ == 0) tt[e & 7] = acc[t][r] * c1;
              if constexpr (st_ == 1) tt[e & 7] = __builtin_amdgcn_exp2f(tt[e & 7]);
              if constexpr (st_ == 2) tt[e & 7] = __builtin_fmaf(tt[e & 7], sigma, sigma);
              if constexpr (st_ == 3) tt[e & 7] = __builtin_amdgcn_rcpf(tt[e & 7]);
              if constexpr (st_ == 4) tt[e & 7] = acc[t][r] * tt[e & 7];
              if constexpr (st_ < 5) asm volatile("" : "+v"(tt[e & 7]));
              if constexpr (st_ == 5 && (e & 1)) {
                u32 pkd = pk2(tt[(e - 1) & 7], tt[e & 7]); pin1(pkd); hp[(e >> 1) & 3] = pkd;
                if constexpr ((e & 7) == 7) gst16_sc1<((e >> 3) & 3) * 1024>(hb + (e >> 5) * 4096, l16, hp);
              }
            }
          };
          constexpr int UB = 37 * 6, UC = 53 * 6;
          gemm1(acc + 2, xf, wg, wa[2], [&](auto I) {
            if constexpr (I.v == 32 + B5C) { DRAIN(); RAWBAR(); }
          }, [&](auto I) {
            constexpr int i = I.v;
            if constexpr (i < 32) { constexpr int u0 = i < 2 ? 12 * i : i < 29 ? 12 + 6 * i : 186 + 12 * (i - 29), u1 = i + 1 < 2 ? 12 : i + 1 < 29 ? 18 + 6 * i : 186 + 12 * (i - 28); sfor<u0, u1>(uop); }
            if constexpr (i == 32) fix(IC<3>{});
            if constexpr (i >= 33) sfor<UB + (i - 33) * (UC - UB) / 31, UB + (i - 32) * (UC - UB) / 31>(uop);
          });
          sfor<UC, 75 * 6>(uop);
        } else { RAWBAR(); RAWBAR(); }
      }
      FRESH();
      {
        const size_t ch = (size_t)(4 * P + 2 * th + cc);
        const int t0 = kh * 8192 + (TRB0 ^ (64 * kh)), t1 = t0 ^ 64;
        LAS char* wa[6] = {opaque(lds + t0), opaque(lds + t1), opaque(lds + 65536 + t0), opaque(lds + 65536 + t1), opaque(lds + 131072 + t0), opaque(lds + 131072 + t1)};
        char* xo = layer == 0 ? ws + WS_X2 + (ch * 32 + 4 * cg + 2 * kh) * 1024 : out + (size_t)(128 * P + 64 * th + 32 * cc) * 1024 + 128 * cg + 64 * kh;
        char* so = ws + WS_ST2 + (ch * 16 + 2 * cg + kh) * 256;
        asm volatile("" : "+s"(xo), "+s"(so));
        RES2ACC()
        RAWBAR();
        sfor<0, NB>([&](auto I) { LDH(I.v); });
        gemm<2, 0, 56, 4, 32>(acc, xf, [&](auto KB, auto T) { constexpr int so = b2slot(KB.v >> 2); return trfrag(wa[(so >> 16) * 2 + T.v] + (so & 65535) + (KB.v & 3) * 2048, 1024); }, [&](auto I) {
          constexpr int s = I.v, kb = s / 2;
          if constexpr (s == 0) DRAIN();
          if constexpr (s == 0 || s == X2S || s == 68 || s == 92) RAWBAR();
          constexpr int lim = s < X2S ? 28 : 56, iss = NB + kb < lim ? NB + kb : lim;
          if constexpr (s == X2S) sfor<28, iss>([&](auto J) { LDH(J.v); });
          if constexpr (s % 2 == 0) waitv<iss - kb - 1>(xf[kb % 32]);
          if constexpr (s % 2 == 1 && NB + kb < lim) LDH(NB + kb);
        }, nop);
        TILE_REDUCE(98304)
        if (layer == 0) {
          TILE_PUBLISH(xo, so)
        } else {
          sfor<0, 2>([&](auto S) {
            auto r0 = __builtin_amdgcn_permlane32_swap(resid[S.v].x, resid[S.v].z, false, false);
            auto r1 = __builtin_amdgcn_permlane32_swap(resid[S.v].y, resid[S.v].w, false, false);
            gst16<32 * S.v>(xo, tk * 1024u + 16u * hh, u32x4{r0[0], r1[0], r0[1], r1[1]});
          });
        }
      }
    }
  }
}

extern "C" void kernel_launch(void* const* d_in, const int* in_sizes, int n_in, void* d_out, int out_size, void* d_ws, size_t ws_size, hipStream_t stream) {
  mega<<<256, 512, 0, stream>>>((const char*)d_in[0], (const char*)d_in[1], (const char*)d_in[2], (const char*)d_in[3], (const char*)d_in[5], (const char*)d_in[6], (char*)d_out, (char*)d_ws);
}
```
